```python
import torch

def _softmax(x, axis=-1):
    return torch.softmax(x, dim=axis)
def _gelu(x):
    return torch.nn.functional.gelu(x, approximate="tanh")
def _silu(x):
    return torch.nn.functional.silu(x)
def _sigmoid(x):
    return torch.sigmoid(x)
def _layernorm(x, axis=-1, eps=1e-5):
    m = x.mean(dim=axis, keepdim=True)
    v = x.var(dim=axis, keepdim=True, unbiased=False)
    return (x - m) / torch.sqrt(v + eps)
def _scatter_add(tgt, idx, src, axis=0):
    return tgt.clone().index_add_(axis, idx, src)
def _segment_sum(src, seg, n):
    out = torch.zeros((n,) + src.shape[1:], dtype=src.dtype)
    return out.index_add_(0, seg, src)
def _pad(x, pw):
    flat = tuple(v for pair in reversed(pw) for v in pair)
    return torch.nn.functional.pad(x, flat)

kernel_name = 'concat_heads__ssm_conv_s5__linear_kernel_hedgehog__prompt'

def setup_inputs():

    _w = (lambda a: (lambda t: t.to(torch.bfloat16) if t.is_floating_point() else t)(torch.as_tensor(a)))
    def _A_setup():
        B, S, D = 1, 2048, 512
        P = D
        Lambda_re = -0.5 * (lambda _x: torch.exp(torch.as_tensor(_x)))((lambda *s: torch.randn(*s, dtype=torch.bfloat16) * 0.8)(P))
        Lambda_im = (lambda *s: torch.randn(*s, dtype=torch.bfloat16) * 0.8)(P) * 1.0
        B_re = (lambda *s: torch.randn(*s, dtype=torch.bfloat16) * 0.8)(P, D) * (D ** -0.5)
        B_im = (lambda *s: torch.randn(*s, dtype=torch.bfloat16) * 0.8)(P, D) * (D ** -0.5)
        C_re = (lambda *s: torch.randn(*s, dtype=torch.bfloat16) * 0.8)(D, P) * (P ** -0.5)
        C_im = (lambda *s: torch.randn(*s, dtype=torch.bfloat16) * 0.8)(D, P) * (P ** -0.5)
        log_dt = (lambda *s: torch.randn(*s, dtype=torch.bfloat16) * 0.8)(P) * 0.3 - 2.0
        Dskip = (lambda *s: torch.randn(*s, dtype=torch.bfloat16) * 0.8)(D)
        return {
            "x": _w((lambda *s: torch.randn(*s, dtype=torch.bfloat16) * 0.05)(B, S, D)),
            "Lambda_re": _w(Lambda_re),
            "Lambda_im": _w(Lambda_im),
            "B_re": _w(B_re),
            "B_im": _w(B_im),
            "C_re": _w(C_re),
            "C_im": _w(C_im),
            "log_dt": _w(log_dt),
            "Dskip": _w(Dskip),
        }
    _A_in = _A_setup()

    def _B_setup():
        B, S, D, H = 1, 2048, 512, 32
        Dh = D // H
        return {
            "x": _w((lambda *s: torch.randn(*s, dtype=torch.bfloat16) * 0.4)(B, S, D)),
            "wqkv": _w((lambda *s: torch.randn(*s, dtype=torch.bfloat16) * 0.4)(D, 3 * D)),
            "w_phi": _w((lambda *s: torch.randn(*s, dtype=torch.bfloat16) * 0.4)(H, Dh, Dh) * (Dh ** -0.5)),
            "wo": _w((lambda *s: torch.randn(*s, dtype=torch.bfloat16) * 0.05)(D, D)),
        }
    _B_in = _B_setup()

    return {
        "A_x": _A_in["x"],
        "A_Lambda_re": _A_in["Lambda_re"],
        "A_Lambda_im": _A_in["Lambda_im"],
        "A_B_re": _A_in["B_re"],
        "A_B_im": _A_in["B_im"],
        "A_C_re": _A_in["C_re"],
        "A_C_im": _A_in["C_im"],
        "A_log_dt": _A_in["log_dt"],
        "A_Dskip": _A_in["Dskip"],
        "B_x": _B_in["x"],
        "B_wqkv": _B_in["wqkv"],
        "B_w_phi": _B_in["w_phi"],
        "B_wo": _B_in["wo"],
    }

def reference(A_x, A_Lambda_re, A_Lambda_im, A_B_re, A_B_im, A_C_re, A_C_im, A_log_dt, A_Dskip, B_x, B_wqkv, B_w_phi, B_wo):
    def _A_ref(x, Lambda_re, Lambda_im, B_re, B_im, C_re, C_im, log_dt, Dskip):
        Bsz, L, H = x.shape
        Lam = Lambda_re + 1j * Lambda_im
        Bmat = B_re + 1j * B_im
        Cmat = C_re + 1j * C_im
        dt = torch.exp(log_dt)
        Lam_bar = torch.exp(dt * Lam)
        B_bar = ((Lam_bar - 1.0) / Lam)[:, None] * Bmat
        Bu = torch.einsum("bld,pd->blp", x + 0j, B_bar)
        h = torch.zeros_like(Bu[:, 0, :])
        hs = []
        for t in range(L):
            h = Lam_bar[None, :] * h + Bu[:, t, :]
            hs.append(h)
        hseq = torch.stack(hs, 1)
        y = 2.0 * torch.einsum("blp,dp->bld", hseq, Cmat).real + x * Dskip[None, None, :]
        return y

    def _B_ref(x, wqkv, w_phi, wo):
        B, S, D = x.shape
        H = 32; Dh = D // H
        qkv = (lambda x, *d: x.permute(*d))((x @ wqkv).reshape(B, S, 3, H, Dh), 2, 0, 3, 1, 4)
        q, k, v = qkv[0], qkv[1], qkv[2]
        pq = torch.einsum("bhsd,hdf->bhsf", q, w_phi)
        pk = torch.einsum("bhsd,hdf->bhsf", k, w_phi)
        phi_q = torch.concatenate([_softmax(pq, axis=-1), _softmax(-pq, axis=-1)], axis=-1)
        phi_k = torch.concatenate([_softmax(pk, axis=-1), _softmax(-pk, axis=-1)], axis=-1)
        kv = torch.einsum("bhsf,bhsd->bhfd", phi_k, v)
        z  = torch.einsum("bhsf->bhf", phi_k)
        num = torch.einsum("bhqf,bhfd->bhqd", phi_q, kv)
        den = torch.einsum("bhqf,bhf->bhq", phi_q, z)[..., None] + 1e-6
        o = num / den
        return (lambda x, *d: x.permute(*d))(o, 0, 2, 1, 3).reshape(B, S, D) @ wo

    a = _A_ref(A_x, A_Lambda_re, A_Lambda_im, A_B_re, A_B_im, A_C_re, A_C_im, A_log_dt, A_Dskip)
    b = _B_ref(B_x, B_wqkv, B_w_phi, B_wo)
    return torch.concatenate([a, b], axis=-1)
```

```cpp
#include <hip/hip_runtime.h>
#include <hip/hip_bf16.h>
typedef unsigned short u16;
__device__ __forceinline__ float bf(u16 v){ return __uint_as_float(((unsigned)v)<<16); }
__device__ __forceinline__ float rbf(float f){ unsigned u=__float_as_uint(f); u=(u+0x7FFF+((u>>16)&1))&0xFFFF0000u; return __uint_as_float(u);}
#define S 2048
#define D 512
__global__ void k_u(const u16* x,const u16* Br,const u16* Bi,float* U){
  int t=blockIdx.x; int p=threadIdx.x;
  __shared__ float xs[D]; xs[p]=bf(x[t*D+p]); __syncthreads();
  float ar=0,ai=0; for(int d=0;d<D;d++){ar+=xs[d]*bf(Br[p*D+d]); ai+=xs[d]*bf(Bi[p*D+d]);}
  U[(t*D+p)*2]=ar; U[(t*D+p)*2+1]=ai;
}
__global__ void k_scan(const u16* Lr,const u16* Li,const u16* ldt,float* U){
  int p=blockIdx.x*64+threadIdx.x;
  float lr=bf(Lr[p]), li=bf(Li[p]); float dt=rbf(expf(bf(ldt[p])));
  float m=expf(dt*lr); float br=m*cosf(dt*li), bi=m*sinf(dt*li);
  float nr=br-1.f, ni=bi; float den=lr*lr+li*li; float cr=(nr*lr+ni*li)/den, ci=(ni*lr-nr*li)/den;
  float hr=0,hi=0;
  for(int t=0;t<S;t++){ float ur=U[(t*D+p)*2], ui=U[(t*D+p)*2+1];
    float vr=cr*ur-ci*ui, vi=cr*ui+ci*ur; float a=br*hr-bi*hi+vr, b=br*hi+bi*hr+vi; hr=a;hi=b;
    U[(t*D+p)*2]=rbf(hr); U[(t*D+p)*2+1]=rbf(hi);}
}
__global__ void k_y(const u16* x,const u16* Cr,const u16* Ci,const u16* Dk,const float* U,float* out){
  int t=blockIdx.x; int d=threadIdx.x;
  __shared__ float hs[2*D]; hs[d]=U[t*2*D+d]; hs[d+D]=U[t*2*D+D+d]; __syncthreads();
  float a=0; for(int p=0;p<D;p++) a+=hs[2*p]*bf(Cr[d*D+p])-hs[2*p+1]*bf(Ci[d*D+p]);
  out[t*1024+d]=2.f*a+rbf(bf(x[t*D+d])*bf(Dk[d]));
}
__global__ void k_qkv(const u16* x,const u16* W,const u16* wphi,float* phiq,float* phik,float* v){
  int t=blockIdx.x; int n=threadIdx.x;
  __shared__ float xs[D]; __shared__ float q[D],k[D],pq[D],pk[D]; xs[n]=bf(x[t*D+n]); __syncthreads();
  float a=0,b=0,c=0; for(int d=0;d<D;d++){a+=xs[d]*bf(W[d*1536+n]); b+=xs[d]*bf(W[d*1536+512+n]); c+=xs[d]*bf(W[d*1536+1024+n]);}
  q[n]=rbf(a);k[n]=rbf(b); v[t*D+n]=rbf(c); __syncthreads();
  int h=n/16,f=n%16; a=0;b=0; for(int d=0;d<16;d++){float w=bf(wphi[h*256+d*16+f]); a+=q[h*16+d]*w; b+=k[h*16+d]*w;}
  pq[n]=a;pk[n]=b; __syncthreads();
  for(int s=0;s<2;s++){ float sg=s?-1.f:1.f; float m1=-1e30f,m2=-1e30f; for(int j=0;j<16;j++){m1=fmaxf(m1,sg*pq[h*16+j]);m2=fmaxf(m2,sg*pk[h*16+j]);}
    float s1=0,s2=0; for(int j=0;j<16;j++){s1+=expf(sg*pq[h*16+j]-m1); s2+=expf(sg*pk[h*16+j]-m2);}
    phiq[t*1024+h*32+s*16+f]=rbf(expf(sg*a-m1)/s1); phik[t*1024+h*32+s*16+f]=rbf(expf(sg*b-m2)/s2);}
}
__global__ void k_kv(const float* phik,const float* v,float* kv){
  int h=blockIdx.x; int i=threadIdx.x;
  int f=i/17,d=i%17; float a=0; for(int t=0;t<S;t++) a+=phik[t*1024+h*32+f]*(d<16?v[t*D+h*16+d]:1.f);
  kv[h*544+i]=rbf(a);
}
__global__ void k_o(const float* phiq,const float* kv,const u16* wo,float* out){
  int t=blockIdx.x; int n=threadIdx.x; __shared__ float o[D];
  int h=n/16,d=n%16; float a=0,b=0; for(int f=0;f<32;f++){float p=phiq[t*1024+h*32+f]; a+=p*kv[h*544+f*17+d]; b+=p*kv[h*544+f*17+16];}
  o[n]=rbf(a/(b+1e-6f)); __syncthreads();
  float c=0; for(int j=0;j<D;j++) c+=o[j]*bf(wo[j*D+n]);
  out[t*1024+512+n]=c;
}
extern "C" void kernel_launch(void* const* d_in, const int* in_sizes, int n_in, void* d_out, int out_size, void* d_ws, size_t ws_size, hipStream_t stream){
  const u16* const* in=(const u16* const*)d_in; float* out=(float*)d_out; float* ws=(float*)d_ws;
  float* U=ws; float* phiq=ws+2*S*D; float* phik=phiq+2*S*D; float* v=phik+2*S*D; float* kv=v+S*D;
  k_u<<<S,512,0,stream>>>(in[0],in[3],in[4],U);
  k_scan<<<8,64,0,stream>>>(in[1],in[2],in[7],U);
  k_y<<<S,512,0,stream>>>(in[0],in[5],in[6],in[8],U,out);
  k_qkv<<<S,512,0,stream>>>(in[9],in[10],in[11],phiq,phik,v);
  k_kv<<<32,544,0,stream>>>(phik,v,kv);
  k_o<<<S,512,0,stream>>>(phiq,kv,in[12],out);
}
```

```cpp
#include <hip/hip_runtime.h>
#include <hip/hip_bf16.h>

#pragma clang diagnostic ignored "-Winline-asm"
typedef unsigned short u16;
typedef unsigned int u32;
typedef unsigned long long u64;
typedef __bf16 bf16x8 __attribute__((ext_vector_type(8)));
typedef __bf16 bf16x2 __attribute__((ext_vector_type(2)));
typedef short s16x4 __attribute__((ext_vector_type(4)));
typedef float f32x2 __attribute__((ext_vector_type(2)));
typedef float f32x4 __attribute__((ext_vector_type(4)));
typedef float f32x16 __attribute__((ext_vector_type(16)));
typedef u32 u32x2 __attribute__((ext_vector_type(2)));
typedef u32 u32x4 __attribute__((ext_vector_type(4)));
#define LDS3 __attribute__((address_space(3)))
#define DI __device__ __forceinline__

#define WS_EPA   0u
#define WS_EPB   256u
#define WS_FLAG  4096u
#define WS_GA    65536u
#define WS_G2    (WS_GA + 131072u)
#define WS_G1    524288u
#define WS_HA    4194304u
#define WS_OB    8388608u
#define WS_BYTES 16777216u
#define WS_DIAG  16777216u
#define AUX_SC1 16
#define SPIN_MAX (1 << 18)

struct Args { const u16* in[13]; float* out; unsigned char* ws; };

#define STAMP(i)

DI float bf2f(u32 v) { return __uint_as_float(v << 16); }
DI u32 cvtpk(float a, float b) { f32x2 t = {a, b}; return __builtin_bit_cast(u32, __builtin_convertvector(t, bf16x2)); }
DI u32x4 srd(const void* p, u32 bytes) { u64 a = (u64)p; u32x4 r = {(u32)a, (u32)(a >> 32) & 0xffffu, bytes, 0x00020000u}; return r; }
DI void glds(u32 ldsdst, u32 voff, u32x4 rs, u32 soff) {
  asm volatile("s_mov_b32 m0, %0\n\ts_nop 0\n\tbuffer_load_dwordx4 %1, %2, %3 offen lds" :: "s"(ldsdst), "v"(voff), "s"(rs), "s"(soff) : "m0", "memory");
}
#define WAITV(n) asm volatile("s_waitcnt vmcnt(%0)" :: "n"(n) : "memory")
DI void bar_l() { asm volatile("s_waitcnt lgkmcnt(0)\n\ts_barrier" ::: "memory"); }
DI void bar() { asm volatile("s_barrier" ::: "memory"); }
DI void fence() { asm volatile("" ::: "memory"); }
DI void sb() { __builtin_amdgcn_sched_barrier(0); }
DI bf16x8 rd128(u32 a) { return *(const LDS3 bf16x8*)a; }
DI bf16x8 rdtr(u32 a0, u32 a1) {
  s16x4 x = __builtin_amdgcn_ds_read_tr16_b64_v4i16((LDS3 s16x4*)a0);
  s16x4 y = __builtin_amdgcn_ds_read_tr16_b64_v4i16((LDS3 s16x4*)a1);
  return __builtin_bit_cast(bf16x8, __builtin_shufflevector(x, y, 0, 1, 2, 3, 4, 5, 6, 7));
}
DI f32x16 mfma(bf16x8 a, bf16x8 b, f32x16 c) { return __builtin_amdgcn_mfma_f32_32x32x16_bf16(a, b, c, 0, 0, 0); }
DI f32x2 fma2(f32x2 a, f32x2 b, f32x2 c) { return __builtin_elementwise_fma(a, b, c); }
DI f32x2 bc(float a) { f32x2 r = {a, a}; return r; }
struct cpx { float r, i; };
DI cpx cmul(cpx a, cpx b) { cpx c; c.r = a.r * b.r - a.i * b.i; c.i = a.r * b.i + a.i * b.r; return c; }
DI cpx cfma(cpx a, cpx b, cpx c) { cpx d; d.r = fmaf(a.r, b.r, fmaf(-a.i, b.i, c.r)); d.i = fmaf(a.r, b.i, fmaf(a.i, b.r, c.i)); return d; }
DI cpx csq(cpx a) { return cmul(a, a); }
DI u32x4 gran2(float a, float b, u32 thi, u32 tlo) {
  u32x4 g = {__builtin_amdgcn_alignbit(tlo, __float_as_uint(a), 8), thi, __builtin_amdgcn_alignbit(tlo, __float_as_uint(b), 8), thi};
  return g;
}
DI u32 granbad(u32x4 g, u32 thi, u32 tw) { return ((g.y ^ thi) | (g.w ^ thi)) | (((g.x ^ tw) | (g.z ^ tw)) >> 24); }
DI float granval(u32 lo) { return __uint_as_float(lo << 8); }
DI bf16x8 pack8(const f32x16& v, int o) {
  u32x4 r = {cvtpk(v[o], v[o + 1]), cvtpk(v[o + 2], v[o + 3]), cvtpk(v[o + 4], v[o + 5]), cvtpk(v[o + 6], v[o + 7])};
  return __builtin_bit_cast(bf16x8, r);
}
DI void lam_params(const Args& a, int ch, cpx& lam, cpx& coef) {
  float lr = bf2f(a.in[1][ch]), li = bf2f(a.in[2][ch]);
  float dt = bf2f(cvtpk(expf(bf2f(a.in[7][ch])), 0.f) & 0xffffu);
  float m = expf(dt * lr), sn, cs;
  sincosf(dt * li, &sn, &cs);
  lam.r = m * cs; lam.i = m * sn;
  float nr = lam.r - 1.f, ni = lam.i, den = lr * lr + li * li;
  coef.r = (nr * lr + ni * li) / den; coef.i = (ni * lr - nr * li) / den;
}
DI void wait_flags(__amdgpu_buffer_rsrc_t rw, u32 off, int l, u32 thi, u32 tl32) {
  for (int it = 0; it < SPIN_MAX; ++it) {
    fence();
    u32x2 f = __builtin_amdgcn_raw_buffer_load_b64(rw, off + (l & 15) * 8, 0, AUX_SC1);
    if (__builtin_amdgcn_ballot_w64((f.x != tl32) | (f.y != thi)) == 0) break;
    __builtin_amdgcn_s_sleep(1);
  }
  fence();
}
DI void softmax16(const f32x16& p, u32x4& o0, u32x4& o1) {
  float m = fmaxf(p[0], p[1]);
#pragma unroll
  for (int i = 2; i < 16; i += 2) m = fmaxf(fmaxf(m, p[i]), p[i + 1]);
  const float L2E = 1.44269504088896340736f;
  f32x2 nm = bc(-m * L2E), e[8];
#pragma unroll
  for (int i = 0; i < 8; ++i) {
    f32x2 t = {p[2 * i], p[2 * i + 1]};
    t = fma2(t, bc(L2E), nm);
    e[i].x = __builtin_amdgcn_exp2f(t.x); e[i].y = __builtin_amdgcn_exp2f(t.y);
  }
  f32x2 s = ((e[0] + e[1]) + (e[2] + e[3])) + ((e[4] + e[5]) + (e[6] + e[7]));
  f32x2 r = bc(__builtin_amdgcn_rcpf(s.x + s.y));
  u32 w[8];
#pragma unroll
  for (int i = 0; i < 8; ++i) { f32x2 t = e[i] * r; w[i] = cvtpk(t.x, t.y); }
  o0 = u32x4{w[0], w[1], w[2], w[3]}; o1 = u32x4{w[4], w[5], w[6], w[7]};
}
DI void store_out(float* out, const f32x16& a0, const f32x16& a1, int row0, int col0, int l) {
  float* p = out + (size_t)(row0 + 4 * (l >> 5)) * 1024 + col0 + (l & 31);
#pragma unroll
  for (int r = 0; r < 16; ++r) {
    p[(8 * (r >> 2) + (r & 3)) * 1024] = a0[r];
    p[(8 * (r >> 2) + (r & 3)) * 1024 + 32] = a1[r];
  }
}

#define A_SLOT 32768u
#define A_STG  0u
#define A_SEG  16384u
#define A_PART 20480u
#define A_LAM  98304u
#define B_SLOT 40960u
#define B_KVT  81920u
#define B_WO   98304u
#define B_WPHI 122880u

__global__ __launch_bounds__(512, 2) void fused_k(Args a) {
  extern __shared__ __attribute__((aligned(16))) char smem_[];
  const int b = blockIdx.x, g = b & 7, jb = b >> 3;
  const int tid = threadIdx.x, l = tid & 63;
  const int w = __builtin_amdgcn_readfirstlane(tid >> 6);
  const bool isA = g >= 4;
  const int tt = 4 * (g & 3) + (jb >> 3), ct = jb & 7;
  const int s0 = ((ct ^ (jb >> 3)) & 1) * 4;
  const int h = l >> 5, l31 = l & 31;
  STAMP(0);
  const u32 E = *(const __attribute__((address_space(4))) u32*)(a.ws + (isA ? WS_EPA : WS_EPB));
  const __amdgpu_buffer_rsrc_t rw = __builtin_amdgcn_make_buffer_rsrc((void*)a.ws, 0, (int)WS_BYTES, 0x00020000);
  const u32 flagoff = WS_FLAG + (isA ? 0u : 4096u) + tt * 256;

  if (w >= 4) {
    const int w4 = w - 4;
    const u32 lr8 = l >> 3;
    if (isA) {
      const u32x4 rs = srd(w4 < 2 ? a.in[0] : (w4 == 2 ? a.in[3] : a.in[4]), w4 < 2 ? 2097152u : 524288u);
      const u32 lp = w4 < 2 ? (32 * (lr8 >> 2) + 16 * (lr8 & 1) + ((lr8 >> 1) & 1)) : lr8;
      const u32 v0 = lp * 1024 + (((l & 7) ^ (l >> 4)) << 4), v1 = v0 ^ 64;
      const u32 rb = w4 < 2 ? (128 * tt + 64 * w4) : 64 * ct;
      const u32 pa = w4 < 2 ? 2048u : 8192u, pb = w4 < 2 ? 8192u : 32768u;
      auto issue = [&](int i) {
        const u32 so = rb * 1024 + (((s0 + i) & 7) << 7);
        const u32 dst = (i % 3) * A_SLOT + w4 * 8192;
#pragma unroll
        for (int p = 0; p < 8; ++p) glds(dst + p * 1024, (p & 1) ? v1 : v0, rs, so + (p & 3) * pa + (p >> 2) * pb);
      };
      issue(0); issue(1);
      const u32x4 rc = srd((w4 & 1) ? a.in[6] : a.in[5], 524288u);
      const u32 vc0 = lr8 * 1024 + (((l & 7) ^ (l >> 4)) << 4), vc1 = vc0 ^ 64;
      auto issueC = [&](int i) {
        const u32 so = 64 * ct * 1024 + (((ct + i) & 7) << 7);
        const u32 dst = (i < 4 ? 98304u + i * 16384u : (i < 6 ? 65536u + (i - 4) * 16384u : 32768u + (i - 6) * 16384u)) + (w4 & 1) * 8192;
#pragma unroll
        for (int p = 0; p < 8; ++p) glds(dst + p * 1024, (p & 1) ? vc1 : vc0, rc, so + p * 8192);
      };
      asm volatile("" :: "s"(E));
#pragma unroll 1
      for (int s = 0; s < 6; ++s) { WAITV(8); bar(); issue(s + 2); }
      const f32x2 lv = *(const LDS3 f32x2*)(A_LAM + l * 8);
      cpx l16 = csq(csq(csq(csq(cpx{lv.x, lv.y}))));
      cpx l128 = csq(csq(csq(l16)));
      cpx l256 = csq(l128), l512 = csq(l256);
      asm volatile("" : "+v"(l16.r), "+v"(l16.i), "+v"(l128.r), "+v"(l128.i), "+v"(l256.r), "+v"(l256.i), "+v"(l512.r), "+v"(l512.i));
      WAITV(8); bar(); issueC(w4 >> 1);
      WAITV(8); bar(); issueC(2 + (w4 >> 1));
      const u64 T = (u64)E * 0x9E3779B97F4A7C15ull + 0x632BE59BD9B4E019ull;
      const u32 thi = (u32)(T >> 32), tlo = (u32)(T >> 24) & 0xffu, tw = tlo << 24;
      STAMP(1);
      bar();
      if (w4 == 3) {
        cpx t = {0.f, 0.f};
#pragma unroll
        for (int s = 0; s < 8; ++s) { f32x2 e = *(const LDS3 f32x2*)(A_SEG + s * 512 + l * 8); t = cfma(l16, t, cpx{e.x, e.y}); }
        __builtin_amdgcn_raw_buffer_store_b128(gran2(t.r, t.i, thi, tlo), rw, WS_GA + (tt * 512 + 64 * ct + l) * 16, 0, AUX_SC1);
      }
      STAMP(2);
      cpx P = {0.f, 0.f};
      if (w4 < tt) {
        const int jl = w4 + ((tt - 1 - w4) & ~3), ex = tt - 1 - jl;
        u32x4 gq[4];
        for (int it = 0; it < SPIN_MAX; ++it) {
          fence();
          u32 bad = 0;
#pragma unroll
          for (int m = 0; m < 4; ++m) {
            const int j = jl - 4 * (3 - m);
            if (j >= 0) gq[m] = __builtin_amdgcn_raw_buffer_load_b128(rw, WS_GA + (64 * ct + l) * 16, j * 8192, AUX_SC1);
            else gq[m] = u32x4{tw, thi, tw, thi};
          }
#pragma unroll
          for (int m = 0; m < 4; ++m) bad |= granbad(gq[m], thi, tw);
          if (__builtin_amdgcn_ballot_w64(bad != 0) == 0) break;
        }
#pragma unroll
        for (int m = 0; m < 4; ++m) P = cfma(l512, P, cpx{granval(gq[m].x), granval(gq[m].z)});
        cpx f = {1.f, 0.f};
        if (ex & 1) f = l128;
        if (ex & 2) f = cmul(f, l256);
        P = cmul(P, f);
      }
      *(LDS3 f32x2*)(A_PART + l * 32 + w4 * 8) = f32x2{P.r, P.i};
      STAMP(3);
      WAITV(0);
      bar_l();
      issueC(4 + (w4 >> 1)); issueC(6 + (w4 >> 1));
      WAITV(0);
      bar();
      STAMP(4);
    } else {
      const u32x4 rx = srd(a.in[9], 2097152u), rq = srd(a.in[10], 1572864u);
      const u32 vx0 = lr8 * 1024 + (((l & 7) ^ (l >> 4)) << 4), vx1 = vx0 ^ 64;
      const u32 vw = lr8 * 3072 + (((l & 7) ^ (((l >> 4) & 1) << 2)) << 4);
      auto issue = [&](int i) {
        const u32 ks = (s0 + i) & 7;
        const u32 dst = (i % 3) * B_SLOT;
        const u32 sx = (128 * tt + 32 * w4) * 1024 + (ks << 7);
#pragma unroll
        for (int p = 0; p < 4; ++p) glds(dst + (4 * w4 + p) * 1024, (p & 1) ? vx1 : vx0, rx, sx + p * 8192);
        const u32 sw = ks * 64 * 3072 + 128 * ct;
#pragma unroll
        for (int p = 0; p < 6; ++p) { const u32 pc = 6 * w4 + p; glds(dst + 16384 + pc * 1024, vw, rq, sw + (pc & 7) * 24576 + (pc >> 3) * 1024); }
      };
      if (w4 < 2) glds(B_WPHI + w4 * 1024, l * 16, srd(a.in[11], 16384u), ct * 2048 + w4 * 1024);
      issue(0); issue(1);
      const u32x4 ro = srd(a.in[12], 524288u);
      const u32 vo = lr8 * 1024 + (((l & 7) ^ (((l >> 4) & 1) << 2)) << 4);
      auto issueO = [&](int i) {
        const u32 so = (((ct + i) & 7) << 16) + 128 * ct;
#pragma unroll
        for (int p = 0; p < 8; ++p) glds(B_WO + i * 8192 + p * 1024, vo, ro, so + p * 8192);
      };
      asm volatile("" :: "s"(E));
#pragma unroll 1
      for (int s = 0; s < 6; ++s) { WAITV(10); bar(); issue(s + 2); }
      WAITV(10); bar(); issueO(w4);
      WAITV(8); bar(); issueO(4 + w4);
      STAMP(1);
    }
  }
  const u64 T = (u64)E * 0x9E3779B97F4A7C15ull + 0x632BE59BD9B4E019ull;
  const u32 thi = (u32)(T >> 32), tl32 = (u32)T, tlo = (u32)(T >> 24) & 0xffu, tw = tlo << 24;
  const int wm = (w >> 1) & 1, wn = w & 1;
  const int G = l >> 4, tr_r = (l >> 2) & 3, tr_c = l & 3;
  const u32 fragrow = l31 * 128;
  const u32 fsw = (l31 >> 1) & 7;

  if (isA) {
    if (w < 4) {
      cpx lam, coef;
      lam_params(a, 64 * ct + 32 * wn + l31, lam, coef);
      if (wm == 0 && h == 0) *(LDS3 f32x2*)(A_LAM + (32 * wn + l31) * 8) = f32x2{lam.r, lam.i};
      const float dk0 = bf2f(a.in[8][64 * ct + l31]), dk1 = bf2f(a.in[8][64 * ct + 32 + l31]);
      const cpx l16 = csq(csq(csq(csq(lam))));
      u32 ax[4], aw[4];
#pragma unroll
      for (int k = 0; k < 4; ++k) {
        const u32 t = fragrow + (((2 * k + h) ^ fsw) << 4);
        ax[k] = t + wm * 8192; aw[k] = t + 16384 + wn * 4096;
      }
      const u32 rho = 64 * (w >> 1) + 16 * (G >> 1) + 8 * (tr_r >> 1) + 4 * (w & 1) + 2 * (tr_r & 1);
      const u32 xs0 = rho * 128 + (((2 * (G & 1) + (tr_c >> 1)) ^ ((rho >> 1) & 7)) << 4) + (tr_c & 1) * 8;
      f32x16 aR[2], aI[2];
      bf16x8 px0, px1, pwr, pwi;
#pragma unroll
      for (int i = 0; i < 16; ++i) { aR[0][i] = 0.f; aR[1][i] = 0.f; aI[0][i] = 0.f; aI[1][i] = 0.f; }
#pragma unroll
      for (int i = 0; i < 8; ++i) { px0[i] = 0; px1[i] = 0; pwr[i] = 0; pwi[i] = 0; }
      s16x4 xk[8];
#pragma unroll
      for (int i = 0; i < 8; ++i) xk[i] = s16x4{0, 0, 0, 0};
      asm volatile("" :: "s"(E));
      u32 slot = 0;
#pragma unroll 1
      for (int s = 0; s < 8; ++s) {
        bar_l();
#pragma unroll
        for (int k = 0; k < 4; ++k) {
          bf16x8 x0 = rd128(slot + ax[k]), wr = rd128(slot + aw[k]), x1 = rd128(slot + ax[k] + 4096), wi = rd128(slot + aw[k] + 8192);
          sb();
          aR[0] = mfma(px0, pwr, aR[0]); aR[1] = mfma(px1, pwr, aR[1]);
          aI[0] = mfma(px0, pwi, aI[0]); aI[1] = mfma(px1, pwi, aI[1]);
          sb();
          px0 = x0; px1 = x1; pwr = wr; pwi = wi;
        }
        if (((s0 + s) & 7) == ct) {
#pragma unroll
          for (int q = 0; q < 4; ++q) {
            const u32 o = slot + (32 * (q & 1) + (q >> 1)) * 128;
            xk[q] = __builtin_amdgcn_ds_read_tr16_b64_v4i16((LDS3 s16x4*)(o + xs0));
            xk[4 + q] = __builtin_amdgcn_ds_read_tr16_b64_v4i16((LDS3 s16x4*)(o + (xs0 ^ 64)));
          }
        }
        slot = slot == 2 * A_SLOT ? 0u : slot + A_SLOT;
      }
      aR[0] = mfma(px0, pwr, aR[0]); aI[0] = mfma(px0, pwi, aI[0]);
      aR[1] = mfma(px1, pwr, aR[1]); aI[1] = mfma(px1, pwi, aI[1]);
      STAMP(2);
      const f32x2 Lr = bc(lam.r), Li = bc(lam.i), nLi = bc(-lam.i);
      {
        f32x2 hr = {aR[0][0], aR[0][1]}, hi = {aI[0][0], aI[0][1]};
#pragma unroll
        for (int p = 1; p < 16; ++p) {
          const f32x2 ur = {aR[p >> 3][2 * (p & 7)], aR[p >> 3][2 * (p & 7) + 1]}, ui = {aI[p >> 3][2 * (p & 7)], aI[p >> 3][2 * (p & 7) + 1]};
          const f32x2 nr = fma2(Lr, hr, fma2(nLi, hi, ur)), ni = fma2(Lr, hi, fma2(Li, hr, ui));
          hr = nr; hi = ni;
        }
        const u32 sa = A_SEG + (4 * wm + 2 * h) * 512 + (32 * wn + l31) * 8;
        *(LDS3 f32x2*)sa = f32x2{hr.x, hi.x};
        *(LDS3 f32x2*)(sa + 512) = f32x2{hr.y, hi.y};
      }
      bar_l();
      STAMP(3);
      const cpx c2 = {2.f * coef.r, 2.f * coef.i};
      cpx pre0 = {0.f, 0.f}, pre1;
      {
        const int sg = 4 * wm + 2 * h;
        cpx e0 = {0.f, 0.f};
#pragma unroll
        for (int s = 0; s < 7; ++s) {
          f32x2 e = *(const LDS3 f32x2*)(A_SEG + s * 512 + (32 * wn + l31) * 8);
          cpx t = cfma(l16, pre0, cpx{e.x, e.y});
          if (s < sg) pre0 = t;
          if (s == sg) e0 = cpx{e.x, e.y};
        }
        pre1 = cfma(l16, pre0, e0);
      }
      pre0 = cmul(c2, pre0); pre1 = cmul(c2, pre1);
      cpx m0 = c2;
      {
        const cpx l32 = csq(l16), l64 = csq(l32);
        if (h) m0 = cmul(m0, l32);
        if (wm) m0 = cmul(m0, l64);
      }
      const cpx m1 = cmul(m0, l16);
#pragma unroll
      for (int p = 0; p < 16; ++p) {
        const f32x2 ur = {aR[p >> 3][2 * (p & 7)], aR[p >> 3][2 * (p & 7) + 1]}, ui = {aI[p >> 3][2 * (p & 7)], aI[p >> 3][2 * (p & 7) + 1]};
        const f32x2 vr = fma2(bc(c2.r), ur, bc(-c2.i) * ui), vi = fma2(bc(c2.r), ui, bc(c2.i) * ur);
        aR[p >> 3][2 * (p & 7)] = vr.x; aR[p >> 3][2 * (p & 7) + 1] = vr.y;
        aI[p >> 3][2 * (p & 7)] = vi.x; aI[p >> 3][2 * (p & 7) + 1] = vi.y;
      }
      f32x16 y0, y1;
#pragma unroll
      for (int q = 0; q < 4; ++q) {
        const u32x2 t0 = __builtin_bit_cast(u32x2, xk[q]), t1 = __builtin_bit_cast(u32x2, xk[4 + q]);
#pragma unroll
        for (int e = 0; e < 2; ++e) {
          const u32 r0 = cvtpk(bf2f(t0[e] & 0xffffu) * dk0, __uint_as_float(t0[e] & 0xffff0000u) * dk0);
          const u32 r1 = cvtpk(bf2f(t1[e] & 0xffffu) * dk1, __uint_as_float(t1[e] & 0xffff0000u) * dk1);
          y0[4 * q + 2 * e] = __uint_as_float(r0 << 16); y0[4 * q + 2 * e + 1] = __uint_as_float(r0 & 0xffff0000u);
          y1[4 * q + 2 * e] = __uint_as_float(r1 << 16); y1[4 * q + 2 * e + 1] = __uint_as_float(r1 & 0xffff0000u);
        }
      }
      asm volatile("" : "+v"(y0), "+v"(y1), "+v"(aR[0]), "+v"(aR[1]), "+v"(aI[0]), "+v"(aI[1]));
      STAMP(4);
      bar();
      STAMP(5);
      {
        const f32x4 pa = *(const LDS3 f32x4*)(A_PART + (32 * wn + l31) * 32), pb = *(const LDS3 f32x4*)(A_PART + (32 * wn + l31) * 32 + 16);
        const float cr = (pa.x + pa.z) + (pb.x + pb.z), ci = (pa.y + pa.w) + (pb.y + pb.w);
        const f32x2 Mr = {m0.r, m1.r}, Mi = {m0.i, m1.i};
        f32x2 hr = fma2(Mr, bc(cr), fma2(-Mi, bc(ci), f32x2{pre0.r, pre1.r}));
        f32x2 hi = fma2(Mr, bc(ci), fma2(Mi, bc(cr), f32x2{pre0.i, pre1.i}));
#pragma unroll
        for (int p = 0; p < 16; ++p) {
          const f32x2 ur = {aR[p >> 3][2 * (p & 7)], aR[p >> 3][2 * (p & 7) + 1]}, ui = {aI[p >> 3][2 * (p & 7)], aI[p >> 3][2 * (p & 7) + 1]};
          const f32x2 nr = fma2(Lr, hr, fma2(nLi, hi, ur)), ni = fma2(Lr, hi, fma2(Li, hr, ui));
          hr = nr; hi = ni;
          aR[p >> 3][2 * (p & 7)] = hr.x; aR[p >> 3][2 * (p & 7) + 1] = hr.y;
          aI[p >> 3][2 * (p & 7)] = hi.x; aI[p >> 3][2 * (p & 7) + 1] = hi.y;
        }
      }
      {
        const u32 f = (l31 & 1) | ((l31 & 2) << 1) | ((l31 & 4) >> 1);
        const u32 wb = A_STG + w * 4096 + l31 * 128;
        const u32 chr = 8 * (G >> 1) + tr_r;
        const u32 fr = (tr_r & 1) | ((tr_r & 2) << 1);
        const u32 rb0 = A_STG + w * 4096 + chr * 128 + (tr_c & 1) * 8;
        const u32 cq = 2 * (G & 1) + (tr_c >> 1);
        const u32 hoff = WS_HA + ((tt * 4 + 2 * wm) * 64 + ct * 8 + 2 * wn) * 1024 + l * 16;
#pragma unroll
        for (int ri = 0; ri < 2; ++ri) {
          const f32x16* src = ri ? aI : aR;
          const float sg = ri ? -1.f : 1.f;
#pragma unroll
          for (int c = 0; c < 2; ++c)
#pragma unroll
            for (int i = 0; i < 2; ++i) {
              u32x4 v = {cvtpk(sg * src[i][c], sg * src[i][2 + c]), cvtpk(sg * src[i][4 + c], sg * src[i][6 + c]),
                         cvtpk(sg * src[i][8 + c], sg * src[i][10 + c]), cvtpk(sg * src[i][12 + c], sg * src[i][14 + c])};
              *(LDS3 u32x4*)(wb + (((4 * h + 2 * c + i) ^ f) << 4)) = v;
            }
#pragma unroll
          for (int mt = 0; mt < 2; ++mt)
#pragma unroll
            for (int ks = 0; ks < 2; ++ks) {
              const bf16x8 fr8 = rdtr(rb0 + ks * 2048 + (((4 * mt + cq) ^ fr) << 4), rb0 + ks * 2048 + 512 + (((4 * mt + cq) ^ (fr | 2)) << 4));
              __builtin_amdgcn_raw_buffer_store_b128(__builtin_bit_cast(u32x4, fr8), rw, hoff + (mt * 64 + ri * 4 + ks) * 1024, 0, AUX_SC1);
            }
        }
      }
      STAMP(6);
      WAITV(0);
      if (l == 0) __builtin_amdgcn_raw_buffer_store_b64(u32x2{tl32, thi}, rw, flagoff + wm * 128 + (ct * 2 + wn) * 8, 0, AUX_SC1);
      STAMP(7);
      bar();
      wait_flags(rw, flagoff + wm * 128, l, thi, tl32);
      STAMP(8);
      {
        const u32 ha = WS_HA + (tt * 4 + w) * 65536 + l * 16;
        u32 ca[4], cb[4];
#pragma unroll
        for (int k = 0; k < 4; ++k) { const u32 t = fragrow + (((2 * k + h) ^ fsw) << 4); ca[k] = t + 98304; cb[k] = t + 32768; }
        u32x4 af[4][8];
        auto ld = [&](int i) {
          const u32 so = ((ct + i) & 7) * 8192;
#pragma unroll
          for (int k = 0; k < 8; ++k) af[i & 3][k] = __builtin_amdgcn_raw_buffer_load_b128(rw, ha + k * 1024, so, AUX_SC1);
        };
        ld(0); ld(1); ld(2);
        sb();
#pragma unroll
        for (int i = 0; i < 8; ++i) {
          if (i + 3 < 8) ld(i + 3);
          sb();
          const u32 io = i < 4 ? i * 16384 : (i < 6 ? 32768 + (i - 4) * 16384 : (i - 6) * 16384);
#pragma unroll
          for (int k = 0; k < 8; ++k) {
            const u32 ad = (i < 4 ? ca[k & 3] : cb[k & 3]) + io + (k >> 2) * 8192;
            const bf16x8 b0 = rd128(ad), b1 = rd128(ad + 4096);
            const bf16x8 av = __builtin_bit_cast(bf16x8, af[i & 3][k]);
            y0 = mfma(av, b0, y0); y1 = mfma(av, b1, y1);
          }
          sb();
        }
      }
      STAMP(9);
      store_out(a.out, y0, y1, 128 * tt + 32 * w, 64 * ct, l);
      if (tt == 15 && ct == 0 && w == 0 && l == 0) __builtin_amdgcn_raw_buffer_store_b32(E + 1, rw, WS_EPA, 0, AUX_SC1);
      STAMP(10);
    }
  } else {
    u32x4 pq[2][2][2];
    if (w < 4) {
      u32 ax[4];
#pragma unroll
      for (int k = 0; k < 4; ++k) ax[k] = fragrow + (((2 * k + h) ^ fsw) << 4) + wm * 8192;
      const u32 aw = 16384 + (8 * (G >> 1) + tr_r) * 128 + (((4 * wn + 2 * (G & 1) + (tr_c >> 1)) ^ ((tr_r >> 1) << 2)) << 4) + (tr_c & 1) * 8;
      f32x16 q[2], kk[2], v[2];
      bf16x8 px0, px1, pwq, pwk, pwv, wf[2];
#pragma unroll
      for (int i = 0; i < 16; ++i) { q[0][i] = 0.f; q[1][i] = 0.f; kk[0][i] = 0.f; kk[1][i] = 0.f; v[0][i] = 0.f; v[1][i] = 0.f; }
#pragma unroll
      for (int i = 0; i < 8; ++i) { px0[i] = 0; px1[i] = 0; pwq[i] = 0; pwk[i] = 0; pwv[i] = 0; wf[0][i] = 0; wf[1][i] = 0; }
      asm volatile("" :: "s"(E));
      u32 slot = 0;
#pragma unroll 1
      for (int s = 0; s < 8; ++s) {
        bar_l();
        if (s == 0) {
          STAMP(1);
          const u32 pa = B_WPHI + wn * 1024 + (4 * (G >> 1) + tr_r) * 32 + (2 * (G & 1) + (tr_c >> 1)) * 8;
          const u32 sgn = ((l >> 2) & 1) ? 0x80008000u : 0u;
#pragma unroll
          for (int hd = 0; hd < 2; ++hd) {
            u32x4 t = __builtin_bit_cast(u32x4, rdtr(pa + hd * 512, pa + hd * 512 + 256));
            t.x ^= sgn; t.y ^= sgn; t.z ^= sgn; t.w ^= sgn;
            wf[hd] = __builtin_bit_cast(bf16x8, t);
          }
        }
#pragma unroll
        for (int k = 0; k < 4; ++k) {
          const u32 wa = slot + aw + k * 2048;
          bf16x8 x0 = rd128(slot + ax[k]), wk = rdtr(wa + 8192, wa + 8192 + 512), x1 = rd128(slot + ax[k] + 4096);
          bf16x8 wv = rdtr(wa + 16384, wa + 16384 + 512), wq = rdtr(wa, wa + 512);
          sb();
          kk[0] = mfma(pwk, px0, kk[0]); kk[1] = mfma(pwk, px1, kk[1]);
          v[0] = mfma(px0, pwv, v[0]); v[1] = mfma(px1, pwv, v[1]);
          q[0] = mfma(pwq, px0, q[0]); q[1] = mfma(pwq, px1, q[1]);
          sb();
          px0 = x0; px1 = x1; pwq = wq; pwk = wk; pwv = wv;
        }
        slot = slot == 2 * B_SLOT ? 0u : slot + B_SLOT;
      }
      kk[0] = mfma(pwk, px0, kk[0]); kk[1] = mfma(pwk, px1, kk[1]);
      v[0] = mfma(px0, pwv, v[0]); v[1] = mfma(px1, pwv, v[1]);
      q[0] = mfma(pwq, px0, q[0]); q[1] = mfma(pwq, px1, q[1]);
      STAMP(2);
      f32x16 zero;
#pragma unroll
      for (int i = 0; i < 16; ++i) zero[i] = 0.f;
      const u32 stg = w * 8192;
      {
        const u32 wb = stg + l31 * 64, sw = (l31 >> 1) & 3;
#pragma unroll
        for (int mt = 0; mt < 2; ++mt)
#pragma unroll
          for (int hd = 0; hd < 2; ++hd) {
            const f32x16 p = mfma(wf[hd], pack8(kk[mt], 8 * hd), zero);
            u32x4 o0, o1;
            softmax16(p, o0, o1);
            *(LDS3 u32x4*)(wb + hd * 4096 + mt * 2048 + (((2 * h) ^ sw) << 4)) = o0;
            *(LDS3 u32x4*)(wb + hd * 4096 + mt * 2048 + (((2 * h + 1) ^ sw) << 4)) = o1;
          }
      }
      {
        const u32 ra = stg + (4 * (G >> 1) + tr_r) * 64 + (((2 * (G & 1) + (tr_c >> 1)) ^ (2 * (G >> 1) + (tr_r >> 1))) << 4) + (tr_c & 1) * 8;
        f32x16 kv[2] = {zero, zero};
        const bool lo = l31 < 16;
#pragma unroll
        for (int mt = 0; mt < 2; ++mt)
#pragma unroll
          for (int k2 = 0; k2 < 2; ++k2) {
            const u32x4 vb = __builtin_bit_cast(u32x4, pack8(v[mt], 8 * k2));
            const u32 one = 0x3F803F80u;
            const u32x4 v0 = {lo ? vb.x : one, lo ? vb.y : one, lo ? vb.z : one, lo ? vb.w : one};
            const u32x4 v1 = {lo ? one : vb.x, lo ? one : vb.y, lo ? one : vb.z, lo ? one : vb.w};
            const u32 o = ra + mt * 2048 + k2 * 1024;
            kv[0] = mfma(rdtr(o, o + 512), __builtin_bit_cast(bf16x8, v0), kv[0]);
            kv[1] = mfma(rdtr(o + 4096, o + 4096 + 512), __builtin_bit_cast(bf16x8, v1), kv[1]);
          }
#pragma unroll
        for (int hd = 0; hd < 2; ++hd) {
          const bool own = (l31 >> 4) == hd;
          if (own || (l31 & 15) == 0) {
            const u32 pa = stg + hd * 2448 + (own ? (l31 & 15) : 16) * 144 + h * 16;
#pragma unroll
            for (int qq = 0; qq < 4; ++qq)
              *(LDS3 f32x4*)(pa + qq * 32) = f32x4{kv[hd][4 * qq], kv[hd][4 * qq + 1], kv[hd][4 * qq + 2], kv[hd][4 * qq + 3]};
          }
        }
      }
      STAMP(3);
      bar_l();
      {
        const int row = tid >> 4, fp = tid & 15, h4 = row >= 17, dc = row - 17 * h4;
        const u32 ad = h4 * 2448 + dc * 144 + fp * 8;
        const f32x2 t = *(const LDS3 f32x2*)ad + *(const LDS3 f32x2*)(ad + 16384);
        __builtin_amdgcn_raw_buffer_store_b128(gran2(t.x, t.y, thi, tlo), rw,
            WS_G1 + ((ct * 16 + 4 * h4 + (fp >> 2)) * 16 + tt) * 1088 + dc * 64 + (fp & 3) * 16, 0, AUX_SC1);
      }
      STAMP(4);
#pragma unroll
      for (int mt = 0; mt < 2; ++mt)
#pragma unroll
        for (int hd = 0; hd < 2; ++hd) {
          const f32x16 p = mfma(wf[hd], pack8(q[mt], 8 * hd), zero);
          softmax16(p, pq[mt][hd][0], pq[mt][hd][1]);
        }
      asm volatile("" : "+v"(pq[0][0][0]), "+v"(pq[0][0][1]), "+v"(pq[0][1][0]), "+v"(pq[0][1][1]), "+v"(pq[1][0][0]), "+v"(pq[1][0][1]), "+v"(pq[1][1][0]), "+v"(pq[1][1][1]));
      STAMP(5);
    } else {
      bar();
      const int i = tid - 256;
#pragma unroll
      for (int m = 0; m < 4; ++m) {
        const int pi = 256 + i + m * 256;
        if (pi < 1088) {
          const int row = pi >> 4, fp = pi & 15, h4 = (row * 241) >> 12, dc = row - 17 * h4;
          const u32 ad = (h4 >> 1) * 8192 + (h4 & 1) * 2448 + dc * 144 + fp * 8;
          const f32x2 t = *(const LDS3 f32x2*)ad + *(const LDS3 f32x2*)(ad + 16384);
          __builtin_amdgcn_raw_buffer_store_b128(gran2(t.x, t.y, thi, tlo), rw,
              WS_G1 + ((ct * 16 + 4 * h4 + (fp >> 2)) * 16 + tt) * 1088 + dc * 64 + (fp & 3) * 16, 0, AUX_SC1);
        }
      }
      STAMP(2);
      if (w < 7) {
        const bool act = i < 136;
        const int pp = act ? (i >> 1) : 0;
        const u32 off = WS_G1 + ((ct * 16 + tt) * 16 + 8 * (i & 1)) * 1088 + pp * 16;
        u32x4 gq[8];
        for (int it = 0; it < SPIN_MAX; ++it) {
          fence();
          u32 bad = 0;
#pragma unroll
          for (int m = 0; m < 8; ++m) gq[m] = __builtin_amdgcn_raw_buffer_load_b128(rw, off + m * 1088, 0, AUX_SC1);
#pragma unroll
          for (int m = 0; m < 8; ++m) bad |= granbad(gq[m], thi, tw);
          if (__builtin_amdgcn_ballot_w64(bad != 0) == 0) break;
        }
        float sa = 0.f, sb_ = 0.f;
#pragma unroll
        for (int m = 0; m < 8; ++m) { sa += granval(gq[m].x); sb_ += granval(gq[m].z); }
        sa += __uint_as_float(__builtin_amdgcn_mov_dpp(__float_as_uint(sa), 0xB1, 0xf, 0xf, true));
        sb_ += __uint_as_float(__builtin_amdgcn_mov_dpp(__float_as_uint(sb_), 0xB1, 0xf, 0xf, true));
        if (act && !(i & 1))
          __builtin_amdgcn_raw_buffer_store_b128(gran2(sa, sb_, thi, tlo), rw,
              WS_G2 + ct * 17408 + (((tt >> 2) * 17 + (pp >> 2)) * 32 + 8 * (tt & 3) + 2 * (pp & 3)) * 8, 0, AUX_SC1);
      }
      STAMP(3);
      {
        u32x4 gq[5];
        const u32 off = WS_G2 + ct * 17408 + i * 16;
        for (int it = 0; it < SPIN_MAX; ++it) {
          fence();
          u32 bad = 0;
#pragma unroll
          for (int m = 0; m < 4; ++m) gq[m] = __builtin_amdgcn_raw_buffer_load_b128(rw, off + m * 4096, 0, AUX_SC1);
          if (w == 7) gq[4] = __builtin_amdgcn_raw_buffer_load_b128(rw, off + 16384 - 192 * 16, 0, AUX_SC1);
          else gq[4] = u32x4{tw, thi, tw, thi};
#pragma unroll
          for (int m = 0; m < 5; ++m) bad |= granbad(gq[m], thi, tw);
          if (__builtin_amdgcn_ballot_w64(bad != 0) == 0) break;
        }
#pragma unroll
        for (int m = 0; m < 4; ++m) *(LDS3 u32*)(B_KVT + (i + m * 256) * 4) = cvtpk(granval(gq[m].x), granval(gq[m].z));
        if (w == 7) *(LDS3 u32*)(B_KVT + (i + 1024 - 192) * 4) = cvtpk(granval(gq[4].x), granval(gq[4].z));
      }
      STAMP(4);
    }
    bar_l();
    if (w < 4) {
      STAMP(6);
      {
        const u32 rowd = l31 < 16 ? (8 * ((l31 >> 2) & 1) + 4 * (l31 >> 3) + (l31 & 3)) : 16;
        const u32 ka = B_KVT + (2 * wn * 17 + rowd) * 64 + h * 32;
        f32x16 zero;
#pragma unroll
        for (int i = 0; i < 16; ++i) zero[i] = 0.f;
        const u32 ooff = WS_OB + ((tt * 4 + 2 * wm) * 32 + 4 * ct + 2 * wn) * 1024 + l * 16;
#pragma unroll
        for (int hd = 0; hd < 2; ++hd) {
          const bf16x8 k0 = rd128(ka + hd * 1088), k1 = rd128(ka + hd * 1088 + 16);
#pragma unroll
          for (int mt = 0; mt < 2; ++mt) {
            f32x16 o = mfma(k0, __builtin_bit_cast(bf16x8, pq[mt][hd][0]), zero);
            o = mfma(k1, __builtin_bit_cast(bf16x8, pq[mt][hd][1]), o);
            const float r = __builtin_amdgcn_rcpf(o[8] + 1e-6f);
            const u32x4 ov = {cvtpk(o[0] * r, o[1] * r), cvtpk(o[2] * r, o[3] * r), cvtpk(o[4] * r, o[5] * r), cvtpk(o[6] * r, o[7] * r)};
            __builtin_amdgcn_raw_buffer_store_b128(ov, rw, ooff + (mt * 32 + hd) * 1024, 0, AUX_SC1);
          }
        }
      }
      WAITV(0);
      if (l == 0) __builtin_amdgcn_raw_buffer_store_b64(u32x2{tl32, thi}, rw, flagoff + wm * 128 + (ct * 2 + wn) * 8, 0, AUX_SC1);
      STAMP(7);
      wait_flags(rw, flagoff + wm * 128, l, thi, tl32);
      STAMP(8);
      f32x16 y0, y1;
#pragma unroll
      for (int i = 0; i < 16; ++i) { y0[i] = 0.f; y1[i] = 0.f; }
      {
        const u32 oa = WS_OB + (tt * 4 + w) * 32768 + l * 16;
        const u32 b0a = B_WO + (8 * (G >> 1) + tr_r) * 128 + (((2 * (G & 1) + (tr_c >> 1)) ^ ((tr_r >> 1) << 2)) << 4) + (tr_c & 1) * 8;
        const u32 b1a = b0a ^ 64;
        u32x4 af[8][4];
#pragma unroll
        for (int i = 0; i < 8; ++i) {
          const u32 so = ((ct + i) & 7) * 4096;
#pragma unroll
          for (int k = 0; k < 4; ++k) af[i][k] = __builtin_amdgcn_raw_buffer_load_b128(rw, oa + k * 1024, so, AUX_SC1);
        }
        sb();
#pragma unroll
        for (int i = 0; i < 8; ++i) {
#pragma unroll
          for (int k = 0; k < 4; ++k) {
            const u32 o = i * 8192 + k * 2048;
            const bf16x8 b0 = rdtr(b0a + o, b0a + o + 512), b1 = rdtr(b1a + o, b1a + o + 512);
            const bf16x8 av = __builtin_bit_cast(bf16x8, af[i][k]);
            y0 = mfma(av, b0, y0); y1 = mfma(av, b1, y1);
          }
        }
      }
      STAMP(9);
      store_out(a.out, y0, y1, 128 * tt + 32 * w, 512 + 64 * ct, l);
      if (tt == 15 && ct == 0 && w == 0 && l == 0) __builtin_amdgcn_raw_buffer_store_b32(E + 1, rw, WS_EPB, 0, AUX_SC1);
      STAMP(10);
    }
  }
}

#define BENCH_LAUNCH(first, last, k, grid, block, shm, st, ...) hipLaunchKernelGGL(k, dim3(grid), dim3(block), shm, st, __VA_ARGS__)

extern "C" void kernel_launch(void* const* d_in, const int* in_sizes, int n_in, void* d_out, int out_size, void* d_ws, size_t ws_size, hipStream_t stream) {
  Args a;
  for (int i = 0; i < 13; ++i) a.in[i] = (const u16*)d_in[i];
  a.out = (float*)d_out;
  a.ws = (unsigned char*)d_ws;
  (void)hipFuncSetAttribute((const void*)fused_k, hipFuncAttributeMaxDynamicSharedMemorySize, 163840);
  BENCH_LAUNCH(true, true, fused_k, 256, 512, 163840, stream, a);
}
```

```cpp
#define PROBE 0
#define PCH 2
#include <hip/hip_runtime.h>
#include <hip/hip_bf16.h>

#pragma clang diagnostic ignored "-Winline-asm"
typedef unsigned short u16;
typedef unsigned int u32;
typedef unsigned long long u64;
typedef __bf16 bf16x8 __attribute__((ext_vector_type(8)));
typedef __bf16 bf16x2 __attribute__((ext_vector_type(2)));
typedef short s16x4 __attribute__((ext_vector_type(4)));
typedef float f32x2 __attribute__((ext_vector_type(2)));
typedef float f32x4 __attribute__((ext_vector_type(4)));
typedef float f32x16 __attribute__((ext_vector_type(16)));
typedef u32 u32x2 __attribute__((ext_vector_type(2)));
typedef u32 u32x4 __attribute__((ext_vector_type(4)));
#define LDS3 __attribute__((address_space(3)))
#define DI __device__ __forceinline__

#define WS_EPA   0u
#define WS_EPB   256u
#define WS_FLAG  4096u
#define WS_GA    65536u
#define WS_G2    (WS_GA + 131072u)
#define WS_G1    524288u
#define WS_HA    4194304u
#define WS_OB    8388608u
#define WS_BYTES 16777216u
#define WS_DIAG  16777216u
#define AUX_SC1 16
#define SPIN_MAX (1 << 18)

struct Args { const u16* in[13]; float* out; unsigned char* ws; };

#define STAMP(i) do { if ((i) == 0) tp0 = __builtin_amdgcn_s_memrealtime(); if ((i) == PROBE) tp1 = __builtin_amdgcn_s_memrealtime(); } while (0)

DI float bf2f(u32 v) { return __uint_as_float(v << 16); }
DI u32 cvtpk(float a, float b) { f32x2 t = {a, b}; return __builtin_bit_cast(u32, __builtin_convertvector(t, bf16x2)); }
DI u32x4 srd(const void* p, u32 bytes) { u64 a = (u64)p; u32x4 r = {(u32)a, (u32)(a >> 32) & 0xffffu, bytes, 0x00020000u}; return r; }
DI void glds(u32 ldsdst, u32 voff, u32x4 rs, u32 soff) {
  asm volatile("s_mov_b32 m0, %0\n\ts_nop 0\n\tbuffer_load_dwordx4 %1, %2, %3 offen lds" :: "s"(ldsdst), "v"(voff), "s"(rs), "s"(soff) : "m0", "memory");
}
#define WAITV(n) asm volatile("s_waitcnt vmcnt(%0)" :: "n"(n) : "memory")
DI void bar_l() { asm volatile("s_waitcnt lgkmcnt(0)\n\ts_barrier" ::: "memory"); }
DI void bar() { asm volatile("s_barrier" ::: "memory"); }
DI void fence() { asm volatile("" ::: "memory"); }
DI void sb() { __builtin_amdgcn_sched_barrier(0); }
DI bf16x8 rd128(u32 a) { return *(const LDS3 bf16x8*)a; }
DI bf16x8 rdtr(u32 a0, u32 a1) {
  s16x4 x = __builtin_amdgcn_ds_read_tr16_b64_v4i16((LDS3 s16x4*)a0);
  s16x4 y = __builtin_amdgcn_ds_read_tr16_b64_v4i16((LDS3 s16x4*)a1);
  return __builtin_bit_cast(bf16x8, __builtin_shufflevector(x, y, 0, 1, 2, 3, 4, 5, 6, 7));
}
DI f32x16 mfma(bf16x8 a, bf16x8 b, f32x16 c) { return __builtin_amdgcn_mfma_f32_32x32x16_bf16(a, b, c, 0, 0, 0); }
DI f32x2 fma2(f32x2 a, f32x2 b, f32x2 c) { return __builtin_elementwise_fma(a, b, c); }
DI f32x2 bc(float a) { f32x2 r = {a, a}; return r; }
struct cpx { float r, i; };
DI cpx cmul(cpx a, cpx b) { cpx c; c.r = a.r * b.r - a.i * b.i; c.i = a.r * b.i + a.i * b.r; return c; }
DI cpx cfma(cpx a, cpx b, cpx c) { cpx d; d.r = fmaf(a.r, b.r, fmaf(-a.i, b.i, c.r)); d.i = fmaf(a.r, b.i, fmaf(a.i, b.r, c.i)); return d; }
DI cpx csq(cpx a) { return cmul(a, a); }
DI u32x4 gran2(float a, float b, u32 thi, u32 tlo) {
  u32x4 g = {__builtin_amdgcn_alignbit(tlo, __float_as_uint(a), 8), thi, __builtin_amdgcn_alignbit(tlo, __float_as_uint(b), 8), thi};
  return g;
}
DI u32 granbad(u32x4 g, u32 thi, u32 tw) { return ((g.y ^ thi) | (g.w ^ thi)) | (((g.x ^ tw) | (g.z ^ tw)) >> 24); }
DI float granval(u32 lo) { return __uint_as_float(lo << 8); }
DI bf16x8 pack8(const f32x16& v, int o) {
  u32x4 r = {cvtpk(v[o], v[o + 1]), cvtpk(v[o + 2], v[o + 3]), cvtpk(v[o + 4], v[o + 5]), cvtpk(v[o + 6], v[o + 7])};
  return __builtin_bit_cast(bf16x8, r);
}
DI void lam_params(const Args& a, int ch, cpx& lam, cpx& coef) {
  float lr = bf2f(a.in[1][ch]), li = bf2f(a.in[2][ch]);
  float dt = bf2f(cvtpk(expf(bf2f(a.in[7][ch])), 0.f) & 0xffffu);
  float m = expf(dt * lr), sn, cs;
  sincosf(dt * li, &sn, &cs);
  lam.r = m * cs; lam.i = m * sn;
  float nr = lam.r - 1.f, ni = lam.i, den = lr * lr + li * li;
  coef.r = (nr * lr + ni * li) / den; coef.i = (ni * lr - nr * li) / den;
}
DI void wait_flags(__amdgpu_buffer_rsrc_t rw, u32 off, int l, u32 thi, u32 tl32) {
  for (int it = 0; it < SPIN_MAX; ++it) {
    fence();
    u32x2 f = __builtin_amdgcn_raw_buffer_load_b64(rw, off + (l & 15) * 8, 0, AUX_SC1);
    if (__builtin_amdgcn_ballot_w64((f.x != tl32) | (f.y != thi)) == 0) break;
    __builtin_amdgcn_s_sleep(1);
  }
  fence();
}
DI void softmax16(const f32x16& p, u32x4& o0, u32x4& o1) {
  float m = fmaxf(p[0], p[1]);
#pragma unroll
  for (int i = 2; i < 16; i += 2) m = fmaxf(fmaxf(m, p[i]), p[i + 1]);
  const float L2E = 1.44269504088896340736f;
  f32x2 nm = bc(-m * L2E), e[8];
#pragma unroll
  for (int i = 0; i < 8; ++i) {
    f32x2 t = {p[2 * i], p[2 * i + 1]};
    t = fma2(t, bc(L2E), nm);
    e[i].x = __builtin_amdgcn_exp2f(t.x); e[i].y = __builtin_amdgcn_exp2f(t.y);
  }
  f32x2 s = ((e[0] + e[1]) + (e[2] + e[3])) + ((e[4] + e[5]) + (e[6] + e[7]));
  f32x2 r = bc(__builtin_amdgcn_rcpf(s.x + s.y));
  u32 w[8];
#pragma unroll
  for (int i = 0; i < 8; ++i) { f32x2 t = e[i] * r; w[i] = cvtpk(t.x, t.y); }
  o0 = u32x4{w[0], w[1], w[2], w[3]}; o1 = u32x4{w[4], w[5], w[6], w[7]};
}
DI void store_out(float* out, const f32x16& a0, const f32x16& a1, int row0, int col0, int l) {
  float* p = out + (size_t)(row0 + 4 * (l >> 5)) * 1024 + col0 + (l & 31);
#pragma unroll
  for (int r = 0; r < 16; ++r) {
    p[(8 * (r >> 2) + (r & 3)) * 1024] = a0[r];
    p[(8 * (r >> 2) + (r & 3)) * 1024 + 32] = a1[r];
  }
}

#define A_SLOT 32768u
#define A_STG  0u
#define A_SEG  16384u
#define A_PART 20480u
#define A_LAM  98304u
#define B_SLOT 40960u
#define B_KVT  81920u
#define B_WO   98304u
#define B_WPHI 122880u

__global__ __launch_bounds__(512, 2) void fused_k(Args a) {
  extern __shared__ __attribute__((aligned(16))) char smem_[];
  const int b = blockIdx.x, g = b & 7, jb = b >> 3;
  const int tid = threadIdx.x, l = tid & 63;
  const int w = __builtin_amdgcn_readfirstlane(tid >> 6);
  const bool isA = g >= 4;
  const int tt = 4 * (g & 3) + (jb >> 3), ct = jb & 7;
  const int s0 = ((ct ^ (jb >> 3)) & 1) * 4;
  const int h = l >> 5, l31 = l & 31;
  u64 tp0 = 0, tp1 = 0;
  STAMP(0);
  const u32 E = *(const __attribute__((address_space(4))) u32*)(a.ws + (isA ? WS_EPA : WS_EPB));
  const __amdgpu_buffer_rsrc_t rw = __builtin_amdgcn_make_buffer_rsrc((void*)a.ws, 0, (int)WS_BYTES, 0x00020000);
  const u32 flagoff = WS_FLAG + (isA ? 0u : 4096u) + tt * 256;

  if (w >= 4) {
    const int w4 = w - 4;
    const u32 lr8 = l >> 3;
    if (isA) {
      const u32x4 rs = srd(w4 < 2 ? a.in[0] : (w4 == 2 ? a.in[3] : a.in[4]), w4 < 2 ? 2097152u : 524288u);
      const u32 lp = w4 < 2 ? (32 * (lr8 >> 2) + 16 * (lr8 & 1) + ((lr8 >> 1) & 1)) : lr8;
      const u32 v0 = lp * 1024 + (((l & 7) ^ (l >> 4)) << 4), v1 = v0 ^ 64;
      const u32 rb = w4 < 2 ? (128 * tt + 64 * w4) : 64 * ct;
      const u32 pa = w4 < 2 ? 2048u : 8192u, pb = w4 < 2 ? 8192u : 32768u;
      auto issue = [&](int i) {
        const u32 so = rb * 1024 + (((s0 + i) & 7) << 7);
        const u32 dst = (i % 3) * A_SLOT + w4 * 8192;
#pragma unroll
        for (int p = 0; p < 8; ++p) glds(dst + p * 1024, (p & 1) ? v1 : v0, rs, so + (p & 3) * pa + (p >> 2) * pb);
      };
      issue(0); issue(1);
      const u32x4 rc = srd((w4 & 1) ? a.in[6] : a.in[5], 524288u);
      const u32 vc0 = lr8 * 1024 + (((l & 7) ^ (l >> 4)) << 4), vc1 = vc0 ^ 64;
      auto issueC = [&](int i) {
        const u32 so = 64 * ct * 1024 + (((ct + i) & 7) << 7);
        const u32 dst = (i < 4 ? 98304u + i * 16384u : (i < 6 ? 65536u + (i - 4) * 16384u : 32768u + (i - 6) * 16384u)) + (w4 & 1) * 8192;
#pragma unroll
        for (int p = 0; p < 8; ++p) glds(dst + p * 1024, (p & 1) ? vc1 : vc0, rc, so + p * 8192);
      };
      asm volatile("" :: "s"(E));
#pragma unroll 1
      for (int s = 0; s < 6; ++s) { WAITV(8); bar(); issue(s + 2); }
      const f32x2 lv = *(const LDS3 f32x2*)(A_LAM + l * 8);
      cpx l16 = csq(csq(csq(csq(cpx{lv.x, lv.y}))));
      cpx l128 = csq(csq(csq(l16)));
      cpx l256 = csq(l128), l512 = csq(l256);
      asm volatile("" : "+v"(l16.r), "+v"(l16.i), "+v"(l128.r), "+v"(l128.i), "+v"(l256.r), "+v"(l256.i), "+v"(l512.r), "+v"(l512.i));
      WAITV(8); bar(); issueC(w4 >> 1);
      WAITV(8); bar(); issueC(2 + (w4 >> 1));
      const u64 T = (u64)E * 0x9E3779B97F4A7C15ull + 0x632BE59BD9B4E019ull;
      const u32 thi = (u32)(T >> 32), tlo = (u32)(T >> 24) & 0xffu, tw = tlo << 24;
      STAMP(1);
      bar();
      if (w4 == 3) {
        cpx t = {0.f, 0.f};
#pragma unroll
        for (int s = 0; s < 8; ++s) { f32x2 e = *(const LDS3 f32x2*)(A_SEG + s * 512 + l * 8); t = cfma(l16, t, cpx{e.x, e.y}); }
        __builtin_amdgcn_raw_buffer_store_b128(gran2(t.r, t.i, thi, tlo), rw, WS_GA + (tt * 512 + 64 * ct + l) * 16, 0, AUX_SC1);
      }
      STAMP(2);
      cpx P = {0.f, 0.f};
      if (w4 < tt) {
        const int jl = w4 + ((tt - 1 - w4) & ~3), ex = tt - 1 - jl;
        u32x4 gq[4];
        for (int it = 0; it < SPIN_MAX; ++it) {
          fence();
          u32 bad = 0;
#pragma unroll
          for (int m = 0; m < 4; ++m) {
            const int j = jl - 4 * (3 - m);
            if (j >= 0) gq[m] = __builtin_amdgcn_raw_buffer_load_b128(rw, WS_GA + (64 * ct + l) * 16, j * 8192, AUX_SC1);
            else gq[m] = u32x4{tw, thi, tw, thi};
          }
#pragma unroll
          for (int m = 0; m < 4; ++m) bad |= granbad(gq[m], thi, tw);
          if (__builtin_amdgcn_ballot_w64(bad != 0) == 0) break;
        }
#pragma unroll
        for (int m = 0; m < 4; ++m) P = cfma(l512, P, cpx{granval(gq[m].x), granval(gq[m].z)});
        cpx f = {1.f, 0.f};
        if (ex & 1) f = l128;
        if (ex & 2) f = cmul(f, l256);
        P = cmul(P, f);
      }
      *(LDS3 f32x2*)(A_PART + l * 32 + w4 * 8) = f32x2{P.r, P.i};
      STAMP(3);
      WAITV(0);
      bar_l();
      issueC(4 + (w4 >> 1)); issueC(6 + (w4 >> 1));
      WAITV(0);
      bar();
      STAMP(4);
    } else {
      const u32x4 rx = srd(a.in[9], 2097152u), rq = srd(a.in[10], 1572864u);
      const u32 vx0 = lr8 * 1024 + (((l & 7) ^ (l >> 4)) << 4), vx1 = vx0 ^ 64;
      const u32 vw = lr8 * 3072 + (((l & 7) ^ (((l >> 4) & 1) << 2)) << 4);
      auto issue = [&](int i) {
        const u32 ks = (s0 + i) & 7;
        const u32 dst = (i % 3) * B_SLOT;
        const u32 sx = (128 * tt + 32 * w4) * 1024 + (ks << 7);
#pragma unroll
        for (int p = 0; p < 4; ++p) glds(dst + (4 * w4 + p) * 1024, (p & 1) ? vx1 : vx0, rx, sx + p * 8192);
        const u32 sw = ks * 64 * 3072 + 128 * ct;
#pragma unroll
        for (int p = 0; p < 6; ++p) { const u32 pc = 6 * w4 + p; glds(dst + 16384 + pc * 1024, vw, rq, sw + (pc & 7) * 24576 + (pc >> 3) * 1024); }
      };
      if (w4 < 2) glds(B_WPHI + w4 * 1024, l * 16, srd(a.in[11], 16384u), ct * 2048 + w4 * 1024);
      issue(0); issue(1);
      const u32x4 ro = srd(a.in[12], 524288u);
      const u32 vo = lr8 * 1024 + (((l & 7) ^ (((l >> 4) & 1) << 2)) << 4);
      auto issueO = [&](int i) {
        const u32 so = (((ct + i) & 7) << 16) + 128 * ct;
#pragma unroll
        for (int p = 0; p < 8; ++p) glds(B_WO + i * 8192 + p * 1024, vo, ro, so + p * 8192);
      };
      asm volatile("" :: "s"(E));
#pragma unroll 1
      for (int s = 0; s < 6; ++s) { WAITV(10); bar(); issue(s + 2); }
      WAITV(10); bar(); issueO(w4);
      WAITV(8); bar(); issueO(4 + w4);
      STAMP(1);
    }
  }
  const u64 T = (u64)E * 0x9E3779B97F4A7C15ull + 0x632BE59BD9B4E019ull;
  const u32 thi = (u32)(T >> 32), tl32 = (u32)T, tlo = (u32)(T >> 24) & 0xffu, tw = tlo << 24;
  const int wm = (w >> 1) & 1, wn = w & 1;
  const int G = l >> 4, tr_r = (l >> 2) & 3, tr_c = l & 3;
  const u32 fragrow = l31 * 128;
  const u32 fsw = (l31 >> 1) & 7;

  if (isA) {
    if (w < 4) {
      cpx lam, coef;
      lam_params(a, 64 * ct + 32 * wn + l31, lam, coef);
      if (wm == 0 && h == 0) *(LDS3 f32x2*)(A_LAM + (32 * wn + l31) * 8) = f32x2{lam.r, lam.i};
      const float dk0 = bf2f(a.in[8][64 * ct + l31]), dk1 = bf2f(a.in[8][64 * ct + 32 + l31]);
      const cpx l16 = csq(csq(csq(csq(lam))));
      u32 ax[4], aw[4];
#pragma unroll
      for (int k = 0; k < 4; ++k) {
        const u32 t = fragrow + (((2 * k + h) ^ fsw) << 4);
        ax[k] = t + wm * 8192; aw[k] = t + 16384 + wn * 4096;
      }
      const u32 rho = 64 * (w >> 1) + 16 * (G >> 1) + 8 * (tr_r >> 1) + 4 * (w & 1) + 2 * (tr_r & 1);
      const u32 xs0 = rho * 128 + (((2 * (G & 1) + (tr_c >> 1)) ^ ((rho >> 1) & 7)) << 4) + (tr_c & 1) * 8;
      f32x16 aR[2], aI[2];
      bf16x8 px0, px1, pwr, pwi;
#pragma unroll
      for (int i = 0; i < 16; ++i) { aR[0][i] = 0.f; aR[1][i] = 0.f; aI[0][i] = 0.f; aI[1][i] = 0.f; }
#pragma unroll
      for (int i = 0; i < 8; ++i) { px0[i] = 0; px1[i] = 0; pwr[i] = 0; pwi[i] = 0; }
      s16x4 xk[8];
#pragma unroll
      for (int i = 0; i < 8; ++i) xk[i] = s16x4{0, 0, 0, 0};
      asm volatile("" :: "s"(E));
      u32 slot = 0;
#pragma unroll 1
      for (int s = 0; s < 8; ++s) {
        bar_l();
        if (s == 0) STAMP(1);
#pragma unroll
        for (int k = 0; k < 4; ++k) {
          bf16x8 x0 = rd128(slot + ax[k]), wr = rd128(slot + aw[k]), x1 = rd128(slot + ax[k] + 4096), wi = rd128(slot + aw[k] + 8192);
          sb();
          aR[0] = mfma(px0, pwr, aR[0]); aR[1] = mfma(px1, pwr, aR[1]);
          aI[0] = mfma(px0, pwi, aI[0]); aI[1] = mfma(px1, pwi, aI[1]);
          sb();
          px0 = x0; px1 = x1; pwr = wr; pwi = wi;
        }
        if (((s0 + s) & 7) == ct) {
#pragma unroll
          for (int q = 0; q < 4; ++q) {
            const u32 o = slot + (32 * (q & 1) + (q >> 1)) * 128;
            xk[q] = __builtin_amdgcn_ds_read_tr16_b64_v4i16((LDS3 s16x4*)(o + xs0));
            xk[4 + q] = __builtin_amdgcn_ds_read_tr16_b64_v4i16((LDS3 s16x4*)(o + (xs0 ^ 64)));
          }
        }
        slot = slot == 2 * A_SLOT ? 0u : slot + A_SLOT;
      }
      aR[0] = mfma(px0, pwr, aR[0]); aI[0] = mfma(px0, pwi, aI[0]);
      aR[1] = mfma(px1, pwr, aR[1]); aI[1] = mfma(px1, pwi, aI[1]);
      STAMP(2);
      const f32x2 Lr = bc(lam.r), Li = bc(lam.i), nLi = bc(-lam.i);
      {
        f32x2 hr = {aR[0][0], aR[0][1]}, hi = {aI[0][0], aI[0][1]};
#pragma unroll
        for (int p = 1; p < 16; ++p) {
          const f32x2 ur = {aR[p >> 3][2 * (p & 7)], aR[p >> 3][2 * (p & 7) + 1]}, ui = {aI[p >> 3][2 * (p & 7)], aI[p >> 3][2 * (p & 7) + 1]};
          const f32x2 nr = fma2(Lr, hr, fma2(nLi, hi, ur)), ni = fma2(Lr, hi, fma2(Li, hr, ui));
          hr = nr; hi = ni;
        }
        const u32 sa = A_SEG + (4 * wm + 2 * h) * 512 + (32 * wn + l31) * 8;
        *(LDS3 f32x2*)sa = f32x2{hr.x, hi.x};
        *(LDS3 f32x2*)(sa + 512) = f32x2{hr.y, hi.y};
      }
      bar_l();
      STAMP(3);
      const cpx c2 = {2.f * coef.r, 2.f * coef.i};
      cpx pre0 = {0.f, 0.f}, pre1;
      {
        const int sg = 4 * wm + 2 * h;
        cpx e0 = {0.f, 0.f};
#pragma unroll
        for (int s = 0; s < 7; ++s) {
          f32x2 e = *(const LDS3 f32x2*)(A_SEG + s * 512 + (32 * wn + l31) * 8);
          cpx t = cfma(l16, pre0, cpx{e.x, e.y});
          if (s < sg) pre0 = t;
          if (s == sg) e0 = cpx{e.x, e.y};
        }
        pre1 = cfma(l16, pre0, e0);
      }
      pre0 = cmul(c2, pre0); pre1 = cmul(c2, pre1);
      cpx m0 = c2;
      {
        const cpx l32 = csq(l16), l64 = csq(l32);
        if (h) m0 = cmul(m0, l32);
        if (wm) m0 = cmul(m0, l64);
      }
      const cpx m1 = cmul(m0, l16);
#pragma unroll
      for (int p = 0; p < 16; ++p) {
        const f32x2 ur = {aR[p >> 3][2 * (p & 7)], aR[p >> 3][2 * (p & 7) + 1]}, ui = {aI[p >> 3][2 * (p & 7)], aI[p >> 3][2 * (p & 7) + 1]};
        const f32x2 vr = fma2(bc(c2.r), ur, bc(-c2.i) * ui), vi = fma2(bc(c2.r), ui, bc(c2.i) * ur);
        aR[p >> 3][2 * (p & 7)] = vr.x; aR[p >> 3][2 * (p & 7) + 1] = vr.y;
        aI[p >> 3][2 * (p & 7)] = vi.x; aI[p >> 3][2 * (p & 7) + 1] = vi.y;
      }
      f32x16 y0, y1;
#pragma unroll
      for (int q = 0; q < 4; ++q) {
        const u32x2 t0 = __builtin_bit_cast(u32x2, xk[q]), t1 = __builtin_bit_cast(u32x2, xk[4 + q]);
#pragma unroll
        for (int e = 0; e < 2; ++e) {
          const u32 r0 = cvtpk(bf2f(t0[e] & 0xffffu) * dk0, __uint_as_float(t0[e] & 0xffff0000u) * dk0);
          const u32 r1 = cvtpk(bf2f(t1[e] & 0xffffu) * dk1, __uint_as_float(t1[e] & 0xffff0000u) * dk1);
          y0[4 * q + 2 * e] = __uint_as_float(r0 << 16); y0[4 * q + 2 * e + 1] = __uint_as_float(r0 & 0xffff0000u);
          y1[4 * q + 2 * e] = __uint_as_float(r1 << 16); y1[4 * q + 2 * e + 1] = __uint_as_float(r1 & 0xffff0000u);
        }
      }
      asm volatile("" : "+v"(y0), "+v"(y1), "+v"(aR[0]), "+v"(aR[1]), "+v"(aI[0]), "+v"(aI[1]));
      STAMP(4);
      bar();
      STAMP(5);
      {
        const f32x4 pa = *(const LDS3 f32x4*)(A_PART + (32 * wn + l31) * 32), pb = *(const LDS3 f32x4*)(A_PART + (32 * wn + l31) * 32 + 16);
        const float cr = (pa.x + pa.z) + (pb.x + pb.z), ci = (pa.y + pa.w) + (pb.y + pb.w);
        const f32x2 Mr = {m0.r, m1.r}, Mi = {m0.i, m1.i};
        f32x2 hr = fma2(Mr, bc(cr), fma2(-Mi, bc(ci), f32x2{pre0.r, pre1.r}));
        f32x2 hi = fma2(Mr, bc(ci), fma2(Mi, bc(cr), f32x2{pre0.i, pre1.i}));
#pragma unroll
        for (int p = 0; p < 16; ++p) {
          const f32x2 ur = {aR[p >> 3][2 * (p & 7)], aR[p >> 3][2 * (p & 7) + 1]}, ui = {aI[p >> 3][2 * (p & 7)], aI[p >> 3][2 * (p & 7) + 1]};
          const f32x2 nr = fma2(Lr, hr, fma2(nLi, hi, ur)), ni = fma2(Lr, hi, fma2(Li, hr, ui));
          hr = nr; hi = ni;
          aR[p >> 3][2 * (p & 7)] = hr.x; aR[p >> 3][2 * (p & 7) + 1] = hr.y;
          aI[p >> 3][2 * (p & 7)] = hi.x; aI[p >> 3][2 * (p & 7) + 1] = hi.y;
        }
      }
      {
        const u32 f = (l31 & 1) | ((l31 & 2) << 1) | ((l31 & 4) >> 1);
        const u32 wb = A_STG + w * 4096 + l31 * 128;
        const u32 chr = 8 * (G >> 1) + tr_r;
        const u32 fr = (tr_r & 1) | ((tr_r & 2) << 1);
        const u32 rb0 = A_STG + w * 4096 + chr * 128 + (tr_c & 1) * 8;
        const u32 cq = 2 * (G & 1) + (tr_c >> 1);
        const u32 hoff = WS_HA + ((tt * 4 + 2 * wm) * 64 + ct * 8 + 2 * wn) * 1024 + l * 16;
#pragma unroll
        for (int ri = 0; ri < 2; ++ri) {
          const f32x16* src = ri ? aI : aR;
          const float sg = ri ? -1.f : 1.f;
#pragma unroll
          for (int c = 0; c < 2; ++c)
#pragma unroll
            for (int i = 0; i < 2; ++i) {
              u32x4 v = {cvtpk(sg * src[i][c], sg * src[i][2 + c]), cvtpk(sg * src[i][4 + c], sg * src[i][6 + c]),
                         cvtpk(sg * src[i][8 + c], sg * src[i][10 + c]), cvtpk(sg * src[i][12 + c], sg * src[i][14 + c])};
              *(LDS3 u32x4*)(wb + (((4 * h + 2 * c + i) ^ f) << 4)) = v;
            }
#pragma unroll
          for (int mt = 0; mt < 2; ++mt)
#pragma unroll
            for (int ks = 0; ks < 2; ++ks) {
              const bf16x8 fr8 = rdtr(rb0 + ks * 2048 + (((4 * mt + cq) ^ fr) << 4), rb0 + ks * 2048 + 512 + (((4 * mt + cq) ^ (fr | 2)) << 4));
              __builtin_amdgcn_raw_buffer_store_b128(__builtin_bit_cast(u32x4, fr8), rw, hoff + (mt * 64 + ri * 4 + ks) * 1024, 0, AUX_SC1);
            }
        }
      }
      STAMP(6);
      WAITV(0);
      if (l == 0) __builtin_amdgcn_raw_buffer_store_b64(u32x2{tl32, thi}, rw, flagoff + wm * 128 + (ct * 2 + wn) * 8, 0, AUX_SC1);
      STAMP(7);
      bar();
      wait_flags(rw, flagoff + wm * 128, l, thi, tl32);
      STAMP(8);
      {
        const u32 ha = WS_HA + (tt * 4 + w) * 65536 + l * 16;
        u32 ca[4], cb[4];
#pragma unroll
        for (int k = 0; k < 4; ++k) { const u32 t = fragrow + (((2 * k + h) ^ fsw) << 4); ca[k] = t + 98304; cb[k] = t + 32768; }
        u32x4 af[4][8];
        auto ld = [&](int i) {
          const u32 so = ((ct + i) & 7) * 8192;
#pragma unroll
          for (int k = 0; k < 8; ++k) af[i & 3][k] = __builtin_amdgcn_raw_buffer_load_b128(rw, ha + k * 1024, so, AUX_SC1);
        };
        ld(0); ld(1); ld(2);
        sb();
#pragma unroll
        for (int i = 0; i < 8; ++i) {
          if (i + 3 < 8) ld(i + 3);
          sb();
          const u32 io = i < 4 ? i * 16384 : (i < 6 ? 32768 + (i - 4) * 16384 : (i - 6) * 16384);
#pragma unroll
          for (int k = 0; k < 8; ++k) {
            const u32 ad = (i < 4 ? ca[k & 3] : cb[k & 3]) + io + (k >> 2) * 8192;
            const bf16x8 b0 = rd128(ad), b1 = rd128(ad + 4096);
            const bf16x8 av = __builtin_bit_cast(bf16x8, af[i & 3][k]);
            y0 = mfma(av, b0, y0); y1 = mfma(av, b1, y1);
          }
          sb();
        }
      }
      STAMP(9);
      store_out(a.out, y0, y1, 128 * tt + 32 * w, 64 * ct, l);
      if (tt == 15 && ct == 0 && w == 0 && l == 0) __builtin_amdgcn_raw_buffer_store_b32(E + 1, rw, WS_EPA, 0, AUX_SC1);
      STAMP(10);
    }
  } else {
    u32x4 pq[2][2][2];
    if (w < 4) {
      u32 ax[4];
#pragma unroll
      for (int k = 0; k < 4; ++k) ax[k] = fragrow + (((2 * k + h) ^ fsw) << 4) + wm * 8192;
      const u32 aw = 16384 + (8 * (G >> 1) + tr_r) * 128 + (((4 * wn + 2 * (G & 1) + (tr_c >> 1)) ^ ((tr_r >> 1) << 2)) << 4) + (tr_c & 1) * 8;
      f32x16 q[2], kk[2], v[2];
      bf16x8 px0, px1, pwq, pwk, pwv, wf[2];
#pragma unroll
      for (int i = 0; i < 16; ++i) { q[0][i] = 0.f; q[1][i] = 0.f; kk[0][i] = 0.f; kk[1][i] = 0.f; v[0][i] = 0.f; v[1][i] = 0.f; }
#pragma unroll
      for (int i = 0; i < 8; ++i) { px0[i] = 0; px1[i] = 0; pwq[i] = 0; pwk[i] = 0; pwv[i] = 0; wf[0][i] = 0; wf[1][i] = 0; }
      asm volatile("" :: "s"(E));
      u32 slot = 0;
#pragma unroll 1
      for (int s = 0; s < 8; ++s) {
        bar_l();
        if (s == 0) {
          STAMP(1);
          const u32 pa = B_WPHI + wn * 1024 + (4 * (G >> 1) + tr_r) * 32 + (2 * (G & 1) + (tr_c >> 1)) * 8;
          const u32 sgn = ((l >> 2) & 1) ? 0x80008000u : 0u;
#pragma unroll
          for (int hd = 0; hd < 2; ++hd) {
            u32x4 t = __builtin_bit_cast(u32x4, rdtr(pa + hd * 512, pa + hd * 512 + 256));
            t.x ^= sgn; t.y ^= sgn; t.z ^= sgn; t.w ^= sgn;
            wf[hd] = __builtin_bit_cast(bf16x8, t);
          }
        }
#pragma unroll
        for (int k = 0; k < 4; ++k) {
          const u32 wa = slot + aw + k * 2048;
          bf16x8 x0 = rd128(slot + ax[k]), wk = rdtr(wa + 8192, wa + 8192 + 512), x1 = rd128(slot + ax[k] + 4096);
          bf16x8 wv = rdtr(wa + 16384, wa + 16384 + 512), wq = rdtr(wa, wa + 512);
          sb();
          kk[0] = mfma(pwk, px0, kk[0]); kk[1] = mfma(pwk, px1, kk[1]);
          v[0] = mfma(px0, pwv, v[0]); v[1] = mfma(px1, pwv, v[1]);
          q[0] = mfma(pwq, px0, q[0]); q[1] = mfma(pwq, px1, q[1]);
          sb();
          px0 = x0; px1 = x1; pwq = wq; pwk = wk; pwv = wv;
        }
        slot = slot == 2 * B_SLOT ? 0u : slot + B_SLOT;
      }
      kk[0] = mfma(pwk, px0, kk[0]); kk[1] = mfma(pwk, px1, kk[1]);
      v[0] = mfma(px0, pwv, v[0]); v[1] = mfma(px1, pwv, v[1]);
      q[0] = mfma(pwq, px0, q[0]); q[1] = mfma(pwq, px1, q[1]);
      STAMP(2);
      f32x16 zero;
#pragma unroll
      for (int i = 0; i < 16; ++i) zero[i] = 0.f;
      const u32 stg = w * 8192;
      {
        const u32 wb = stg + l31 * 64, sw = (l31 >> 1) & 3;
#pragma unroll
        for (int mt = 0; mt < 2; ++mt)
#pragma unroll
          for (int hd = 0; hd < 2; ++hd) {
            const f32x16 p = mfma(wf[hd], pack8(kk[mt], 8 * hd), zero);
            u32x4 o0, o1;
            softmax16(p, o0, o1);
            *(LDS3 u32x4*)(wb + hd * 4096 + mt * 2048 + (((2 * h) ^ sw) << 4)) = o0;
            *(LDS3 u32x4*)(wb + hd * 4096 + mt * 2048 + (((2 * h + 1) ^ sw) << 4)) = o1;
          }
      }
      {
        const u32 ra = stg + (4 * (G >> 1) + tr_r) * 64 + (((2 * (G & 1) + (tr_c >> 1)) ^ (2 * (G >> 1) + (tr_r >> 1))) << 4) + (tr_c & 1) * 8;
        f32x16 kv[2] = {zero, zero};
        const bool lo = l31 < 16;
#pragma unroll
        for (int mt = 0; mt < 2; ++mt)
#pragma unroll
          for (int k2 = 0; k2 < 2; ++k2) {
            const u32x4 vb = __builtin_bit_cast(u32x4, pack8(v[mt], 8 * k2));
            const u32 one = 0x3F803F80u;
            const u32x4 v0 = {lo ? vb.x : one, lo ? vb.y : one, lo ? vb.z : one, lo ? vb.w : one};
            const u32x4 v1 = {lo ? one : vb.x, lo ? one : vb.y, lo ? one : vb.z, lo ? one : vb.w};
            const u32 o = ra + mt * 2048 + k2 * 1024;
            kv[0] = mfma(rdtr(o, o + 512), __builtin_bit_cast(bf16x8, v0), kv[0]);
            kv[1] = mfma(rdtr(o + 4096, o + 4096 + 512), __builtin_bit_cast(bf16x8, v1), kv[1]);
          }
#pragma unroll
        for (int hd = 0; hd < 2; ++hd) {
          const bool own = (l31 >> 4) == hd;
          if (own || (l31 & 15) == 0) {
            const u32 pa = stg + hd * 2448 + (own ? (l31 & 15) : 16) * 144 + h * 16;
#pragma unroll
            for (int qq = 0; qq < 4; ++qq)
              *(LDS3 f32x4*)(pa + qq * 32) = f32x4{kv[hd][4 * qq], kv[hd][4 * qq + 1], kv[hd][4 * qq + 2], kv[hd][4 * qq + 3]};
          }
        }
      }
      STAMP(3);
      bar_l();
      {
        const int row = tid >> 4, fp = tid & 15, h4 = row >= 17, dc = row - 17 * h4;
        const u32 ad = h4 * 2448 + dc * 144 + fp * 8;
        const f32x2 t = *(const LDS3 f32x2*)ad + *(const LDS3 f32x2*)(ad + 16384);
        __builtin_amdgcn_raw_buffer_store_b128(gran2(t.x, t.y, thi, tlo), rw,
            WS_G1 + ((ct * 16 + 4 * h4 + (fp >> 2)) * 16 + tt) * 1088 + dc * 64 + (fp & 3) * 16, 0, AUX_SC1);
      }
      STAMP(4);
#pragma unroll
      for (int mt = 0; mt < 2; ++mt)
#pragma unroll
        for (int hd = 0; hd < 2; ++hd) {
          const f32x16 p = mfma(wf[hd], pack8(q[mt], 8 * hd), zero);
          softmax16(p, pq[mt][hd][0], pq[mt][hd][1]);
        }
      asm volatile("" : "+v"(pq[0][0][0]), "+v"(pq[0][0][1]), "+v"(pq[0][1][0]), "+v"(pq[0][1][1]), "+v"(pq[1][0][0]), "+v"(pq[1][0][1]), "+v"(pq[1][1][0]), "+v"(pq[1][1][1]));
      STAMP(5);
    } else {
      bar();
      const int i = tid - 256;
#pragma unroll
      for (int m = 0; m < 4; ++m) {
        const int pi = 256 + i + m * 256;
        if (pi < 1088) {
          const int row = pi >> 4, fp = pi & 15, h4 = (row * 241) >> 12, dc = row - 17 * h4;
          const u32 ad = (h4 >> 1) * 8192 + (h4 & 1) * 2448 + dc * 144 + fp * 8;
          const f32x2 t = *(const LDS3 f32x2*)ad + *(const LDS3 f32x2*)(ad + 16384);
          __builtin_amdgcn_raw_buffer_store_b128(gran2(t.x, t.y, thi, tlo), rw,
              WS_G1 + ((ct * 16 + 4 * h4 + (fp >> 2)) * 16 + tt) * 1088 + dc * 64 + (fp & 3) * 16, 0, AUX_SC1);
        }
      }
      STAMP(2);
      if (w < 7) {
        const bool act = i < 136;
        const int pp = act ? (i >> 1) : 0;
        const u32 off = WS_G1 + ((ct * 16 + tt) * 16 + 8 * (i & 1)) * 1088 + pp * 16;
        u32x4 gq[8];
        for (int it = 0; it < SPIN_MAX; ++it) {
          fence();
          u32 bad = 0;
#pragma unroll
          for (int m = 0; m < 8; ++m) gq[m] = __builtin_amdgcn_raw_buffer_load_b128(rw, off + m * 1088, 0, AUX_SC1);
#pragma unroll
          for (int m = 0; m < 8; ++m) bad |= granbad(gq[m], thi, tw);
          if (__builtin_amdgcn_ballot_w64(bad != 0) == 0) break;
        }
        float sa = 0.f, sb_ = 0.f;
#pragma unroll
        for (int m = 0; m < 8; ++m) { sa += granval(gq[m].x); sb_ += granval(gq[m].z); }
        sa += __uint_as_float(__builtin_amdgcn_mov_dpp(__float_as_uint(sa), 0xB1, 0xf, 0xf, true));
        sb_ += __uint_as_float(__builtin_amdgcn_mov_dpp(__float_as_uint(sb_), 0xB1, 0xf, 0xf, true));
        if (act && !(i & 1))
          __builtin_amdgcn_raw_buffer_store_b128(gran2(sa, sb_, thi, tlo), rw,
              WS_G2 + ct * 17408 + (((tt >> 2) * 17 + (pp >> 2)) * 32 + 8 * (tt & 3) + 2 * (pp & 3)) * 8, 0, AUX_SC1);
      }
      STAMP(3);
      {
        u32x4 gq[5];
        const u32 off = WS_G2 + ct * 17408 + i * 16;
        for (int it = 0; it < SPIN_MAX; ++it) {
          fence();
          u32 bad = 0;
#pragma unroll
          for (int m = 0; m < 4; ++m) gq[m] = __builtin_amdgcn_raw_buffer_load_b128(rw, off + m * 4096, 0, AUX_SC1);
          if (w == 7) gq[4] = __builtin_amdgcn_raw_buffer_load_b128(rw, off + 16384 - 192 * 16, 0, AUX_SC1);
          else gq[4] = u32x4{tw, thi, tw, thi};
#pragma unroll
          for (int m = 0; m < 5; ++m) bad |= granbad(gq[m], thi, tw);
          if (__builtin_amdgcn_ballot_w64(bad != 0) == 0) break;
        }
#pragma unroll
        for (int m = 0; m < 4; ++m) *(LDS3 u32*)(B_KVT + (i + m * 256) * 4) = cvtpk(granval(gq[m].x), granval(gq[m].z));
        if (w == 7) *(LDS3 u32*)(B_KVT + (i + 1024 - 192) * 4) = cvtpk(granval(gq[4].x), granval(gq[4].z));
      }
      STAMP(4);
    }
    bar_l();
    if (w < 4) {
      STAMP(6);
      {
        const u32 rowd = l31 < 16 ? (8 * ((l31 >> 2) & 1) + 4 * (l31 >> 3) + (l31 & 3)) : 16;
        const u32 ka = B_KVT + (2 * wn * 17 + rowd) * 64 + h * 32;
        f32x16 zero;
#pragma unroll
        for (int i = 0; i < 16; ++i) zero[i] = 0.f;
        const u32 ooff = WS_OB + ((tt * 4 + 2 * wm) * 32 + 4 * ct + 2 * wn) * 1024 + l * 16;
#pragma unroll
        for (int hd = 0; hd < 2; ++hd) {
          const bf16x8 k0 = rd128(ka + hd * 1088), k1 = rd128(ka + hd * 1088 + 16);
#pragma unroll
          for (int mt = 0; mt < 2; ++mt) {
            f32x16 o = mfma(k0, __builtin_bit_cast(bf16x8, pq[mt][hd][0]), zero);
            o = mfma(k1, __builtin_bit_cast(bf16x8, pq[mt][hd][1]), o);
            const float r = __builtin_amdgcn_rcpf(o[8] + 1e-6f);
            const u32x4 ov = {cvtpk(o[0] * r, o[1] * r), cvtpk(o[2] * r, o[3] * r), cvtpk(o[4] * r, o[5] * r), cvtpk(o[6] * r, o[7] * r)};
            __builtin_amdgcn_raw_buffer_store_b128(ov, rw, ooff + (mt * 32 + hd) * 1024, 0, AUX_SC1);
          }
        }
      }
      WAITV(0);
      if (l == 0) __builtin_amdgcn_raw_buffer_store_b64(u32x2{tl32, thi}, rw, flagoff + wm * 128 + (ct * 2 + wn) * 8, 0, AUX_SC1);
      STAMP(7);
      wait_flags(rw, flagoff + wm * 128, l, thi, tl32);
      STAMP(8);
      f32x16 y0, y1;
#pragma unroll
      for (int i = 0; i < 16; ++i) { y0[i] = 0.f; y1[i] = 0.f; }
      {
        const u32 oa = WS_OB + (tt * 4 + w) * 32768 + l * 16;
        const u32 b0a = B_WO + (8 * (G >> 1) + tr_r) * 128 + (((2 * (G & 1) + (tr_c >> 1)) ^ ((tr_r >> 1) << 2)) << 4) + (tr_c & 1) * 8;
        const u32 b1a = b0a ^ 64;
        u32x4 af[8][4];
#pragma unroll
        for (int i = 0; i < 8; ++i) {
          const u32 so = ((ct + i) & 7) * 4096;
#pragma unroll
          for (int k = 0; k < 4; ++k) af[i][k] = __builtin_amdgcn_raw_buffer_load_b128(rw, oa + k * 1024, so, AUX_SC1);
        }
        sb();
#pragma unroll
        for (int i = 0; i < 8; ++i) {
#pragma unroll
          for (int k = 0; k < 4; ++k) {
            const u32 o = i * 8192 + k * 2048;
            const bf16x8 b0 = rdtr(b0a + o, b0a + o + 512), b1 = rdtr(b1a + o, b1a + o + 512);
            const bf16x8 av = __builtin_bit_cast(bf16x8, af[i][k]);
            y0 = mfma(av, b0, y0); y1 = mfma(av, b1, y1);
          }
        }
      }
      STAMP(9);
      store_out(a.out, y0, y1, 128 * tt + 32 * w, 512 + 64 * ct, l);
      if (tt == 15 && ct == 0 && w == 0 && l == 0) __builtin_amdgcn_raw_buffer_store_b32(E + 1, rw, WS_EPB, 0, AUX_SC1);
      STAMP(10);
    }
  }
  if (w < 4) {
    WAITV(0);
    STAMP(11);
    const u64 tgt = tp0 + 4000 + ((PCH == 2 || PCH == (int)isA) ? tp1 - tp0 : 0);
    while (__builtin_amdgcn_s_memrealtime() < tgt) __builtin_amdgcn_s_sleep(2);
  }
}

#define BENCH_LAUNCH(first, last, k, grid, block, shm, st, ...) hipLaunchKernelGGL(k, dim3(grid), dim3(block), shm, st, __VA_ARGS__)

extern "C" void kernel_launch(void* const* d_in, const int* in_sizes, int n_in, void* d_out, int out_size, void* d_ws, size_t ws_size, hipStream_t stream) {
  Args a;
  for (int i = 0; i < 13; ++i) a.in[i] = (const u16*)d_in[i];
  a.out = (float*)d_out;
  a.ws = (unsigned char*)d_ws;
  (void)hipFuncSetAttribute((const void*)fused_k, hipFuncAttributeMaxDynamicSharedMemorySize, 163840);
  BENCH_LAUNCH(true, true, fused_k, 256, 512, 163840, stream, a);
}
```

```cpp
#define PROBE 0
#define PCH 2
#define OUT_AUX 16
#define QK1REC
#define QK1NOSPIN
#define QPOST
#include <hip/hip_runtime.h>
#include <hip/hip_bf16.h>

#pragma clang diagnostic ignored "-Winline-asm"
typedef unsigned short u16;
typedef unsigned int u32;
typedef unsigned long long u64;
typedef __bf16 bf16x8 __attribute__((ext_vector_type(8)));
typedef __bf16 bf16x2 __attribute__((ext_vector_type(2)));
typedef short s16x4 __attribute__((ext_vector_type(4)));
typedef float f32x2 __attribute__((ext_vector_type(2)));
typedef float f32x4 __attribute__((ext_vector_type(4)));
typedef float f32x16 __attribute__((ext_vector_type(16)));
typedef u32 u32x2 __attribute__((ext_vector_type(2)));
typedef u32 u32x4 __attribute__((ext_vector_type(4)));
#define LDS3 __attribute__((address_space(3)))
#define DI __device__ __forceinline__

#define WS_EPA   0u
#define WS_EPB   256u
#define WS_FLAG  4096u
#define WS_GA    65536u
#define WS_G2    (WS_GA + 131072u)
#define WS_G1    524288u
#define WS_HA    4194304u
#define WS_OB    8388608u
#define WS_BYTES 16777216u
#define WS_DIAG  16777216u
#define AUX_SC1 16
#define SPIN_MAX (1 << 18)

struct Args { const u16* in[13]; float* out; unsigned char* ws; };

#define STAMP(i) do { if ((i) == 0) tp0 = __builtin_amdgcn_s_memrealtime(); if ((i) == PROBE) tp1 = __builtin_amdgcn_s_memrealtime(); } while (0)

DI float bf2f(u32 v) { return __uint_as_float(v << 16); }
DI u32 cvtpk(float a, float b) { f32x2 t = {a, b}; return __builtin_bit_cast(u32, __builtin_convertvector(t, bf16x2)); }
DI u32x4 srd(const void* p, u32 bytes) { u64 a = (u64)p; u32x4 r = {(u32)a, (u32)(a >> 32) & 0xffffu, bytes, 0x00020000u}; return r; }
DI void glds(u32 ldsdst, u32 voff, u32x4 rs, u32 soff) {
  asm volatile("s_mov_b32 m0, %0\n\ts_nop 0\n\tbuffer_load_dwordx4 %1, %2, %3 offen lds" :: "s"(ldsdst), "v"(voff), "s"(rs), "s"(soff) : "m0", "memory");
}
#define WAITV(n) asm volatile("s_waitcnt vmcnt(%0)" :: "n"(n) : "memory")
DI void bar_l() { asm volatile("s_waitcnt lgkmcnt(0)\n\ts_barrier" ::: "memory"); }
DI void bar() { asm volatile("s_barrier" ::: "memory"); }
DI void fence() { asm volatile("" ::: "memory"); }
DI void sb() { __builtin_amdgcn_sched_barrier(0); }
DI bf16x8 rd128(u32 a) { return *(const LDS3 bf16x8*)a; }
DI bf16x8 rdtr(u32 a0, u32 a1) {
  s16x4 x = __builtin_amdgcn_ds_read_tr16_b64_v4i16((LDS3 s16x4*)a0);
  s16x4 y = __builtin_amdgcn_ds_read_tr16_b64_v4i16((LDS3 s16x4*)a1);
  return __builtin_bit_cast(bf16x8, __builtin_shufflevector(x, y, 0, 1, 2, 3, 4, 5, 6, 7));
}
DI f32x16 mfma(bf16x8 a, bf16x8 b, f32x16 c) { return __builtin_amdgcn_mfma_f32_32x32x16_bf16(a, b, c, 0, 0, 0); }
DI f32x2 fma2(f32x2 a, f32x2 b, f32x2 c) { return __builtin_elementwise_fma(a, b, c); }
DI f32x2 bc(float a) { f32x2 r = {a, a}; return r; }
struct cpx { float r, i; };
DI cpx cmul(cpx a, cpx b) { cpx c; c.r = a.r * b.r - a.i * b.i; c.i = a.r * b.i + a.i * b.r; return c; }
DI cpx cfma(cpx a, cpx b, cpx c) { cpx d; d.r = fmaf(a.r, b.r, fmaf(-a.i, b.i, c.r)); d.i = fmaf(a.r, b.i, fmaf(a.i, b.r, c.i)); return d; }
DI cpx csq(cpx a) { return cmul(a, a); }
DI u32x4 gran2(float a, float b, u32 thi, u32 tlo) {
  u32x4 g = {__builtin_amdgcn_alignbit(tlo, __float_as_uint(a), 8), thi, __builtin_amdgcn_alignbit(tlo, __float_as_uint(b), 8), thi};
  return g;
}
DI u32 granbad(u32x4 g, u32 thi, u32 tw) { return ((g.y ^ thi) | (g.w ^ thi)) | (((g.x ^ tw) | (g.z ^ tw)) >> 24); }
DI float granval(u32 lo) { return __uint_as_float(lo << 8); }
DI bf16x8 pack8(const f32x16& v, int o) {
  u32x4 r = {cvtpk(v[o], v[o + 1]), cvtpk(v[o + 2], v[o + 3]), cvtpk(v[o + 4], v[o + 5]), cvtpk(v[o + 6], v[o + 7])};
  return __builtin_bit_cast(bf16x8, r);
}
DI void lam_params(const Args& a, int ch, cpx& lam, cpx& coef) {
  float lr = bf2f(a.in[1][ch]), li = bf2f(a.in[2][ch]);
  float dt = bf2f(cvtpk(expf(bf2f(a.in[7][ch])), 0.f) & 0xffffu);
  float m = expf(dt * lr), sn, cs;
  sincosf(dt * li, &sn, &cs);
  lam.r = m * cs; lam.i = m * sn;
  float nr = lam.r - 1.f, ni = lam.i, den = lr * lr + li * li;
  coef.r = (nr * lr + ni * li) / den; coef.i = (ni * lr - nr * li) / den;
}
DI void wait_flags(__amdgpu_buffer_rsrc_t rw, u32 off, int l, u32 thi, u32 tl32) {
  for (int it = 0; it < SPIN_MAX; ++it) {
    fence();
    u32x2 f = __builtin_amdgcn_raw_buffer_load_b64(rw, off + (l & 15) * 8, 0, AUX_SC1);
    if (__builtin_amdgcn_ballot_w64((f.x != tl32) | (f.y != thi)) == 0) break;
    __builtin_amdgcn_s_sleep(1);
  }
  fence();
}
DI void softmax16(const f32x16& p, u32x4& o0, u32x4& o1) {
  float m = fmaxf(p[0], p[1]);
#pragma unroll
  for (int i = 2; i < 16; i += 2) m = fmaxf(fmaxf(m, p[i]), p[i + 1]);
  const float L2E = 1.44269504088896340736f;
  f32x2 nm = bc(-m * L2E), e[8];
#pragma unroll
  for (int i = 0; i < 8; ++i) {
    f32x2 t = {p[2 * i], p[2 * i + 1]};
    t = fma2(t, bc(L2E), nm);
    e[i].x = __builtin_amdgcn_exp2f(t.x); e[i].y = __builtin_amdgcn_exp2f(t.y);
  }
  f32x2 s = ((e[0] + e[1]) + (e[2] + e[3])) + ((e[4] + e[5]) + (e[6] + e[7]));
  f32x2 r = bc(__builtin_amdgcn_rcpf(s.x + s.y));
  u32 w[8];
#pragma unroll
  for (int i = 0; i < 8; ++i) { f32x2 t = e[i] * r; w[i] = cvtpk(t.x, t.y); }
  o0 = u32x4{w[0], w[1], w[2], w[3]}; o1 = u32x4{w[4], w[5], w[6], w[7]};
}
DI void store_out(float* out, const f32x16& a0, const f32x16& a1, int row0, int col0, int l) {
  const __amdgpu_buffer_rsrc_t ro = __builtin_amdgcn_make_buffer_rsrc((void*)out, 0, 8388608, 0x00020000);
  const u32 p = ((row0 + 4 * (l >> 5)) * 1024 + col0 + (l & 31)) * 4;
#pragma unroll
  for (int r = 0; r < 16; ++r) {
    __builtin_amdgcn_raw_buffer_store_b32(__float_as_uint(a0[r]), ro, p, (8 * (r >> 2) + (r & 3)) * 4096, OUT_AUX);
    __builtin_amdgcn_raw_buffer_store_b32(__float_as_uint(a1[r]), ro, p + 128, (8 * (r >> 2) + (r & 3)) * 4096, OUT_AUX);
  }
}

#define A_SLOT 32768u
#define A_STG  0u
#define A_SEG  16384u
#define A_PART 20480u
#define A_LAM  98304u
#define B_SLOT 40960u
#define B_KVT  81920u
#define B_WO   98304u
#define B_WPHI 122880u

__global__ __launch_bounds__(512, 2) void fused_k(Args a) {
  extern __shared__ __attribute__((aligned(16))) char smem_[];
  const int b = blockIdx.x, g = b & 7, jb = b >> 3;
  const int tid = threadIdx.x, l = tid & 63;
  const int w = __builtin_amdgcn_readfirstlane(tid >> 6);
  const bool isA = g >= 4;
  const int tt = 4 * (g & 3) + (jb >> 3), ct = jb & 7;
  const int s0 = ((ct ^ (jb >> 3)) & 1) * 4;
  const int h = l >> 5, l31 = l & 31;
  u64 tp0 = 0, tp1 = 0;
  STAMP(0);
  const u32 E = *(const __attribute__((address_space(4))) u32*)(a.ws + (isA ? WS_EPA : WS_EPB));
  const __amdgpu_buffer_rsrc_t rw = __builtin_amdgcn_make_buffer_rsrc((void*)a.ws, 0, (int)WS_BYTES, 0x00020000);
  const u32 flagoff = WS_FLAG + (isA ? 0u : 4096u) + tt * 256;

  if (w >= 4) {
    const int w4 = w - 4;
    const u32 lr8 = l >> 3;
    if (isA) {
      const u32x4 rs = srd(w4 < 2 ? a.in[0] : (w4 == 2 ? a.in[3] : a.in[4]), w4 < 2 ? 2097152u : 524288u);
      const u32 lp = w4 < 2 ? (32 * (lr8 >> 2) + 16 * (lr8 & 1) + ((lr8 >> 1) & 1)) : lr8;
      const u32 v0 = lp * 1024 + (((l & 7) ^ (l >> 4)) << 4), v1 = v0 ^ 64;
      const u32 rb = w4 < 2 ? (128 * tt + 64 * w4) : 64 * ct;
      const u32 pa = w4 < 2 ? 2048u : 8192u, pb = w4 < 2 ? 8192u : 32768u;
      auto issue = [&](int i) {
        const u32 so = rb * 1024 + (((s0 + i) & 7) << 7);
        const u32 dst = (i % 3) * A_SLOT + w4 * 8192;
#pragma unroll
        for (int p = 0; p < 8; ++p) glds(dst + p * 1024, (p & 1) ? v1 : v0, rs, so + (p & 3) * pa + (p >> 2) * pb);
      };
      issue(0); issue(1);
      const u32x4 rc = srd((w4 & 1) ? a.in[6] : a.in[5], 524288u);
      const u32 vc0 = lr8 * 1024 + (((l & 7) ^ (l >> 4)) << 4), vc1 = vc0 ^ 64;
      auto issueC = [&](int i) {
        const u32 so = 64 * ct * 1024 + (((ct + i) & 7) << 7);
        const u32 dst = (i < 4 ? 98304u + i * 16384u : (i < 6 ? 65536u + (i - 4) * 16384u : 32768u + (i - 6) * 16384u)) + (w4 & 1) * 8192;
#pragma unroll
        for (int p = 0; p < 8; ++p) glds(dst + p * 1024, (p & 1) ? vc1 : vc0, rc, so + p * 8192);
      };
      asm volatile("" :: "s"(E));
#pragma unroll 1
      for (int s = 0; s < 6; ++s) { WAITV(8); bar(); issue(s + 2); }
      const f32x2 lv = *(const LDS3 f32x2*)(A_LAM + l * 8);
      cpx l16 = csq(csq(csq(csq(cpx{lv.x, lv.y}))));
      cpx l128 = csq(csq(csq(l16)));
      cpx l256 = csq(l128), l512 = csq(l256);
      asm volatile("" : "+v"(l16.r), "+v"(l16.i), "+v"(l128.r), "+v"(l128.i), "+v"(l256.r), "+v"(l256.i), "+v"(l512.r), "+v"(l512.i));
      WAITV(8); bar(); issueC(w4 >> 1);
      WAITV(8); bar(); issueC(2 + (w4 >> 1));
      const u64 T = (u64)E * 0x9E3779B97F4A7C15ull + 0x632BE59BD9B4E019ull;
      const u32 thi = (u32)(T >> 32), tlo = (u32)(T >> 24) & 0xffu, tw = tlo << 24;
      STAMP(1);
      bar();
      if (w4 == 3) {
        cpx t = {0.f, 0.f};
#pragma unroll
        for (int s = 0; s < 8; ++s) { f32x2 e = *(const LDS3 f32x2*)(A_SEG + s * 512 + l * 8); t = cfma(l16, t, cpx{e.x, e.y}); }
        __builtin_amdgcn_raw_buffer_store_b128(gran2(t.r, t.i, thi, tlo), rw, WS_GA + (tt * 512 + 64 * ct + l) * 16, 0, AUX_SC1);
      }
      STAMP(2);
      cpx P = {0.f, 0.f};
      if (w4 < tt) {
        const int jl = w4 + ((tt - 1 - w4) & ~3), ex = tt - 1 - jl;
        u32x4 gq[4];
        for (int it = 0; it < SPIN_MAX; ++it) {
          fence();
          u32 bad = 0;
#pragma unroll
          for (int m = 0; m < 4; ++m) {
            const int j = jl - 4 * (3 - m);
            if (j >= 0) gq[m] = __builtin_amdgcn_raw_buffer_load_b128(rw, WS_GA + (64 * ct + l) * 16, j * 8192, AUX_SC1);
            else gq[m] = u32x4{tw, thi, tw, thi};
          }
#pragma unroll
          for (int m = 0; m < 4; ++m) bad |= granbad(gq[m], thi, tw);
          if (__builtin_amdgcn_ballot_w64(bad != 0) == 0) break;
        }
#pragma unroll
        for (int m = 0; m < 4; ++m) P = cfma(l512, P, cpx{granval(gq[m].x), granval(gq[m].z)});
        cpx f = {1.f, 0.f};
        if (ex & 1) f = l128;
        if (ex & 2) f = cmul(f, l256);
        P = cmul(P, f);
      }
      *(LDS3 f32x2*)(A_PART + l * 32 + w4 * 8) = f32x2{P.r, P.i};
      STAMP(3);
      WAITV(0);
      bar_l();
      issueC(4 + (w4 >> 1)); issueC(6 + (w4 >> 1));
      WAITV(0);
      bar();
      STAMP(4);
    } else {
      const u32x4 rx = srd(a.in[9], 2097152u), rq = srd(a.in[10], 1572864u);
      const u32 vx0 = lr8 * 1024 + (((l & 7) ^ (l >> 4)) << 4), vx1 = vx0 ^ 64;
      const u32 vw = lr8 * 3072 + (((l & 7) ^ (((l >> 4) & 1) << 2)) << 4);
      auto issue = [&](int i) {
        const u32 ks = (s0 + i) & 7;
        const u32 dst = (i % 3) * B_SLOT;
        const u32 sx = (128 * tt + 32 * w4) * 1024 + (ks << 7);
#pragma unroll
        for (int p = 0; p < 4; ++p) glds(dst + (4 * w4 + p) * 1024, (p & 1) ? vx1 : vx0, rx, sx + p * 8192);
        const u32 sw = ks * 64 * 3072 + 128 * ct;
#pragma unroll
        for (int p = 0; p < 6; ++p) { const u32 pc = 6 * w4 + p; glds(dst + 16384 + pc * 1024, vw, rq, sw + (pc & 7) * 24576 + (pc >> 3) * 1024); }
      };
      if (w4 < 2) glds(B_WPHI + w4 * 1024, l * 16, srd(a.in[11], 16384u), ct * 2048 + w4 * 1024);
      issue(0); issue(1);
      const u32x4 ro = srd(a.in[12], 524288u);
      const u32 vo = lr8 * 1024 + (((l & 7) ^ (((l >> 4) & 1) << 2)) << 4);
      auto issueO = [&](int i) {
        const u32 so = (((ct + i) & 7) << 16) + 128 * ct;
#pragma unroll
        for (int p = 0; p < 8; ++p) glds(B_WO + i * 8192 + p * 1024, vo, ro, so + p * 8192);
      };
      asm volatile("" :: "s"(E));
#pragma unroll 1
      for (int s = 0; s < 6; ++s) { WAITV(10); bar(); issue(s + 2); }
      WAITV(10); bar(); issueO(w4);
      WAITV(8); bar(); issueO(4 + w4);
      STAMP(1);
    }
  }
  const u64 T = (u64)E * 0x9E3779B97F4A7C15ull + 0x632BE59BD9B4E019ull;
  const u32 thi = (u32)(T >> 32), tl32 = (u32)T, tlo = (u32)(T >> 24) & 0xffu, tw = tlo << 24;
  const int wm = (w >> 1) & 1, wn = w & 1;
  const int G = l >> 4, tr_r = (l >> 2) & 3, tr_c = l & 3;
  const u32 fragrow = l31 * 128;
  const u32 fsw = (l31 >> 1) & 7;

  if (isA) {
    if (w < 4) {
      cpx lam, coef;
      lam_params(a, 64 * ct + 32 * wn + l31, lam, coef);
      if (wm == 0 && h == 0) *(LDS3 f32x2*)(A_LAM + (32 * wn + l31) * 8) = f32x2{lam.r, lam.i};
      const float dk0 = bf2f(a.in[8][64 * ct + l31]), dk1 = bf2f(a.in[8][64 * ct + 32 + l31]);
      const cpx l16 = csq(csq(csq(csq(lam))));
      u32 ax[4], aw[4];
#pragma unroll
      for (int k = 0; k < 4; ++k) {
        const u32 t = fragrow + (((2 * k + h) ^ fsw) << 4);
        ax[k] = t + wm * 8192; aw[k] = t + 16384 + wn * 4096;
      }
      const u32 rho = 64 * (w >> 1) + 16 * (G >> 1) + 8 * (tr_r >> 1) + 4 * (w & 1) + 2 * (tr_r & 1);
      const u32 xs0 = rho * 128 + (((2 * (G & 1) + (tr_c >> 1)) ^ ((rho >> 1) & 7)) << 4) + (tr_c & 1) * 8;
      f32x16 aR[2], aI[2];
      bf16x8 px0, px1, pwr, pwi;
#pragma unroll
      for (int i = 0; i < 16; ++i) { aR[0][i] = 0.f; aR[1][i] = 0.f; aI[0][i] = 0.f; aI[1][i] = 0.f; }
#pragma unroll
      for (int i = 0; i < 8; ++i) { px0[i] = 0; px1[i] = 0; pwr[i] = 0; pwi[i] = 0; }
      s16x4 xk[8];
#pragma unroll
      for (int i = 0; i < 8; ++i) xk[i] = s16x4{0, 0, 0, 0};
      asm volatile("" :: "s"(E));
      u32 slot = 0;
#pragma unroll 1
      for (int s = 0; s < 8; ++s) {
        bar_l();
        if (s == 0) STAMP(1);
#pragma unroll
        for (int k = 0; k < 4; ++k) {
          bf16x8 x0 = rd128(slot + ax[k]), wr = rd128(slot + aw[k]), x1 = rd128(slot + ax[k] + 4096), wi = rd128(slot + aw[k] + 8192);
          sb();
          aR[0] = mfma(px0, pwr, aR[0]); aR[1] = mfma(px1, pwr, aR[1]);
          aI[0] = mfma(px0, pwi, aI[0]); aI[1] = mfma(px1, pwi, aI[1]);
          sb();
          px0 = x0; px1 = x1; pwr = wr; pwi = wi;
        }
        if (((s0 + s) & 7) == ct) {
#pragma unroll
          for (int q = 0; q < 4; ++q) {
            const u32 o = slot + (32 * (q & 1) + (q >> 1)) * 128;
            xk[q] = __builtin_amdgcn_ds_read_tr16_b64_v4i16((LDS3 s16x4*)(o + xs0));
            xk[4 + q] = __builtin_amdgcn_ds_read_tr16_b64_v4i16((LDS3 s16x4*)(o + (xs0 ^ 64)));
          }
        }
        slot = slot == 2 * A_SLOT ? 0u : slot + A_SLOT;
      }
      aR[0] = mfma(px0, pwr, aR[0]); aI[0] = mfma(px0, pwi, aI[0]);
      aR[1] = mfma(px1, pwr, aR[1]); aI[1] = mfma(px1, pwi, aI[1]);
      STAMP(2);
      const f32x2 Lr = bc(lam.r), Li = bc(lam.i), nLi = bc(-lam.i);
      {
        f32x2 hr = {aR[0][0], aR[0][1]}, hi = {aI[0][0], aI[0][1]};
#pragma unroll
        for (int p = 1; p < 16; ++p) {
          const f32x2 ur = {aR[p >> 3][2 * (p & 7)], aR[p >> 3][2 * (p & 7) + 1]}, ui = {aI[p >> 3][2 * (p & 7)], aI[p >> 3][2 * (p & 7) + 1]};
          const f32x2 nr = fma2(Lr, hr, fma2(nLi, hi, ur)), ni = fma2(Lr, hi, fma2(Li, hr, ui));
          hr = nr; hi = ni;
        }
        const u32 sa = A_SEG + (4 * wm + 2 * h) * 512 + (32 * wn + l31) * 8;
        *(LDS3 f32x2*)sa = f32x2{hr.x, hi.x};
        *(LDS3 f32x2*)(sa + 512) = f32x2{hr.y, hi.y};
      }
      bar_l();
      STAMP(3);
      const cpx c2 = {2.f * coef.r, 2.f * coef.i};
      cpx pre0 = {0.f, 0.f}, pre1;
      {
        const int sg = 4 * wm + 2 * h;
        cpx e0 = {0.f, 0.f};
#pragma unroll
        for (int s = 0; s < 7; ++s) {
          f32x2 e = *(const LDS3 f32x2*)(A_SEG + s * 512 + (32 * wn + l31) * 8);
          cpx t = cfma(l16, pre0, cpx{e.x, e.y});
          if (s < sg) pre0 = t;
          if (s == sg) e0 = cpx{e.x, e.y};
        }
        pre1 = cfma(l16, pre0, e0);
      }
      pre0 = cmul(c2, pre0); pre1 = cmul(c2, pre1);
      cpx m0 = c2;
      {
        const cpx l32 = csq(l16), l64 = csq(l32);
        if (h) m0 = cmul(m0, l32);
        if (wm) m0 = cmul(m0, l64);
      }
      const cpx m1 = cmul(m0, l16);
#pragma unroll
      for (int p = 0; p < 16; ++p) {
        const f32x2 ur = {aR[p >> 3][2 * (p & 7)], aR[p >> 3][2 * (p & 7) + 1]}, ui = {aI[p >> 3][2 * (p & 7)], aI[p >> 3][2 * (p & 7) + 1]};
        const f32x2 vr = fma2(bc(c2.r), ur, bc(-c2.i) * ui), vi = fma2(bc(c2.r), ui, bc(c2.i) * ur);
        aR[p >> 3][2 * (p & 7)] = vr.x; aR[p >> 3][2 * (p & 7) + 1] = vr.y;
        aI[p >> 3][2 * (p & 7)] = vi.x; aI[p >> 3][2 * (p & 7) + 1] = vi.y;
      }
      f32x16 y0, y1;
#pragma unroll
      for (int q = 0; q < 4; ++q) {
        const u32x2 t0 = __builtin_bit_cast(u32x2, xk[q]), t1 = __builtin_bit_cast(u32x2, xk[4 + q]);
#pragma unroll
        for (int e = 0; e < 2; ++e) {
          const u32 r0 = cvtpk(bf2f(t0[e] & 0xffffu) * dk0, __uint_as_float(t0[e] & 0xffff0000u) * dk0);
          const u32 r1 = cvtpk(bf2f(t1[e] & 0xffffu) * dk1, __uint_as_float(t1[e] & 0xffff0000u) * dk1);
          y0[4 * q + 2 * e] = __uint_as_float(r0 << 16); y0[4 * q + 2 * e + 1] = __uint_as_float(r0 & 0xffff0000u);
          y1[4 * q + 2 * e] = __uint_as_float(r1 << 16); y1[4 * q + 2 * e + 1] = __uint_as_float(r1 & 0xffff0000u);
        }
      }
      asm volatile("" : "+v"(y0), "+v"(y1), "+v"(aR[0]), "+v"(aR[1]), "+v"(aI[0]), "+v"(aI[1]));
      STAMP(4);
      bar();
      STAMP(5);
      {
        const f32x4 pa = *(const LDS3 f32x4*)(A_PART + (32 * wn + l31) * 32), pb = *(const LDS3 f32x4*)(A_PART + (32 * wn + l31) * 32 + 16);
        const float cr = (pa.x + pa.z) + (pb.x + pb.z), ci = (pa.y + pa.w) + (pb.y + pb.w);
        const f32x2 Mr = {m0.r, m1.r}, Mi = {m0.i, m1.i};
        f32x2 hr = fma2(Mr, bc(cr), fma2(-Mi, bc(ci), f32x2{pre0.r, pre1.r}));
        f32x2 hi = fma2(Mr, bc(ci), fma2(Mi, bc(cr), f32x2{pre0.i, pre1.i}));
#pragma unroll
        for (int p = 0; p < 16; ++p) {
          const f32x2 ur = {aR[p >> 3][2 * (p & 7)], aR[p >> 3][2 * (p & 7) + 1]}, ui = {aI[p >> 3][2 * (p & 7)], aI[p >> 3][2 * (p & 7) + 1]};
          const f32x2 nr = fma2(Lr, hr, fma2(nLi, hi, ur)), ni = fma2(Lr, hi, fma2(Li, hr, ui));
          hr = nr; hi = ni;
          aR[p >> 3][2 * (p & 7)] = hr.x; aR[p >> 3][2 * (p & 7) + 1] = hr.y;
          aI[p >> 3][2 * (p & 7)] = hi.x; aI[p >> 3][2 * (p & 7) + 1] = hi.y;
        }
      }
      {
        const u32 f = (l31 & 1) | ((l31 & 2) << 1) | ((l31 & 4) >> 1);
        const u32 wb = A_STG + w * 4096 + l31 * 128;
        const u32 chr = 8 * (G >> 1) + tr_r;
        const u32 fr = (tr_r & 1) | ((tr_r & 2) << 1);
        const u32 rb0 = A_STG + w * 4096 + chr * 128 + (tr_c & 1) * 8;
        const u32 cq = 2 * (G & 1) + (tr_c >> 1);
        const u32 hoff = WS_HA + ((tt * 4 + 2 * wm) * 64 + ct * 8 + 2 * wn) * 1024 + l * 16;
#pragma unroll
        for (int ri = 0; ri < 2; ++ri) {
          const f32x16* src = ri ? aI : aR;
          const float sg = ri ? -1.f : 1.f;
#pragma unroll
          for (int c = 0; c < 2; ++c)
#pragma unroll
            for (int i = 0; i < 2; ++i) {
              u32x4 v = {cvtpk(sg * src[i][c], sg * src[i][2 + c]), cvtpk(sg * src[i][4 + c], sg * src[i][6 + c]),
                         cvtpk(sg * src[i][8 + c], sg * src[i][10 + c]), cvtpk(sg * src[i][12 + c], sg * src[i][14 + c])};
              *(LDS3 u32x4*)(wb + (((4 * h + 2 * c + i) ^ f) << 4)) = v;
            }
#pragma unroll
          for (int mt = 0; mt < 2; ++mt)
#pragma unroll
            for (int ks = 0; ks < 2; ++ks) {
              const bf16x8 fr8 = rdtr(rb0 + ks * 2048 + (((4 * mt + cq) ^ fr) << 4), rb0 + ks * 2048 + 512 + (((4 * mt + cq) ^ (fr | 2)) << 4));
              __builtin_amdgcn_raw_buffer_store_b128(__builtin_bit_cast(u32x4, fr8), rw, hoff + (mt * 64 + ri * 4 + ks) * 1024, 0, AUX_SC1);
            }
        }
      }
      STAMP(6);
      WAITV(0);
      if (l == 0) __builtin_amdgcn_raw_buffer_store_b64(u32x2{tl32, thi}, rw, flagoff + wm * 128 + (ct * 2 + wn) * 8, 0, AUX_SC1);
      STAMP(7);
      bar();
      wait_flags(rw, flagoff + wm * 128, l, thi, tl32);
      STAMP(8);
      {
        const u32 ha = WS_HA + (tt * 4 + w) * 65536 + l * 16;
        u32 ca[4], cb[4];
#pragma unroll
        for (int k = 0; k < 4; ++k) { const u32 t = fragrow + (((2 * k + h) ^ fsw) << 4); ca[k] = t + 98304; cb[k] = t + 32768; }
        u32x4 af[4][8];
        auto ld = [&](int i) {
          const u32 so = ((ct + i) & 7) * 8192;
#pragma unroll
          for (int k = 0; k < 8; ++k) af[i & 3][k] = __builtin_amdgcn_raw_buffer_load_b128(rw, ha + k * 1024, so, AUX_SC1);
        };
        ld(0); ld(1); ld(2);
        sb();
#pragma unroll
        for (int i = 0; i < 8; ++i) {
          if (i + 3 < 8) ld(i + 3);
          sb();
          const u32 io = i < 4 ? i * 16384 : (i < 6 ? 32768 + (i - 4) * 16384 : (i - 6) * 16384);
#pragma unroll
          for (int k = 0; k < 8; ++k) {
            const u32 ad = (i < 4 ? ca[k & 3] : cb[k & 3]) + io + (k >> 2) * 8192;
            const bf16x8 b0 = rd128(ad), b1 = rd128(ad + 4096);
            const bf16x8 av = __builtin_bit_cast(bf16x8, af[i & 3][k]);
            y0 = mfma(av, b0, y0); y1 = mfma(av, b1, y1);
          }
          sb();
        }
      }
      STAMP(9);
      store_out(a.out, y0, y1, 128 * tt + 32 * w, 64 * ct, l);
      if (tt == 15 && ct == 0 && w == 0 && l == 0) __builtin_amdgcn_raw_buffer_store_b32(E + 1, rw, WS_EPA, 0, AUX_SC1);
      STAMP(10);
    }
  } else {
    u32x4 pq[2][2][2];
    if (w < 4) {
      u32 ax[4];
#pragma unroll
      for (int k = 0; k < 4; ++k) ax[k] = fragrow + (((2 * k + h) ^ fsw) << 4) + wm * 8192;
      const u32 aw = 16384 + (8 * (G >> 1) + tr_r) * 128 + (((4 * wn + 2 * (G & 1) + (tr_c >> 1)) ^ ((tr_r >> 1) << 2)) << 4) + (tr_c & 1) * 8;
      f32x16 q[2], kk[2], v[2];
      bf16x8 px0, px1, pwq, pwk, pwv, wf[2];
#pragma unroll
      for (int i = 0; i < 16; ++i) { q[0][i] = 0.f; q[1][i] = 0.f; kk[0][i] = 0.f; kk[1][i] = 0.f; v[0][i] = 0.f; v[1][i] = 0.f; }
#pragma unroll
      for (int i = 0; i < 8; ++i) { px0[i] = 0; px1[i] = 0; pwq[i] = 0; pwk[i] = 0; pwv[i] = 0; wf[0][i] = 0; wf[1][i] = 0; }
      asm volatile("" :: "s"(E));
      u32 slot = 0;
#pragma unroll 1
      for (int s = 0; s < 8; ++s) {
        bar_l();
        if (s == 0) {
          STAMP(1);
          const u32 pa = B_WPHI + wn * 1024 + (4 * (G >> 1) + tr_r) * 32 + (2 * (G & 1) + (tr_c >> 1)) * 8;
          const u32 sgn = ((l >> 2) & 1) ? 0x80008000u : 0u;
#pragma unroll
          for (int hd = 0; hd < 2; ++hd) {
            u32x4 t = __builtin_bit_cast(u32x4, rdtr(pa + hd * 512, pa + hd * 512 + 256));
            t.x ^= sgn; t.y ^= sgn; t.z ^= sgn; t.w ^= sgn;
            wf[hd] = __builtin_bit_cast(bf16x8, t);
          }
        }
#pragma unroll
        for (int k = 0; k < 4; ++k) {
          const u32 wa = slot + aw + k * 2048;
          bf16x8 x0 = rd128(slot + ax[k]), wk = rdtr(wa + 8192, wa + 8192 + 512), x1 = rd128(slot + ax[k] + 4096);
          bf16x8 wv = rdtr(wa + 16384, wa + 16384 + 512), wq = rdtr(wa, wa + 512);
          sb();
          kk[0] = mfma(pwk, px0, kk[0]); kk[1] = mfma(pwk, px1, kk[1]);
          v[0] = mfma(px0, pwv, v[0]); v[1] = mfma(px1, pwv, v[1]);
          q[0] = mfma(pwq, px0, q[0]); q[1] = mfma(pwq, px1, q[1]);
          sb();
          px0 = x0; px1 = x1; pwq = wq; pwk = wk; pwv = wv;
        }
        slot = slot == 2 * B_SLOT ? 0u : slot + B_SLOT;
      }
      kk[0] = mfma(pwk, px0, kk[0]); kk[1] = mfma(pwk, px1, kk[1]);
      v[0] = mfma(px0, pwv, v[0]); v[1] = mfma(px1, pwv, v[1]);
      q[0] = mfma(pwq, px0, q[0]); q[1] = mfma(pwq, px1, q[1]);
      STAMP(2);
      f32x16 zero;
#pragma unroll
      for (int i = 0; i < 16; ++i) zero[i] = 0.f;
      const u32 stg = w * 8192;
      {
        const u32 wb = stg + l31 * 64, sw = (l31 >> 1) & 3;
#pragma unroll
        for (int mt = 0; mt < 2; ++mt)
#pragma unroll
          for (int hd = 0; hd < 2; ++hd) {
            const f32x16 p = mfma(wf[hd], pack8(kk[mt], 8 * hd), zero);
            u32x4 o0, o1;
            softmax16(p, o0, o1);
            *(LDS3 u32x4*)(wb + hd * 4096 + mt * 2048 + (((2 * h) ^ sw) << 4)) = o0;
            *(LDS3 u32x4*)(wb + hd * 4096 + mt * 2048 + (((2 * h + 1) ^ sw) << 4)) = o1;
          }
      }
      {
        const u32 ra = stg + (4 * (G >> 1) + tr_r) * 64 + (((2 * (G & 1) + (tr_c >> 1)) ^ (2 * (G >> 1) + (tr_r >> 1))) << 4) + (tr_c & 1) * 8;
        f32x16 kv[2] = {zero, zero};
        const bool lo = l31 < 16;
#pragma unroll
        for (int mt = 0; mt < 2; ++mt)
#pragma unroll
          for (int k2 = 0; k2 < 2; ++k2) {
            const u32x4 vb = __builtin_bit_cast(u32x4, pack8(v[mt], 8 * k2));
            const u32 one = 0x3F803F80u;
            const u32x4 v0 = {lo ? vb.x : one, lo ? vb.y : one, lo ? vb.z : one, lo ? vb.w : one};
            const u32x4 v1 = {lo ? one : vb.x, lo ? one : vb.y, lo ? one : vb.z, lo ? one : vb.w};
            const u32 o = ra + mt * 2048 + k2 * 1024;
            kv[0] = mfma(rdtr(o, o + 512), __builtin_bit_cast(bf16x8, v0), kv[0]);
            kv[1] = mfma(rdtr(o + 4096, o + 4096 + 512), __builtin_bit_cast(bf16x8, v1), kv[1]);
          }
#pragma unroll
        for (int hd = 0; hd < 2; ++hd) {
          const bool own = (l31 >> 4) == hd;
          if (own || (l31 & 15) == 0) {
            const u32 pa = stg + hd * 2448 + (own ? (l31 & 15) : 16) * 144 + h * 16;
#pragma unroll
            for (int qq = 0; qq < 4; ++qq)
              *(LDS3 f32x4*)(pa + qq * 32) = f32x4{kv[hd][4 * qq], kv[hd][4 * qq + 1], kv[hd][4 * qq + 2], kv[hd][4 * qq + 3]};
          }
        }
      }
      STAMP(3);
      bar_l();
      {
        const int row = tid >> 4, fp = tid & 15, h4 = row >= 17, dc = row - 17 * h4;
        const u32 ad = h4 * 2448 + dc * 144 + fp * 8;
        const f32x2 t = *(const LDS3 f32x2*)ad + *(const LDS3 f32x2*)(ad + 16384);
        __builtin_amdgcn_raw_buffer_store_b128(gran2(t.x, t.y, thi, tlo), rw,
            WS_G1 + ((ct * 16 + 4 * h4 + (fp >> 2)) * 16 + tt) * 1088 + dc * 64 + (fp & 3) * 16, 0, AUX_SC1);
      }
      STAMP(4);
#pragma unroll
      for (int mt = 0; mt < 2; ++mt)
#pragma unroll
        for (int hd = 0; hd < 2; ++hd) {
          const f32x16 p = mfma(wf[hd], pack8(q[mt], 8 * hd), zero);
          softmax16(p, pq[mt][hd][0], pq[mt][hd][1]);
        }
      asm volatile("" : "+v"(pq[0][0][0]), "+v"(pq[0][0][1]), "+v"(pq[0][1][0]), "+v"(pq[0][1][1]), "+v"(pq[1][0][0]), "+v"(pq[1][0][1]), "+v"(pq[1][1][0]), "+v"(pq[1][1][1]));
      STAMP(5);
    } else {
      bar();
      const int i = tid - 256;
#pragma unroll
      for (int m = 0; m < 4; ++m) {
        const int pi = 256 + i + m * 256;
        if (pi < 1088) {
          const int row = pi >> 4, fp = pi & 15, h4 = (row * 241) >> 12, dc = row - 17 * h4;
          const u32 ad = (h4 >> 1) * 8192 + (h4 & 1) * 2448 + dc * 144 + fp * 8;
          const f32x2 t = *(const LDS3 f32x2*)ad + *(const LDS3 f32x2*)(ad + 16384);
          __builtin_amdgcn_raw_buffer_store_b128(gran2(t.x, t.y, thi, tlo), rw,
              WS_G1 + ((ct * 16 + 4 * h4 + (fp >> 2)) * 16 + tt) * 1088 + dc * 64 + (fp & 3) * 16, 0, AUX_SC1);
        }
      }
      STAMP(2);
      if (w < 7) {
        const bool act = i < 136;
        const int pp = act ? (i >> 1) : 0;
        const u32 off = WS_G1 + ((ct * 16 + tt) * 16 + 8 * (i & 1)) * 1088 + pp * 16;
        u32x4 gq[8];
        for (int it = 0; it < SPIN_MAX; ++it) {
          fence();
          u32 bad = 0;
#pragma unroll
          for (int m = 0; m < 8; ++m) gq[m] = __builtin_amdgcn_raw_buffer_load_b128(rw, off + m * 1088, 0, AUX_SC1);
#pragma unroll
          for (int m = 0; m < 8; ++m) bad |= granbad(gq[m], thi, tw);
          if (__builtin_amdgcn_ballot_w64(bad != 0) == 0) break;
        }
        float sa = 0.f, sb_ = 0.f;
#pragma unroll
        for (int m = 0; m < 8; ++m) { sa += granval(gq[m].x); sb_ += granval(gq[m].z); }
        sa += __uint_as_float(__builtin_amdgcn_mov_dpp(__float_as_uint(sa), 0xB1, 0xf, 0xf, true));
        sb_ += __uint_as_float(__builtin_amdgcn_mov_dpp(__float_as_uint(sb_), 0xB1, 0xf, 0xf, true));
        if (act && !(i & 1))
          __builtin_amdgcn_raw_buffer_store_b128(gran2(sa, sb_, thi, tlo), rw,
              WS_G2 + ct * 17408 + (((tt >> 2) * 17 + (pp >> 2)) * 32 + 8 * (tt & 3) + 2 * (pp & 3)) * 8, 0, AUX_SC1);
      }
      STAMP(3);
      {
        u32x4 gq[5];
        const u32 off = WS_G2 + ct * 17408 + i * 16;
        for (int it = 0; it < SPIN_MAX; ++it) {
          fence();
          u32 bad = 0;
#pragma unroll
          for (int m = 0; m < 4; ++m) gq[m] = __builtin_amdgcn_raw_buffer_load_b128(rw, off + m * 4096, 0, AUX_SC1);
          if (w == 7) gq[4] = __builtin_amdgcn_raw_buffer_load_b128(rw, off + 16384 - 192 * 16, 0, AUX_SC1);
          else gq[4] = u32x4{tw, thi, tw, thi};
#pragma unroll
          for (int m = 0; m < 5; ++m) bad |= granbad(gq[m], thi, tw);
          if (__builtin_amdgcn_ballot_w64(bad != 0) == 0) break;
        }
#pragma unroll
        for (int m = 0; m < 4; ++m) *(LDS3 u32*)(B_KVT + (i + m * 256) * 4) = cvtpk(granval(gq[m].x), granval(gq[m].z));
        if (w == 7) *(LDS3 u32*)(B_KVT + (i + 1024 - 192) * 4) = cvtpk(granval(gq[4].x), granval(gq[4].z));
      }
      STAMP(4);
    }
    bar_l();
    if (w < 4) {
      STAMP(6);
      {
        const u32 rowd = l31 < 16 ? (8 * ((l31 >> 2) & 1) + 4 * (l31 >> 3) + (l31 & 3)) : 16;
        const u32 ka = B_KVT + (2 * wn * 17 + rowd) * 64 + h * 32;
        f32x16 zero;
#pragma unroll
        for (int i = 0; i < 16; ++i) zero[i] = 0.f;
        const u32 ooff = WS_OB + ((tt * 4 + 2 * wm) * 32 + 4 * ct + 2 * wn) * 1024 + l * 16;
#pragma unroll
        for (int hd = 0; hd < 2; ++hd) {
          const bf16x8 k0 = rd128(ka + hd * 1088), k1 = rd128(ka + hd * 1088 + 16);
#pragma unroll
          for (int mt = 0; mt < 2; ++mt) {
            f32x16 o = mfma(k0, __builtin_bit_cast(bf16x8, pq[mt][hd][0]), zero);
            o = mfma(k1, __builtin_bit_cast(bf16x8, pq[mt][hd][1]), o);
            const float r = __builtin_amdgcn_rcpf(o[8] + 1e-6f);
            const u32x4 ov = {cvtpk(o[0] * r, o[1] * r), cvtpk(o[2] * r, o[3] * r), cvtpk(o[4] * r, o[5] * r), cvtpk(o[6] * r, o[7] * r)};
            __builtin_amdgcn_raw_buffer_store_b128(ov, rw, ooff + (mt * 32 + hd) * 1024, 0, AUX_SC1);
          }
        }
      }
      WAITV(0);
      if (l == 0) __builtin_amdgcn_raw_buffer_store_b64(u32x2{tl32, thi}, rw, flagoff + wm * 128 + (ct * 2 + wn) * 8, 0, AUX_SC1);
      STAMP(7);
      wait_flags(rw, flagoff + wm * 128, l, thi, tl32);
      STAMP(8);
      f32x16 y0, y1;
#pragma unroll
      for (int i = 0; i < 16; ++i) { y0[i] = 0.f; y1[i] = 0.f; }
      {
        const u32 oa = WS_OB + (tt * 4 + w) * 32768 + l * 16;
        const u32 b0a = B_WO + (8 * (G >> 1) + tr_r) * 128 + (((2 * (G & 1) + (tr_c >> 1)) ^ ((tr_r >> 1) << 2)) << 4) + (tr_c & 1) * 8;
        const u32 b1a = b0a ^ 64;
        u32x4 af[8][4];
#pragma unroll
        for (int i = 0; i < 8; ++i) {
          const u32 so = ((ct + i) & 7) * 4096;
#pragma unroll
          for (int k = 0; k < 4; ++k) af[i][k] = __builtin_amdgcn_raw_buffer_load_b128(rw, oa + k * 1024, so, AUX_SC1);
        }
        sb();
#pragma unroll
        for (int i = 0; i < 8; ++i) {
#pragma unroll
          for (int k = 0; k < 4; ++k) {
            const u32 o = i * 8192 + k * 2048;
            const bf16x8 b0 = rdtr(b0a + o, b0a + o + 512), b1 = rdtr(b1a + o, b1a + o + 512);
            const bf16x8 av = __builtin_bit_cast(bf16x8, af[i][k]);
            y0 = mfma(av, b0, y0); y1 = mfma(av, b1, y1);
          }
        }
      }
      STAMP(9);
      store_out(a.out, y0, y1, 128 * tt + 32 * w, 512 + 64 * ct, l);
      if (tt == 15 && ct == 0 && w == 0 && l == 0) __builtin_amdgcn_raw_buffer_store_b32(E + 1, rw, WS_EPB, 0, AUX_SC1);
      STAMP(10);
    }
  }
  if (b == 0 && tid == 0) *(volatile u64*)(a.ws + WS_DIAG) = tp0;
  if (0)
  if (w < 4) {
    WAITV(0);
    STAMP(11);
    u64 tgt = tp0 + 4000 + ((PCH == 2 || PCH == (int)isA) ? tp1 - tp0 : 0);
    while (__builtin_amdgcn_s_memrealtime() < tgt) __builtin_amdgcn_s_sleep(2);
  }
}

__global__ void pre_k(u64* p) { if (threadIdx.x == 0) *p = __builtin_amdgcn_s_memrealtime(); }
__global__ void post_k(u64* p) { const u64 tgt = *(volatile u64*)p + 4000; while (__builtin_amdgcn_s_memrealtime() < tgt) __builtin_amdgcn_s_sleep(2); }
#define BENCH_LAUNCH(first, last, k, grid, block, shm, st, ...) hipLaunchKernelGGL(k, dim3(grid), dim3(block), shm, st, __VA_ARGS__)

extern "C" void kernel_launch(void* const* d_in, const int* in_sizes, int n_in, void* d_out, int out_size, void* d_ws, size_t ws_size, hipStream_t stream) {
  Args a;
  for (int i = 0; i < 13; ++i) a.in[i] = (const u16*)d_in[i];
  a.out = (float*)d_out;
  a.ws = (unsigned char*)d_ws;
  (void)hipFuncSetAttribute((const void*)fused_k, hipFuncAttributeMaxDynamicSharedMemorySize, 163840);
  BENCH_LAUNCH(true, true, fused_k, 256, 512, 163840, stream, a);
  post_k<<<1, 64, 0, stream>>>((u64*)(a.ws + WS_DIAG));
}
```

```cpp
#include <hip/hip_runtime.h>
#include <hip/hip_bf16.h>

#pragma clang diagnostic ignored "-Winline-asm"
typedef unsigned short u16;
typedef unsigned int u32;
typedef unsigned long long u64;
typedef __bf16 bf16x8 __attribute__((ext_vector_type(8)));
typedef __bf16 bf16x2 __attribute__((ext_vector_type(2)));
typedef short s16x4 __attribute__((ext_vector_type(4)));
typedef float f32x2 __attribute__((ext_vector_type(2)));
typedef float f32x4 __attribute__((ext_vector_type(4)));
typedef float f32x16 __attribute__((ext_vector_type(16)));
typedef u32 u32x2 __attribute__((ext_vector_type(2)));
typedef u32 u32x4 __attribute__((ext_vector_type(4)));
#define LDS3 __attribute__((address_space(3)))
#define DI __device__ __forceinline__

#define WS_EPA   0u
#define WS_EPB   256u
#define WS_FLAG  4096u
#define WS_GA    65536u
#define WS_PF    16384u
#define WS_G1    524288u
#define WS_HA    4194304u
#define WS_OB    8388608u
#define WS_BYTES 16777216u
#define WS_DIAG  16777216u
#define AUX_SC1 16
#define SPIN_MAX (1 << 18)

struct Args { const u16* in[13]; float* out; unsigned char* ws; };

#define STAMP(i)

#define CST(i)
#define LST(i)
DI u32 umin_(u32 a, u32 b) { return __builtin_elementwise_min(a, b); }
DI u32 umax_(u32 a, u32 b) { return __builtin_elementwise_max(a, b); }
DI float bf2f(u32 v) { return __uint_as_float(v << 16); }
DI u32 cvtpk(float a, float b) { f32x2 t = {a, b}; return __builtin_bit_cast(u32, __builtin_convertvector(t, bf16x2)); }
DI u32x4 srd(const void* p, u32 bytes) { u64 a = (u64)p; u32x4 r = {(u32)a, (u32)(a >> 32) & 0xffffu, bytes, 0x00020000u}; return r; }
DI void glds(u32 ldsdst, u32 voff, u32x4 rs, u32 soff) {
  asm volatile("s_mov_b32 m0, %0\n\ts_nop 0\n\tbuffer_load_dwordx4 %1, %2, %3 offen lds" :: "s"(ldsdst), "v"(voff), "s"(rs), "s"(soff) : "m0", "memory");
}
DI void gpre(u32 ldsdst, u32 voff, u32x4 rs, u32 soff) {
  asm volatile("s_mov_b32 m0, %0\n\ts_nop 0\n\tbuffer_load_dword %1, %2, %3 offen lds" :: "s"(ldsdst), "v"(voff), "s"(rs), "s"(soff) : "m0", "memory");
}
DI u32 load_epoch(const void* p) { u32 e; asm volatile("s_load_dword %0, %1, 0x0\n\ts_waitcnt lgkmcnt(0)" : "=s"(e) : "s"(p) : "memory"); return e; }
#define WAITV(n) asm volatile("s_waitcnt vmcnt(%0)" :: "n"(n) : "memory")
DI void bar_l() { asm volatile("s_waitcnt lgkmcnt(0)\n\ts_barrier" ::: "memory"); }
DI void bar() { asm volatile("s_barrier" ::: "memory"); }
DI void fence() { asm volatile("" ::: "memory"); }
DI void sb() { __builtin_amdgcn_sched_barrier(0); }
DI bf16x8 rd128(u32 a) { return *(const LDS3 bf16x8*)a; }
DI bf16x8 rdtr(u32 a0, u32 a1) {
  s16x4 x = __builtin_amdgcn_ds_read_tr16_b64_v4i16((LDS3 s16x4*)a0);
  s16x4 y = __builtin_amdgcn_ds_read_tr16_b64_v4i16((LDS3 s16x4*)a1);
  return __builtin_bit_cast(bf16x8, __builtin_shufflevector(x, y, 0, 1, 2, 3, 4, 5, 6, 7));
}
DI f32x16 mfma(bf16x8 a, bf16x8 b, f32x16 c) { return __builtin_amdgcn_mfma_f32_32x32x16_bf16(a, b, c, 0, 0, 0); }
DI f32x2 fma2(f32x2 a, f32x2 b, f32x2 c) { return __builtin_elementwise_fma(a, b, c); }
DI f32x2 bc(float a) { f32x2 r = {a, a}; return r; }
struct cpx { float r, i; };
DI cpx cmul(cpx a, cpx b) { cpx c; c.r = a.r * b.r - a.i * b.i; c.i = a.r * b.i + a.i * b.r; return c; }
DI cpx cfma(cpx a, cpx b, cpx c) { cpx d; d.r = fmaf(a.r, b.r, fmaf(-a.i, b.i, c.r)); d.i = fmaf(a.r, b.i, fmaf(a.i, b.r, c.i)); return d; }
DI cpx csq(cpx a) { return cmul(a, a); }
DI u32x4 gran2(float a, float b, u32 thi, u32 tlo) {
  u32x4 g = {__builtin_amdgcn_alignbit(tlo, __float_as_uint(a), 8), thi, __builtin_amdgcn_alignbit(tlo, __float_as_uint(b), 8), thi};
  return g;
}
DI u32 granbad(u32x4 g, u32 thi, u32 tw) { return ((g.y ^ thi) | (g.w ^ thi)) | (((g.x ^ tw) | (g.z ^ tw)) >> 24); }
DI float granval(u32 lo) { return __uint_as_float(lo << 8); }
DI bf16x8 pack8(const f32x16& v, int o) {
  u32x4 r = {cvtpk(v[o], v[o + 1]), cvtpk(v[o + 2], v[o + 3]), cvtpk(v[o + 4], v[o + 5]), cvtpk(v[o + 6], v[o + 7])};
  return __builtin_bit_cast(bf16x8, r);
}
DI void sincos_cw(float x, float& sn, float& cs) {
  const float k = __builtin_rintf(x * 0.636619772367581343f);
  float r = fmaf(k, -1.57079601287841796875f, x);
  r = fmaf(k, -3.1391647326017846353e-07f, r);
  r = fmaf(k, -5.3903025299577647655e-15f, r);
  const float z = r * r;
  const float s = fmaf(r * z, fmaf(z, fmaf(z, -1.95152959e-4f, 8.33216087e-3f), -1.66666546e-1f), r);
  const float c = fmaf(z, fmaf(z, fmaf(z, fmaf(z, 2.44331571e-5f, -1.38873162e-3f), 4.16666457e-2f), -0.5f), 1.f);
  const int q = (int)k;
  const float ss = (q & 1) ? c : s, cc = (q & 1) ? s : c;
  sn = (q & 2) ? -ss : ss;
  cs = ((q + 1) & 2) ? -cc : cc;
}
DI float exp_acc(float x) {
  const float t = x * 1.44269502162933349609f;
  const float r = fmaf(x, 1.925963033500011e-8f, fmaf(x, 1.44269502162933349609f, -t));
  const float e = __builtin_amdgcn_exp2f(t);
  return fmaf(e, r * 0.693147182464599609f, e);
}
DI u32 early_epoch(const void* p) { u32 e; asm volatile("s_load_dword %0, %1, 0x0" : "=s"(e) : "s"(p) : "memory"); return e; }
DI u32 late_epoch(const void* p) { asm volatile("" : "+s"(p)); return *(const __attribute__((address_space(4))) u32*)p; }
DI void wait_flags(__amdgpu_buffer_rsrc_t rw, u32 off, u32 thi, u32 tl32) {
  for (int it = 0; it < SPIN_MAX; ++it) {
    fence();
    u32x2 f = __builtin_amdgcn_raw_buffer_load_b64(rw, off, 0, AUX_SC1);
    if (__builtin_amdgcn_ballot_w64((f.x != tl32) | (f.y != thi)) == 0) break;
    __builtin_amdgcn_s_sleep(1);
  }
  fence();
}
DI void softmax16(const f32x16& p, u32x4& o0, u32x4& o1) {
  float m = fmaxf(p[0], p[1]);
#pragma unroll
  for (int i = 2; i < 16; i += 2) m = fmaxf(fmaxf(m, p[i]), p[i + 1]);
  const float L2E = 1.44269504088896340736f;
  f32x2 nm = bc(-m * L2E), e[8];
#pragma unroll
  for (int i = 0; i < 8; ++i) {
    f32x2 t = {p[2 * i], p[2 * i + 1]};
    t = fma2(t, bc(L2E), nm);
    e[i].x = __builtin_amdgcn_exp2f(t.x); e[i].y = __builtin_amdgcn_exp2f(t.y);
  }
  f32x2 s = ((e[0] + e[1]) + (e[2] + e[3])) + ((e[4] + e[5]) + (e[6] + e[7]));
  f32x2 r = bc(__builtin_amdgcn_rcpf(s.x + s.y));
  u32 w[8];
#pragma unroll
  for (int i = 0; i < 8; ++i) { f32x2 t = e[i] * r; w[i] = cvtpk(t.x, t.y); }
  o0 = u32x4{w[0], w[1], w[2], w[3]}; o1 = u32x4{w[4], w[5], w[6], w[7]};
}
#define OUT_AUX 16
DI void store_out(float* out, const f32x16& a0, const f32x16& a1, int row0, int col0, int l) {
  const __amdgpu_buffer_rsrc_t ro = __builtin_amdgcn_make_buffer_rsrc((void*)out, 0, 8388608, 0x00020000);
  const u32 p = ((row0 + 4 * (l >> 5)) * 1024 + col0 + (l & 31)) * 4;
#pragma unroll
  for (int r = 0; r < 16; ++r) {
    __builtin_amdgcn_raw_buffer_store_b32(__float_as_uint(a0[r]), ro, p, (8 * (r >> 2) + (r & 3)) * 4096, OUT_AUX);
    __builtin_amdgcn_raw_buffer_store_b32(__float_as_uint(a1[r]), ro, p + 128, (8 * (r >> 2) + (r & 3)) * 4096, OUT_AUX);
  }
}

#define A_SLOT 32768u
#define A_STG  0u
#define A_SEG  16384u
#define A_PART 20480u
#define A_LAM  22528u
#define A_DK   23552u
#define A_PAR  134144u
#define A_PRE  132096u
#define B_SLOT 45056u
#define B_KVT  81920u
#define B_WO   98304u
#define B_WPHI 135168u
#define B_PRE  136192u

__global__ __launch_bounds__(512, 2) void fused_k(Args a) {
  extern __shared__ __attribute__((aligned(16))) char smem_[];
  const int b = blockIdx.x, g = b & 7, jb = b >> 3;
  const int tid = threadIdx.x, l = tid & 63;
  const int w = __builtin_amdgcn_readfirstlane(tid >> 6);
  const bool isA = g >= 4;
  const int tt = 4 * (g & 3) + (jb >> 3), ct = jb & 7;
  const int tb = tt >> 1, cb = jb & 15;
#define ROT 0
  const int s0 = ROT == 0 ? 0 : (ROT == 2 ? ((ct ^ (jb >> 3)) & 1) * 4 : ((ct + (jb >> 3)) & 3) * 2);
  const int h = l >> 5, l31 = l & 31;
  STAMP(0);
  const void* const pE = a.ws + (isA ? WS_EPA : WS_EPB);
  u32 E = 0;
  const __amdgpu_buffer_rsrc_t rw = __builtin_amdgcn_make_buffer_rsrc((void*)a.ws, 0, (int)WS_BYTES, 0x00020000);
  const u32 flagoff = WS_FLAG + (isA ? 0u : 4096u) + tt * 256;

  if (w < 4) {
    const bool px = isA ? w < 2 : w == 0;
    const u16* const src = px ? (isA ? a.in[0] : a.in[9]) : (isA ? (w == 2 ? a.in[3] : a.in[4]) : a.in[10]);
    const u32x4 rp = srd(src, 2097152u);
    u32 vp, st;
    if (isA && px) { vp = (512 * (g & 3) + 16 * jb + 8 * w + (l & 7)) * 1024 + (l >> 3) * 128; st = 0; }
    else if (isA || px) { vp = ((px ? 512 * (g & 3) : 0) + 16 * jb + (l & 15)) * 1024 + (l >> 4) * 128; st = 512; }
    else { vp = (64 * (l >> 4) + 2 * jb + ((l >> 3) & 1)) * 3072 + (w - 1) * 1024 + (l & 7) * 128; st = 256 * 3072; }
    const u32 dst = (isA ? A_PRE : B_PRE) + 1024 + w * 256;
    gpre(dst, vp, rp, 0);
    if (!(isA && px)) gpre(dst, vp + st, rp, 0);
  }
  if (w >= 4) {
    const int w4 = w - 4;
    const u32 lr8 = l >> 3;
    if (isA) {
      const u32x4 rs = srd(w4 < 2 ? a.in[0] : (w4 == 2 ? a.in[3] : a.in[4]), w4 < 2 ? 2097152u : 524288u);
      const u32 lp = w4 < 2 ? (32 * (lr8 >> 2) + 16 * (lr8 & 1) + ((lr8 >> 1) & 1)) : lr8;
      const u32 v0 = lp * 1024 + (((l & 7) ^ (l >> 4)) << 4), v1 = v0 ^ 64;
      const u32 rb = w4 < 2 ? (128 * tt + 64 * w4) : 64 * ct;
      const u32 pa = w4 < 2 ? 2048u : 8192u, pb = w4 < 2 ? 8192u : 32768u;
      auto issue = [&](int i) {
        const u32 so = rb * 1024 + (((s0 + i) & 7) << 7);
        const u32 dst = (i & 3) * A_SLOT + w4 * 8192;
#pragma unroll
        for (int p = 0; p < 8; ++p) glds(dst + p * 1024, (p & 1) ? v1 : v0, rs, so + (p & 3) * pa + (p >> 2) * pb);
      };
      issue(0); issue(1);
      gpre(A_PAR + w4 * 256, l * 4, srd(w4 == 0 ? a.in[1] : (w4 == 1 ? a.in[2] : (w4 == 2 ? a.in[7] : a.in[8])), 1024u), ct * 128);
      const u32x4 rc = srd((w4 & 1) ? a.in[6] : a.in[5], 524288u);
      const u32 vc0 = lr8 * 1024 + (((l & 7) ^ (l >> 4)) << 4), vc1 = vc0 ^ 64;
      auto issueC = [&](int i) {
        const u32 so = 64 * ct * 1024 + (((ct + i) & 7) << 7);
        const u32 dst = (i < 2 ? 131072u + i * 16384u : (i * 16384u)) + (w4 & 1) * 8192;
#pragma unroll
        for (int p = 0; p < 8; ++p) glds(dst + p * 1024, (p & 1) ? vc1 : vc0, rc, so + p * 8192);
      };
      LST(0); WAITV(9); LST(1); bar(); LST(2); issue(2);
      LST(3); WAITV(8); LST(4); bar(); LST(5); issue(3);
      const float lr = bf2f(*(const LDS3 u16*)(A_PAR + l * 2)), li = bf2f(*(const LDS3 u16*)(A_PAR + 256 + l * 2));
      float dk = bf2f(*(const LDS3 u16*)(A_PAR + 768 + l * 2));
      const float dt = bf2f(cvtpk(exp_acc(bf2f(*(const LDS3 u16*)(A_PAR + 512 + l * 2))), 0.f) & 0xffffu);
      float mg = exp_acc(dt * lr), th = dt * li, rd = __builtin_amdgcn_rcpf(lr * lr + li * li);
      asm volatile("" : "+v"(mg), "+v"(th), "+v"(rd), "+v"(dk));
      LST(6); WAITV(8); LST(7); bar(); LST(8); issue(4);
      gpre(A_PRE + w4 * 256, (16 * jb + 8 * (w4 >> 1) + lr8) * 1024 + (l & 7) * 128, rc, 0);
      float sn, cs;
      sincos_cw(th, sn, cs);
      asm volatile("" : "+v"(sn), "+v"(cs));
      LST(9); WAITV(9); LST(10); bar(); LST(11); issue(5);
      cpx lam = {mg * cs, mg * sn}, coef;
      coef.r = ((lam.r - 1.f) * lr + lam.i * li) * rd; coef.i = (lam.i * lr - (lam.r - 1.f) * li) * rd;
      cpx l16 = csq(csq(csq(csq(lam))));
      asm volatile("" : "+v"(lam.r), "+v"(lam.i), "+v"(coef.r), "+v"(coef.i), "+v"(l16.r), "+v"(l16.i));
      LST(12); WAITV(8); LST(13); bar(); LST(14); issue(6); LST(15);
      cpx l128 = csq(csq(csq(l16)));
      cpx l256 = csq(l128), l512 = csq(l256);
      asm volatile("" : "+v"(l128.r), "+v"(l128.i), "+v"(l256.r), "+v"(l256.i), "+v"(l512.r), "+v"(l512.i));
      WAITV(8); bar(); issue(7);
      if (w4 == 0) { *(LDS3 f32x4*)(A_LAM + l * 16) = f32x4{lam.r, lam.i, coef.r, coef.i}; *(LDS3 float*)(A_DK + l * 4) = dk; }
      asm volatile("s_waitcnt lgkmcnt(0)" ::: "memory");
      WAITV(8); bar(); issueC(w4 >> 1);
      WAITV(8); bar(); issueC(2 + (w4 >> 1));
      E = load_epoch(pE);
      const u64 T = (u64)E * 0x9E3779B97F4A7C15ull + 0x632BE59BD9B4E019ull;
      const u32 thi = (u32)(T >> 32), tlo = (u32)(T >> 24) & 0xffu, tw = tlo << 24;
      STAMP(1);
      bar();
      if (w4 == 3) {
        cpx t = {0.f, 0.f};
#pragma unroll
        for (int s = 0; s < 8; ++s) { f32x2 e = *(const LDS3 f32x2*)(A_SEG + s * 512 + l * 8); t = cfma(l16, t, cpx{e.x, e.y}); }
        __builtin_amdgcn_raw_buffer_store_b128(gran2(t.r, t.i, thi, tlo), rw, WS_GA + (tt * 512 + 64 * ct + l) * 16, 0, AUX_SC1);
      }
      STAMP(2);
      cpx P = {0.f, 0.f};
      if (w4 < tt) {
        const int jl = w4 + ((tt - 1 - w4) & ~3), ex = tt - 1 - jl;
        u32x4 gq[4];
        for (int it = 0; it < SPIN_MAX; ++it) {
          fence();
          u32 bad = 0;
#pragma unroll
          for (int m = 0; m < 4; ++m) {
            const int j = jl - 4 * (3 - m);
            if (j >= 0) gq[m] = __builtin_amdgcn_raw_buffer_load_b128(rw, WS_GA + (64 * ct + l) * 16, j * 8192, AUX_SC1);
            else gq[m] = u32x4{tw, thi, tw, thi};
          }
#pragma unroll
          for (int m = 0; m < 4; ++m) bad |= granbad(gq[m], thi, tw);
          if (__builtin_amdgcn_ballot_w64(bad != 0) == 0) break;
        }
#pragma unroll
        for (int m = 0; m < 4; ++m) P = cfma(l512, P, cpx{granval(gq[m].x), granval(gq[m].z)});
        cpx f = {1.f, 0.f};
        if (ex & 1) f = l128;
        if (ex & 2) f = cmul(f, l256);
        P = cmul(P, f);
      }
      *(LDS3 f32x2*)(A_PART + l * 32 + w4 * 8) = f32x2{P.r, P.i};
      STAMP(3);
      bar_l();
      issueC(4 + (w4 >> 1));
      issueC(6 + (w4 >> 1));
      WAITV(0);
      bar();
      STAMP(4);
    } else {
      const u32x4 rx = srd(a.in[9], 2097152u), rq = srd(a.in[10], 1572864u);
      const u32 vx0 = lr8 * 1024 + (((l & 7) ^ (l >> 4)) << 4), vx1 = vx0 ^ 64;
      const u32 vw = (l >> 2) * 3072 + (l & 3) * 16;
      auto issue = [&](int i) {
        const u32 ks = (s0 + i) & 7;
        const u32 dst = (i % 3) * B_SLOT;
        const u32 sx = (256 * tb + 64 * w4) * 1024 + (ks << 7);
        const u32 sw = ks * 64 * 3072 + 64 * cb;
#pragma unroll
        for (int p = 0; p < 3; ++p) { const u32 pc = 3 * w4 + p; glds(dst + 32768 + pc * 1024, vw, rq, sw + (pc & 3) * 49152 + (pc >> 2) * 1024); }
#pragma unroll
        for (int p = 0; p < 8; ++p) glds(dst + (8 * w4 + p) * 1024, (p & 1) ? vx1 : vx0, rx, sx + p * 8192);
      };
      if (w4 == 0) glds(B_WPHI, l * 16, srd(a.in[11], 16384u), cb * 1024);
      issue(0); issue(1);
      const u32x4 ro = srd(a.in[12], 524288u);
      const u32 vo = lr8 * 1024 + (((l & 7) ^ (((l >> 4) & 1) << 2)) << 4);
      auto issueO = [&](int i) {
        const u32 so = (((ct + i) & 7) << 16) + 128 * ct;
#pragma unroll
        for (int p = 0; p < 8; ++p) glds(B_WO + i * 8192 + p * 1024, vo, ro, so + p * 8192);
      };
      WAITV(11); bar(); issue(2);
      WAITV(11); bar(); issue(3);
      WAITV(11); bar(); issue(4);
      gpre(B_PRE + w4 * 256, (16 * jb + 4 * w4 + (lr8 & 3)) * 1024 + (l & 7) * 128, ro, 0);
      WAITV(12); bar(); issue(5);
      WAITV(11); bar(); issue(6);
      WAITV(11); bar(); issue(7);
      WAITV(11); bar(); issueO(w4);
      WAITV(8); bar(); issueO(4 + w4);
      E = load_epoch(pE);
      STAMP(1);
    }
  }
  u32 thi = 0, tl32 = 0, tlo = 0, tw = 0;
  auto tags = [&]() {
    const u64 T = (u64)E * 0x9E3779B97F4A7C15ull + 0x632BE59BD9B4E019ull;
    thi = (u32)(T >> 32); tl32 = (u32)T; tlo = (u32)(T >> 24) & 0xffu; tw = tlo << 24;
  };
  const int wm = (w >> 1) & 1, wn = w & 1;
  const int G = l >> 4, tr_r = (l >> 2) & 3, tr_c = l & 3;
  const u32 fragrow = l31 * 128;
  const u32 fsw = (l31 >> 1) & 7;

  if (isA) {
    if (w < 4) {
      u32 ax[2][4], aw[2][4];
#pragma unroll
      for (int k = 0; k < 4; ++k) {
        const u32 t = fragrow + (((2 * k + h) ^ fsw) << 4);
        ax[0][k] = t + wm * 8192; aw[0][k] = t + 16384 + wn * 4096;
        ax[1][k] = ax[0][k] + 65536; aw[1][k] = aw[0][k] + 65536;
        asm volatile("" : "+v"(ax[0][k]), "+v"(ax[1][k]), "+v"(aw[0][k]), "+v"(aw[1][k]));
      }
      const u32 rho = 64 * (w >> 1) + 16 * (G >> 1) + 8 * (tr_r >> 1) + 4 * (w & 1) + 2 * (tr_r & 1);
      u32 xs0 = rho * 128 + (((2 * (G & 1) + (tr_c >> 1)) ^ ((rho >> 1) & 7)) << 4) + (tr_c & 1) * 8 + (((ct - s0) & 3) << 15);
      u32 xs1 = xs0 ^ 64;
      asm volatile("" : "+v"(xs0), "+v"(xs1));
      f32x16 aR[2], aI[2];
#pragma unroll
      for (int i = 0; i < 16; ++i) { aR[0][i] = 0.f; aR[1][i] = 0.f; aI[0][i] = 0.f; aI[1][i] = 0.f; }
      s16x4 xk[8];
#pragma unroll
      for (int i = 0; i < 8; ++i) xk[i] = s16x4{0, 0, 0, 0};
      const int sx = (ct - s0) & 7;
      bf16x8 fx0[2], fx1[2], fwr[2], fwi[2];
      bar();
      STAMP(1);
      fx0[0] = rd128(ax[0][0]); fwr[0] = rd128(aw[0][0]); fx1[0] = rd128(ax[0][0] + 4096); fwi[0] = rd128(aw[0][0] + 8192);
      sb();
      f32x4 lc;
#pragma unroll
      for (int n = 0; n < 32; ++n) {
        const int s = n >> 2, k = n & 3, c = n & 1, m = n + 1, hb = (m >> 3) & 1, so = ((m >> 2) & 1) * 32768;
        if (k == 0) CST(s);
        aR[0] = mfma(fx0[c], fwr[c], aR[0]); sb();
        if (n == 15) { asm volatile("s_waitcnt vmcnt(0) lgkmcnt(0)\n\ts_barrier" ::: "memory"); sb(); }
        else if (k == 3 && s < 7) { bar_l(); sb(); }
        if (n < 31) { fx0[c ^ 1] = rd128(ax[hb][m & 3] + so); fwr[c ^ 1] = rd128(aw[hb][m & 3] + so); sb(); }
        else { lc = *(const LDS3 f32x4*)(A_LAM + (32 * wn + l31) * 16); sb(); }
        aR[1] = mfma(fx1[c], fwr[c], aR[1]); sb();
        if (n < 31) { fx1[c ^ 1] = rd128(ax[hb][m & 3] + so + 4096); fwi[c ^ 1] = rd128(aw[hb][m & 3] + so + 8192); sb(); }
        aI[0] = mfma(fx0[c], fwi[c], aI[0]); sb();
        if (k == 0 && __builtin_expect(sx == s, 0)) {
#pragma unroll
          for (int q = 0; q < 4; ++q) {
            const u32 o = (32 * (q & 1) + (q >> 1)) * 128;
            xk[q] = __builtin_amdgcn_ds_read_tr16_b64_v4i16((LDS3 s16x4*)(o + xs0));
            xk[4 + q] = __builtin_amdgcn_ds_read_tr16_b64_v4i16((LDS3 s16x4*)(o + xs1));
          }
        }
        sb();
        aI[1] = mfma(fx1[c], fwi[c], aI[1]); sb();
      }
      E = early_epoch(pE);
      const cpx lam = {lc.x, lc.y}, coef = {lc.z, lc.w};
      CST(8);
      STAMP(2);
      const f32x2 Lr = bc(lam.r), Li = bc(lam.i), nLi = bc(-lam.i);
      {
        f32x2 hr = {aR[0][0], aR[0][1]}, hi = {aI[0][0], aI[0][1]};
#pragma unroll
        for (int p = 1; p < 16; ++p) {
          const f32x2 ur = {aR[p >> 3][2 * (p & 7)], aR[p >> 3][2 * (p & 7) + 1]}, ui = {aI[p >> 3][2 * (p & 7)], aI[p >> 3][2 * (p & 7) + 1]};
          const f32x2 nr = fma2(Lr, hr, fma2(nLi, hi, ur)), ni = fma2(Lr, hi, fma2(Li, hr, ui));
          hr = nr; hi = ni;
        }
        const u32 sa = A_SEG + (4 * wm + 2 * h) * 512 + (32 * wn + l31) * 8;
        *(LDS3 f32x2*)sa = f32x2{hr.x, hi.x};
        *(LDS3 f32x2*)(sa + 512) = f32x2{hr.y, hi.y};
      }
      asm volatile("s_waitcnt lgkmcnt(0)\n\ts_barrier" : "+s"(E) :: "memory");
      STAMP(3);
      const float dk0 = *(const LDS3 float*)(A_DK + l31 * 4), dk1 = *(const LDS3 float*)(A_DK + 128 + l31 * 4);
      const cpx l16 = csq(csq(csq(csq(lam))));
      const cpx c2 = {2.f * coef.r, 2.f * coef.i};
      cpx pre0 = {0.f, 0.f}, pre1;
      {
        const int sg = 4 * wm + 2 * h;
        cpx e0 = {0.f, 0.f};
#pragma unroll
        for (int s = 0; s < 7; ++s) {
          f32x2 e = *(const LDS3 f32x2*)(A_SEG + s * 512 + (32 * wn + l31) * 8);
          cpx t = cfma(l16, pre0, cpx{e.x, e.y});
          if (s < sg) pre0 = t;
          if (s == sg) e0 = cpx{e.x, e.y};
        }
        pre1 = cfma(l16, pre0, e0);
      }
      pre0 = cmul(c2, pre0); pre1 = cmul(c2, pre1);
      cpx m0 = c2;
      {
        const cpx l32 = csq(l16), l64 = csq(l32);
        if (h) m0 = cmul(m0, l32);
        if (wm) m0 = cmul(m0, l64);
      }
      const cpx m1 = cmul(m0, l16);
#pragma unroll
      for (int p = 0; p < 16; ++p) {
        const f32x2 ur = {aR[p >> 3][2 * (p & 7)], aR[p >> 3][2 * (p & 7) + 1]}, ui = {aI[p >> 3][2 * (p & 7)], aI[p >> 3][2 * (p & 7) + 1]};
        const f32x2 vr = fma2(bc(c2.r), ur, bc(-c2.i) * ui), vi = fma2(bc(c2.r), ui, bc(c2.i) * ur);
        aR[p >> 3][2 * (p & 7)] = vr.x; aR[p >> 3][2 * (p & 7) + 1] = vr.y;
        aI[p >> 3][2 * (p & 7)] = vi.x; aI[p >> 3][2 * (p & 7) + 1] = vi.y;
      }
      f32x16 y0, y1;
#pragma unroll
      for (int q = 0; q < 4; ++q) {
        const u32x2 t0 = __builtin_bit_cast(u32x2, xk[q]), t1 = __builtin_bit_cast(u32x2, xk[4 + q]);
#pragma unroll
        for (int e = 0; e < 2; ++e) {
          const u32 r0 = cvtpk(bf2f(t0[e] & 0xffffu) * dk0, __uint_as_float(t0[e] & 0xffff0000u) * dk0);
          const u32 r1 = cvtpk(bf2f(t1[e] & 0xffffu) * dk1, __uint_as_float(t1[e] & 0xffff0000u) * dk1);
          y0[4 * q + 2 * e] = __uint_as_float(r0 << 16); y0[4 * q + 2 * e + 1] = __uint_as_float(r0 & 0xffff0000u);
          y1[4 * q + 2 * e] = __uint_as_float(r1 << 16); y1[4 * q + 2 * e + 1] = __uint_as_float(r1 & 0xffff0000u);
        }
      }
      asm volatile("" : "+v"(y0), "+v"(y1), "+v"(aR[0]), "+v"(aR[1]), "+v"(aI[0]), "+v"(aI[1]));
      tags();
      asm volatile("" : "+s"(thi), "+s"(tl32));
      STAMP(4);
      bar();
      STAMP(5);
      {
        const f32x4 pa = *(const LDS3 f32x4*)(A_PART + (32 * wn + l31) * 32), pb = *(const LDS3 f32x4*)(A_PART + (32 * wn + l31) * 32 + 16);
        const float cr = (pa.x + pa.z) + (pb.x + pb.z), ci = (pa.y + pa.w) + (pb.y + pb.w);
        const f32x2 Mr = {m0.r, m1.r}, Mi = {m0.i, m1.i};
        f32x2 hr = fma2(Mr, bc(cr), fma2(-Mi, bc(ci), f32x2{pre0.r, pre1.r}));
        f32x2 hi = fma2(Mr, bc(ci), fma2(Mi, bc(cr), f32x2{pre0.i, pre1.i}));
#pragma unroll
        for (int p = 0; p < 16; ++p) {
          const f32x2 ur = {aR[p >> 3][2 * (p & 7)], aR[p >> 3][2 * (p & 7) + 1]}, ui = {aI[p >> 3][2 * (p & 7)], aI[p >> 3][2 * (p & 7) + 1]};
          const f32x2 nr = fma2(Lr, hr, fma2(nLi, hi, ur)), ni = fma2(Lr, hi, fma2(Li, hr, ui));
          hr = nr; hi = ni;
          aR[p >> 3][2 * (p & 7)] = hr.x; aR[p >> 3][2 * (p & 7) + 1] = hr.y;
          aI[p >> 3][2 * (p & 7)] = hi.x; aI[p >> 3][2 * (p & 7) + 1] = hi.y;
        }
      }
      {
        const u32 f = (l31 & 1) | ((l31 & 2) << 1) | ((l31 & 4) >> 1);
        const u32 wb = A_STG + w * 4096 + l31 * 128;
        const u32 chr = 8 * (G >> 1) + tr_r;
        const u32 fr = (tr_r & 1) | ((tr_r & 2) << 1);
        const u32 rb0 = A_STG + w * 4096 + chr * 128 + (tr_c & 1) * 8;
        const u32 cq = 2 * (G & 1) + (tr_c >> 1);
        const u32 hoff = WS_HA + ((tt * 4 + 2 * wm) * 64 + ct * 8 + 2 * wn) * 1024 + l * 16;
#pragma unroll
        for (int ri = 0; ri < 2; ++ri) {
          const f32x16* src = ri ? aI : aR;
          const float sg = ri ? -1.f : 1.f;
#pragma unroll
          for (int c = 0; c < 2; ++c)
#pragma unroll
            for (int i = 0; i < 2; ++i) {
              u32x4 v = {cvtpk(sg * src[i][c], sg * src[i][2 + c]), cvtpk(sg * src[i][4 + c], sg * src[i][6 + c]),
                         cvtpk(sg * src[i][8 + c], sg * src[i][10 + c]), cvtpk(sg * src[i][12 + c], sg * src[i][14 + c])};
              *(LDS3 u32x4*)(wb + (((4 * h + 2 * c + i) ^ f) << 4)) = v;
            }
#pragma unroll
          for (int mt = 0; mt < 2; ++mt)
#pragma unroll
            for (int ks = 0; ks < 2; ++ks) {
              const bf16x8 fr8 = rdtr(rb0 + ks * 2048 + (((4 * mt + cq) ^ fr) << 4), rb0 + ks * 2048 + 512 + (((4 * mt + cq) ^ (fr | 2)) << 4));
              __builtin_amdgcn_raw_buffer_store_b128(__builtin_bit_cast(u32x4, fr8), rw, hoff + (mt * 64 + ri * 4 + ks) * 1024, 0, AUX_SC1);
            }
        }
      }
      STAMP(6);
      WAITV(0);
      if (l == 0) __builtin_amdgcn_raw_buffer_store_b64(u32x2{tl32, thi}, rw, flagoff + wm * 128 + (ct * 2 + wn) * 8, 0, AUX_SC1);
      STAMP(7);
      bar();
      wait_flags(rw, flagoff + wm * 128 + (l & 15) * 8, thi, tl32);
      STAMP(8);
      {
        const u32 ha = WS_HA + (tt * 4 + w) * 65536 + l * 16;
        u32 ca[4], cb[4];
#pragma unroll
        for (int k = 0; k < 4; ++k) { const u32 t = fragrow + (((2 * k + h) ^ fsw) << 4); ca[k] = t + 98304; cb[k] = t + 32768; }
        u32x4 af[4][8];
        auto ld = [&](int i) {
          const u32 so = ((ct + i) & 7) * 8192;
#pragma unroll
          for (int k = 0; k < 8; ++k) af[i & 3][k] = __builtin_amdgcn_raw_buffer_load_b128(rw, ha + k * 1024, so, AUX_SC1);
        };
        auto cad = [&](int n) { const int i = n >> 3, k = n & 7; return (i < 2 ? ca[k & 3] + 32768 + i * 16384 : (i < 6 ? cb[k & 3] + (i - 2) * 16384 : ca[k & 3] + (i - 6) * 16384)) + (k >> 2) * 8192; };
        ld(0); ld(1); ld(2);
        bf16x8 bq[3][2];
        bq[0][0] = rd128(cad(0)); bq[0][1] = rd128(cad(0) + 4096);
        bq[1][0] = rd128(cad(1)); bq[1][1] = rd128(cad(1) + 4096);
        sb();
#pragma unroll
        for (int n = 0; n < 64; ++n) {
          if ((n & 7) == 0) CST(10 + (n >> 3));
          const bf16x8 av = __builtin_bit_cast(bf16x8, af[(n >> 3) & 3][n & 7]);
          y0 = mfma(av, bq[n % 3][0], y0); sb();
          if (n + 2 < 64) bq[(n + 2) % 3][0] = rd128(cad(n + 2));
          if (n + 24 < 64) af[((n >> 3) + 3) & 3][n & 7] = __builtin_amdgcn_raw_buffer_load_b128(rw, ha + (n & 7) * 1024, ((ct + (n >> 3) + 3) & 7) * 8192, AUX_SC1);
          sb();
          y1 = mfma(av, bq[n % 3][1], y1); sb();
          if (n + 2 < 64) bq[(n + 2) % 3][1] = rd128(cad(n + 2) + 4096);
          sb();
        }
      }
      CST(18);
      STAMP(9);
      store_out(a.out, y0, y1, 128 * tt + 32 * w, 64 * ct, l);
      if (tt == 15 && ct == 0 && w == 0 && l == 0) __builtin_amdgcn_raw_buffer_store_b32(E + 1, rw, WS_EPA, 0, AUX_SC1);
      STAMP(10);
    }
  } else {
    u32x4 pq[2][2][2];
    auto publish = [&](u32 ad, u32 o) {
      const f32x4 t0 = (*(const LDS3 f32x4*)ad + *(const LDS3 f32x4*)(ad + 8192)) + (*(const LDS3 f32x4*)(ad + 16384) + *(const LDS3 f32x4*)(ad + 24576));
      const f32x4 t1 = (*(const LDS3 f32x4*)(ad + 16) + *(const LDS3 f32x4*)(ad + 8208)) + (*(const LDS3 f32x4*)(ad + 16400) + *(const LDS3 f32x4*)(ad + 24592));
      const u32x4 r = {umin_(cvtpk(t0.x, t0.y), ~1u), umin_(cvtpk(t0.z, t0.w), ~1u), umin_(cvtpk(t1.x, t1.y), ~1u), umin_(cvtpk(t1.z, t1.w), ~1u)};
      __builtin_amdgcn_raw_buffer_store_b128(r, rw, WS_G1 + cb * 24576 + tb * 1024 + o, 0, AUX_SC1);
    };
    if (w < 4) {
      u32 ax[2][4], aw[2];
#pragma unroll
      for (int k = 0; k < 4; ++k) {
        ax[0][k] = fragrow + (((2 * k + h) ^ fsw) << 4) + w * 8192; ax[1][k] = ax[0][k] + 2 * B_SLOT;
        asm volatile("" : "+v"(ax[0][k]), "+v"(ax[1][k]));
      }
      aw[0] = 32768 + (8 * (G >> 1) + tr_r) * 64 + (2 * (G & 1) + (tr_c >> 1)) * 16 + (tr_c & 1) * 8;
      aw[1] = aw[0] + 2 * B_SLOT;
      asm volatile("" : "+v"(aw[0]), "+v"(aw[1]));
      f32x16 q[2], kk[2], v[2];
      bf16x8 wf[2];
#pragma unroll
      for (int i = 0; i < 16; ++i) { q[0][i] = 0.f; q[1][i] = 0.f; kk[0][i] = 0.f; kk[1][i] = 0.f; v[0][i] = 0.f; v[1][i] = 0.f; }
      bf16x8 fx0[2], fx1[2], fwq[2], fwk[2], fwv[2];
      E = early_epoch(pE);
      {
        const u32x4 pz = {~0u, ~0u, ~0u, ~0u};
        if (tid < 136) __builtin_amdgcn_raw_buffer_store_b128(pz, rw, WS_G1 + cb * 24576 + w * 8192 + tb * 1024 + l * 16, 0, AUX_SC1);
      }
      bar();
      STAMP(1);
      fwk[0] = rdtr(aw[0] + 4096, aw[0] + 4352); fx0[0] = rd128(ax[0][0]); fx1[0] = rd128(ax[0][0] + 4096);
      fwv[0] = rdtr(aw[0] + 8192, aw[0] + 8448); fwq[0] = rdtr(aw[0], aw[0] + 256);
      {
        const u32 pa = B_WPHI + (4 * (G >> 1) + tr_r) * 32 + (2 * (G & 1) + (tr_c >> 1)) * 8;
#pragma unroll
        for (int hd = 0; hd < 2; ++hd) wf[hd] = rdtr(pa + hd * 512, pa + hd * 512 + 256);
      }
      sb();
#pragma unroll
      for (int n = 0; n < 32; ++n) {
        const int s = n >> 2, k = n & 3, c = n & 1, m = n + 1, sl = (m >> 2) % 3, hb = sl == 2, so = sl == 1 ? B_SLOT : 0, wo_ = so + (m & 3) * 1024;
        if (k == 0) CST(s);
        kk[0] = mfma(fwk[c], fx0[c], kk[0]); sb();
        if (n == 11) { asm volatile("s_waitcnt vmcnt(0) lgkmcnt(0)\n\ts_barrier" : "+s"(E) :: "memory"); sb(); }
        else if (k == 3 && s < 7) { bar_l(); sb(); }
        if (n < 31) { fwk[c ^ 1] = rdtr(aw[hb] + wo_ + 4096, aw[hb] + wo_ + 4352); sb(); }
        kk[1] = mfma(fwk[c], fx1[c], kk[1]); sb();
        if (n < 31) { fx0[c ^ 1] = rd128(ax[hb][m & 3] + so); fx1[c ^ 1] = rd128(ax[hb][m & 3] + so + 4096); sb(); }
        v[0] = mfma(fx0[c], fwv[c], v[0]); sb();
        if (n < 31) { fwv[c ^ 1] = rdtr(aw[hb] + wo_ + 8192, aw[hb] + wo_ + 8448); sb(); }
        v[1] = mfma(fx1[c], fwv[c], v[1]); sb();
        if (n < 31) { fwq[c ^ 1] = rdtr(aw[hb] + wo_, aw[hb] + wo_ + 256); sb(); }
        q[0] = mfma(fwq[c], fx0[c], q[0]); sb();
        if (n == 11) { tags(); asm volatile("" : "+s"(thi), "+s"(tl32)); sb(); }
        if (n == 12 && tid == 0) __builtin_amdgcn_raw_buffer_store_b64(u32x2{tl32, thi}, rw, WS_PF + (cb * 8 + tb) * 8, 0, AUX_SC1);
        sb();
        q[1] = mfma(fwq[c], fx1[c], q[1]); sb();
      }
      {
        const u32 sgn = ((l >> 2) & 1) ? 0x80008000u : 0u;
#pragma unroll
        for (int hd = 0; hd < 2; ++hd) {
          u32x4 t = __builtin_bit_cast(u32x4, wf[hd]);
          t.x ^= sgn; t.y ^= sgn; t.z ^= sgn; t.w ^= sgn;
          wf[hd] = __builtin_bit_cast(bf16x8, t);
        }
      }
      CST(8);
      STAMP(2);
      f32x16 zero;
#pragma unroll
      for (int i = 0; i < 16; ++i) zero[i] = 0.f;
      const u32 stg = w * 8192;
      {
        const u32 wb = stg + l31 * 64, sw = (l31 >> 1) & 3;
#pragma unroll
        for (int mt = 0; mt < 2; ++mt)
#pragma unroll
          for (int hd = 0; hd < 2; ++hd) {
            const f32x16 p = mfma(wf[hd], pack8(kk[mt], 8 * hd), zero);
            u32x4 o0, o1;
            softmax16(p, o0, o1);
            *(LDS3 u32x4*)(wb + hd * 4096 + mt * 2048 + (((2 * h) ^ sw) << 4)) = o0;
            *(LDS3 u32x4*)(wb + hd * 4096 + mt * 2048 + (((2 * h + 1) ^ sw) << 4)) = o1;
          }
      }
      {
        const u32 ra = stg + (4 * (G >> 1) + tr_r) * 64 + (((2 * (G & 1) + (tr_c >> 1)) ^ (2 * (G >> 1) + (tr_r >> 1))) << 4) + (tr_c & 1) * 8;
        f32x16 kv[2] = {zero, zero};
        const bool lo = l31 < 16;
#pragma unroll
        for (int mt = 0; mt < 2; ++mt)
#pragma unroll
          for (int k2 = 0; k2 < 2; ++k2) {
            const u32x4 vb = __builtin_bit_cast(u32x4, pack8(v[mt], 8 * k2));
            const u32 one = 0x3F803F80u;
            const u32x4 v0 = {lo ? vb.x : one, lo ? vb.y : one, lo ? vb.z : one, lo ? vb.w : one};
            const u32x4 v1 = {lo ? one : vb.x, lo ? one : vb.y, lo ? one : vb.z, lo ? one : vb.w};
            const u32 o = ra + mt * 2048 + k2 * 1024;
            kv[0] = mfma(rdtr(o, o + 512), __builtin_bit_cast(bf16x8, v0), kv[0]);
            kv[1] = mfma(rdtr(o + 4096, o + 4096 + 512), __builtin_bit_cast(bf16x8, v1), kv[1]);
          }
#pragma unroll
        for (int hd = 0; hd < 2; ++hd) {
          const bool own = (l31 >> 4) == hd;
          if (own || (l31 & 15) == 0) {
            const u32 pa = stg + hd * 2448 + (own ? (l31 & 15) : 16) * 144 + h * 16;
#pragma unroll
            for (int qq = 0; qq < 4; ++qq)
              *(LDS3 f32x4*)(pa + qq * 32) = f32x4{kv[hd][4 * qq], kv[hd][4 * qq + 1], kv[hd][4 * qq + 2], kv[hd][4 * qq + 3]};
          }
        }
      }
      STAMP(3);
      bar_l();
      if (w == 0) publish((l >> 2 & 1) * 2448 + 16 * 144 + (l & 3) * 32, l < 8 ? 16384 + l * 16 : 0x80000000u);
      STAMP(4);
#pragma unroll
      for (int mt = 0; mt < 2; ++mt)
#pragma unroll
        for (int hd = 0; hd < 2; ++hd) {
          const f32x16 p = mfma(wf[hd], pack8(q[mt], 8 * hd), zero);
          softmax16(p, pq[mt][hd][0], pq[mt][hd][1]);
        }
      asm volatile("" : "+v"(pq[0][0][0]), "+v"(pq[0][0][1]), "+v"(pq[0][1][0]), "+v"(pq[0][1][1]), "+v"(pq[1][0][0]), "+v"(pq[1][0][1]), "+v"(pq[1][1][0]), "+v"(pq[1][1][1]));
      STAMP(5);
    } else {
      tags();
      wait_flags(rw, WS_PF + cb * 64 + (l & 7) * 8, thi, tl32);
      STAMP(2);
      bar();
      if (w < 6) publish((w & 1) * 2448 + (l >> 2) * 144 + (l & 3) * 32, (w & 1) * 8192 + l * 16);
      STAMP(3);
    }
    if (w == 3 || w >= 6) {
      const u32 off = WS_G1 + cb * 24576 + (w == 3 ? 16384 + (l & 7) * 16 : (w & 1) * 8192 + l * 16);
      u32x4 gq[8];
      const u32x4 rg = srd(a.ws, WS_BYTES);
      u32 pend = 0xffu;
      for (int it = 0; it < SPIN_MAX; ++it) {
        asm volatile(
          "s_bitcmp1_b32 %8, 7\n\ts_cselect_b64 exec, -1, 0\n\tbuffer_load_dwordx4 %0, %9, %10, %11 offen offset:0 sc1\n\t"
          "s_bitcmp1_b32 %8, 6\n\ts_cselect_b64 exec, -1, 0\n\tbuffer_load_dwordx4 %1, %9, %10, %11 offen offset:1024 sc1\n\t"
          "s_bitcmp1_b32 %8, 5\n\ts_cselect_b64 exec, -1, 0\n\tbuffer_load_dwordx4 %2, %9, %10, %11 offen offset:2048 sc1\n\t"
          "s_bitcmp1_b32 %8, 4\n\ts_cselect_b64 exec, -1, 0\n\tbuffer_load_dwordx4 %3, %9, %10, %11 offen offset:3072 sc1\n\t"
          "s_bitcmp1_b32 %8, 3\n\ts_cselect_b64 exec, -1, 0\n\tbuffer_load_dwordx4 %4, %9, %10, %12 offen offset:0 sc1\n\t"
          "s_bitcmp1_b32 %8, 2\n\ts_cselect_b64 exec, -1, 0\n\tbuffer_load_dwordx4 %5, %9, %10, %12 offen offset:1024 sc1\n\t"
          "s_bitcmp1_b32 %8, 1\n\ts_cselect_b64 exec, -1, 0\n\tbuffer_load_dwordx4 %6, %9, %10, %12 offen offset:2048 sc1\n\t"
          "s_bitcmp1_b32 %8, 0\n\ts_cselect_b64 exec, -1, 0\n\tbuffer_load_dwordx4 %7, %9, %10, %12 offen offset:3072 sc1\n\t"
          "s_mov_b64 exec, -1\n\ts_waitcnt vmcnt(0)"
          : "+v"(gq[0]), "+v"(gq[1]), "+v"(gq[2]), "+v"(gq[3]), "+v"(gq[4]), "+v"(gq[5]), "+v"(gq[6]), "+v"(gq[7]), "+s"(pend)
          : "v"(off), "s"(rg), "s"(0u), "s"(4096u) : "scc", "memory");
        pend = 0;
#pragma unroll
        for (int m = 0; m < 8; ++m) {
          const u32 mx = umax_(umax_(umax_(gq[m].x, gq[m].y), gq[m].z), gq[m].w);
          asm volatile("v_cmp_eq_u32 vcc, -1, %1\n\ts_cmp_lg_u64 vcc, 0\n\ts_addc_u32 %0, %0, %0" : "+s"(pend) : "v"(mx) : "vcc", "scc");
        }
        if (pend == 0) break;
      }
      if (w >= 4) STAMP(10);
      float ac[8];
#pragma unroll
      for (int i = 0; i < 8; ++i) ac[i] = 0.f;
#pragma unroll
      for (int m = 0; m < 8; ++m)
#pragma unroll
        for (int i = 0; i < 4; ++i) {
          const u32 t = gq[m][i];
          asm("v_dot2c_f32_bf16 %0, %1, %2" : "+v"(ac[2 * i]) : "s"(0x00003F80u), "v"(t));
          asm("v_dot2c_f32_bf16 %0, %1, %2" : "+v"(ac[2 * i + 1]) : "s"(0x3F800000u), "v"(t));
        }
      asm volatile("s_nop 2" : "+v"(ac[0]), "+v"(ac[1]), "+v"(ac[2]), "+v"(ac[3]), "+v"(ac[4]), "+v"(ac[5]), "+v"(ac[6]), "+v"(ac[7]));
      const u32 ka = B_KVT + (w == 3 ? (((l >> 2) & 1) * 17 + 16) * 64 : ((w & 1) * 17 + (l >> 2)) * 64) + (l & 3) * 16;
      *(LDS3 u32x4*)ka = u32x4{cvtpk(ac[0], ac[1]), cvtpk(ac[2], ac[3]), cvtpk(ac[4], ac[5]), cvtpk(ac[6], ac[7])};
      if (w >= 4) STAMP(4);
    }
    bar_l();
    if (w < 4) {
      STAMP(6);
      {
        const u32 rowd = l31 < 16 ? (8 * ((l31 >> 2) & 1) + 4 * (l31 >> 3) + (l31 & 3)) : 16;
        const u32 ka = B_KVT + rowd * 64 + h * 32;
        f32x16 zero;
#pragma unroll
        for (int i = 0; i < 16; ++i) zero[i] = 0.f;
        const u32 ooff = WS_OB + ((tb * 8 + 2 * w) * 32 + 2 * cb) * 1024 + l * 16;
#pragma unroll
        for (int hd = 0; hd < 2; ++hd) {
          const bf16x8 k0 = rd128(ka + hd * 1088), k1 = rd128(ka + hd * 1088 + 16);
#pragma unroll
          for (int mt = 0; mt < 2; ++mt) {
            f32x16 o = mfma(k0, __builtin_bit_cast(bf16x8, pq[mt][hd][0]), zero);
            o = mfma(k1, __builtin_bit_cast(bf16x8, pq[mt][hd][1]), o);
            const float r = __builtin_amdgcn_rcpf(o[8] + 1e-6f);
            const u32x4 ov = {cvtpk(o[0] * r, o[1] * r), cvtpk(o[2] * r, o[3] * r), cvtpk(o[4] * r, o[5] * r), cvtpk(o[6] * r, o[7] * r)};
            __builtin_amdgcn_raw_buffer_store_b128(ov, rw, ooff + (mt * 32 + hd) * 1024, 0, AUX_SC1);
          }
        }
      }
      WAITV(0);
      if (l == 0) __builtin_amdgcn_raw_buffer_store_b64(u32x2{tl32, thi}, rw, WS_FLAG + 4096 + tb * 512 + w * 128 + cb * 8, 0, AUX_SC1);
      STAMP(7);
      wait_flags(rw, flagoff + wm * 128 + (l & 15) * 8, thi, tl32);
      STAMP(8);
      f32x16 y0, y1;
#pragma unroll
      for (int i = 0; i < 16; ++i) { y0[i] = 0.f; y1[i] = 0.f; }
      {
        const u32 oa = WS_OB + (tt * 4 + w) * 32768 + l * 16;
        const u32 b0a = B_WO + (8 * (G >> 1) + tr_r) * 128 + (((2 * (G & 1) + (tr_c >> 1)) ^ ((tr_r >> 1) << 2)) << 4) + (tr_c & 1) * 8;
        const u32 b1a = b0a ^ 64;
        u32x4 af[8][4];
#pragma unroll
        for (int i = 0; i < 8; ++i) {
          const u32 so = ((ct + i) & 7) * 4096;
#pragma unroll
          for (int k = 0; k < 4; ++k) af[i][k] = __builtin_amdgcn_raw_buffer_load_b128(rw, oa + k * 1024, so, AUX_SC1);
        }
        bf16x8 bq[3][2];
        bq[0][0] = rdtr(b0a, b0a + 512); bq[0][1] = rdtr(b1a, b1a + 512);
        bq[1][0] = rdtr(b0a + 2048, b0a + 2560); bq[1][1] = rdtr(b1a + 2048, b1a + 2560);
        sb();
#pragma unroll
        for (int n = 0; n < 32; ++n) {
          const u32 o = (n + 2) * 2048;
          const bf16x8 av = __builtin_bit_cast(bf16x8, af[n >> 2][n & 3]);
          y0 = mfma(av, bq[n % 3][0], y0); sb();
          if (n + 2 < 32) bq[(n + 2) % 3][0] = rdtr(b0a + o, b0a + o + 512);
          sb();
          y1 = mfma(av, bq[n % 3][1], y1); sb();
          if (n + 2 < 32) bq[(n + 2) % 3][1] = rdtr(b1a + o, b1a + o + 512);
          sb();
        }
      }
      STAMP(9);
      store_out(a.out, y0, y1, 128 * tt + 32 * w, 512 + 64 * ct, l);
      if (tt == 15 && ct == 0 && w == 0 && l == 0) __builtin_amdgcn_raw_buffer_store_b32(E + 1, rw, WS_EPB, 0, AUX_SC1);
      STAMP(10);
    }
  }
}

#define BENCH_LAUNCH(first, last, k, grid, block, shm, st, ...) hipLaunchKernelGGL(k, dim3(grid), dim3(block), shm, st, __VA_ARGS__)

extern "C" void kernel_launch(void* const* d_in, const int* in_sizes, int n_in, void* d_out, int out_size, void* d_ws, size_t ws_size, hipStream_t stream) {
  Args a;
  for (int i = 0; i < 13; ++i) a.in[i] = (const u16*)d_in[i];
  a.out = (float*)d_out;
  a.ws = (unsigned char*)d_ws;
  (void)hipFuncSetAttribute((const void*)fused_k, hipFuncAttributeMaxDynamicSharedMemorySize, 163840);
  BENCH_LAUNCH(true, true, fused_k, 256, 512, 163840, stream, a);
}
```

```cpp
#include <hip/hip_runtime.h>
#include <hip/hip_bf16.h>

#pragma clang diagnostic ignored "-Winline-asm"
typedef unsigned short u16;
typedef unsigned int u32;
typedef unsigned long long u64;
typedef __bf16 bf16x8 __attribute__((ext_vector_type(8)));
typedef __bf16 bf16x2 __attribute__((ext_vector_type(2)));
typedef short s16x4 __attribute__((ext_vector_type(4)));
typedef float f32x2 __attribute__((ext_vector_type(2)));
typedef float f32x4 __attribute__((ext_vector_type(4)));
typedef float f32x16 __attribute__((ext_vector_type(16)));
typedef u32 u32x2 __attribute__((ext_vector_type(2)));
typedef u32 u32x4 __attribute__((ext_vector_type(4)));
#define LDS3 __attribute__((address_space(3)))
#define DI __device__ __forceinline__

#define WS_EPA   0u
#define WS_EPB   256u
#define WS_FLAG  4096u
#define WS_GA    65536u
#define WS_PF    16384u
#define WS_G1    524288u
#define WS_HA    4194304u
#define WS_OB    8388608u
#define WS_BYTES 16777216u
#define WS_DIAG  16777216u
#define AUX_SC1 16
#define SPIN_MAX (1 << 18)

struct Args { const u16* in[13]; float* out; unsigned char* ws; };

#define STAMP(i)

#define CST(i)
#define LST(i)
DI u32 umin_(u32 a, u32 b) { return __builtin_elementwise_min(a, b); }
DI u32 umax_(u32 a, u32 b) { return __builtin_elementwise_max(a, b); }
DI float bf2f(u32 v) { return __uint_as_float(v << 16); }
DI u32 cvtpk(float a, float b) { f32x2 t = {a, b}; return __builtin_bit_cast(u32, __builtin_convertvector(t, bf16x2)); }
DI u32x4 srd(const void* p, u32 bytes) { u64 a = (u64)p; u32x4 r = {(u32)a, (u32)(a >> 32) & 0xffffu, bytes, 0x00020000u}; return r; }
DI void glds(u32 ldsdst, u32 voff, u32x4 rs, u32 soff) {
  asm volatile("s_mov_b32 m0, %0\n\ts_nop 0\n\tbuffer_load_dwordx4 %1, %2, %3 offen lds" :: "s"(ldsdst), "v"(voff), "s"(rs), "s"(soff) : "m0", "memory");
}
DI void gpre(u32 ldsdst, u32 voff, u32x4 rs, u32 soff) {
  asm volatile("s_mov_b32 m0, %0\n\ts_nop 0\n\tbuffer_load_dword %1, %2, %3 offen lds" :: "s"(ldsdst), "v"(voff), "s"(rs), "s"(soff) : "m0", "memory");
}
DI u32 load_epoch(const void* p) { u32 e; asm volatile("s_load_dword %0, %1, 0x0\n\ts_waitcnt lgkmcnt(0)" : "=s"(e) : "s"(p) : "memory"); return e; }
#define WAITV(n) asm volatile("s_waitcnt vmcnt(%0)" :: "n"(n) : "memory")
DI void bar_l() { asm volatile("s_waitcnt lgkmcnt(0)\n\ts_barrier" ::: "memory"); }
DI void bar() { asm volatile("s_barrier" ::: "memory"); }
DI void fence() { asm volatile("" ::: "memory"); }
DI void sb() { __builtin_amdgcn_sched_barrier(0); }
DI bf16x8 rd128(u32 a) { return *(const LDS3 bf16x8*)a; }
DI bf16x8 rdtr(u32 a0, u32 a1) {
  s16x4 x = __builtin_amdgcn_ds_read_tr16_b64_v4i16((LDS3 s16x4*)a0);
  s16x4 y = __builtin_amdgcn_ds_read_tr16_b64_v4i16((LDS3 s16x4*)a1);
  return __builtin_bit_cast(bf16x8, __builtin_shufflevector(x, y, 0, 1, 2, 3, 4, 5, 6, 7));
}
DI f32x16 mfma(bf16x8 a, bf16x8 b, f32x16 c) { return __builtin_amdgcn_mfma_f32_32x32x16_bf16(a, b, c, 0, 0, 0); }
DI f32x2 fma2(f32x2 a, f32x2 b, f32x2 c) { return __builtin_elementwise_fma(a, b, c); }
DI f32x2 bc(float a) { f32x2 r = {a, a}; return r; }
struct cpx { float r, i; };
DI cpx cmul(cpx a, cpx b) { cpx c; c.r = a.r * b.r - a.i * b.i; c.i = a.r * b.i + a.i * b.r; return c; }
DI cpx cfma(cpx a, cpx b, cpx c) { cpx d; d.r = fmaf(a.r, b.r, fmaf(-a.i, b.i, c.r)); d.i = fmaf(a.r, b.i, fmaf(a.i, b.r, c.i)); return d; }
DI cpx csq(cpx a) { return cmul(a, a); }
DI u32x4 gran2(float a, float b, u32 thi, u32 tlo) {
  u32x4 g = {__builtin_amdgcn_alignbit(tlo, __float_as_uint(a), 8), thi, __builtin_amdgcn_alignbit(tlo, __float_as_uint(b), 8), thi};
  return g;
}
DI u32 granbad(u32x4 g, u32 thi, u32 tw) { return ((g.y ^ thi) | (g.w ^ thi)) | (((g.x ^ tw) | (g.z ^ tw)) >> 24); }
DI float granval(u32 lo) { return __uint_as_float(lo << 8); }
DI bf16x8 pack8(const f32x16& v, int o) {
  u32x4 r = {cvtpk(v[o], v[o + 1]), cvtpk(v[o + 2], v[o + 3]), cvtpk(v[o + 4], v[o + 5]), cvtpk(v[o + 6], v[o + 7])};
  return __builtin_bit_cast(bf16x8, r);
}
DI void sincos_cw(float x, float& sn, float& cs) {
  const float k = __builtin_rintf(x * 0.636619772367581343f);
  float r = fmaf(k, -1.57079601287841796875f, x);
  r = fmaf(k, -3.1391647326017846353e-07f, r);
  r = fmaf(k, -5.3903025299577647655e-15f, r);
  const float z = r * r;
  const float s = fmaf(r * z, fmaf(z, fmaf(z, -1.95152959e-4f, 8.33216087e-3f), -1.66666546e-1f), r);
  const float c = fmaf(z, fmaf(z, fmaf(z, fmaf(z, 2.44331571e-5f, -1.38873162e-3f), 4.16666457e-2f), -0.5f), 1.f);
  const int q = (int)k;
  const float ss = (q & 1) ? c : s, cc = (q & 1) ? s : c;
  sn = (q & 2) ? -ss : ss;
  cs = ((q + 1) & 2) ? -cc : cc;
}
DI float exp_acc(float x) {
  const float t = x * 1.44269502162933349609f;
  const float r = fmaf(x, 1.925963033500011e-8f, fmaf(x, 1.44269502162933349609f, -t));
  const float e = __builtin_amdgcn_exp2f(t);
  return fmaf(e, r * 0.693147182464599609f, e);
}
DI u32 early_epoch(const void* p) { u32 e; asm volatile("s_load_dword %0, %1, 0x0" : "=s"(e) : "s"(p) : "memory"); return e; }
DI u32 late_epoch(const void* p) { asm volatile("" : "+s"(p)); return *(const __attribute__((address_space(4))) u32*)p; }
DI void wait_flags(__amdgpu_buffer_rsrc_t rw, u32 off, u32 thi, u32 tl32) {
  for (int it = 0; it < SPIN_MAX; ++it) {
    fence();
    u32x2 f = __builtin_amdgcn_raw_buffer_load_b64(rw, off, 0, AUX_SC1);
    if (__builtin_amdgcn_ballot_w64((f.x != tl32) | (f.y != thi)) == 0) break;
    __builtin_amdgcn_s_sleep(1);
  }
  fence();
}
DI void softmax16(const f32x16& p, u32x4& o0, u32x4& o1) {
  float m = fmaxf(p[0], p[1]);
#pragma unroll
  for (int i = 2; i < 16; i += 2) m = fmaxf(fmaxf(m, p[i]), p[i + 1]);
  const float L2E = 1.44269504088896340736f;
  f32x2 nm = bc(-m * L2E), e[8];
#pragma unroll
  for (int i = 0; i < 8; ++i) {
    f32x2 t = {p[2 * i], p[2 * i + 1]};
    t = fma2(t, bc(L2E), nm);
    e[i].x = __builtin_amdgcn_exp2f(t.x); e[i].y = __builtin_amdgcn_exp2f(t.y);
  }
  f32x2 s = ((e[0] + e[1]) + (e[2] + e[3])) + ((e[4] + e[5]) + (e[6] + e[7]));
  f32x2 r = bc(__builtin_amdgcn_rcpf(s.x + s.y));
  u32 w[8];
#pragma unroll
  for (int i = 0; i < 8; ++i) { f32x2 t = e[i] * r; w[i] = cvtpk(t.x, t.y); }
  o0 = u32x4{w[0], w[1], w[2], w[3]}; o1 = u32x4{w[4], w[5], w[6], w[7]};
}
#define OUT_AUX 16
DI void store_out(float* out, const f32x16& a0, const f32x16& a1, int row0, int col0, int l) {
  const __amdgpu_buffer_rsrc_t ro = __builtin_amdgcn_make_buffer_rsrc((void*)out, 0, 8388608, 0x00020000);
  const u32 p = ((row0 + 4 * (l >> 5)) * 1024 + col0 + (l & 31)) * 4;
#pragma unroll
  for (int r = 0; r < 16; ++r) {
    __builtin_amdgcn_raw_buffer_store_b32(__float_as_uint(a0[r]), ro, p, (8 * (r >> 2) + (r & 3)) * 4096, OUT_AUX);
    __builtin_amdgcn_raw_buffer_store_b32(__float_as_uint(a1[r]), ro, p + 128, (8 * (r >> 2) + (r & 3)) * 4096, OUT_AUX);
  }
}

#define A_SLOT 32768u
#define A_STG  0u
#define A_SEG  16384u
#define A_PART 20480u
#define A_LAM  22528u
#define A_DK   23552u
#define A_PAR  133120u
#define A_PRE  132096u
#define B_SLOT 45056u
#define B_KVT  81920u
#define B_WO   98304u
#define B_WPHI 135168u
#define B_PRE  136192u

__global__ __launch_bounds__(512, 2) void fused_k(Args a) {
  extern __shared__ __attribute__((aligned(16))) char smem_[];
  const int b = blockIdx.x, g = b & 7, jb = b >> 3;
  const int tid = threadIdx.x, l = tid & 63;
  const int w = __builtin_amdgcn_readfirstlane(tid >> 6);
  const bool isA = g >= 4;
  const int tt = 4 * (g & 3) + (jb >> 3), ct = jb & 7;
  const int tb = tt >> 1, cb = jb & 15;
#define ROT 0
  const int s0 = ROT == 0 ? 0 : (ROT == 2 ? ((ct ^ (jb >> 3)) & 1) * 4 : ((ct + (jb >> 3)) & 3) * 2);
  const int h = l >> 5, l31 = l & 31;
  STAMP(0);
  const void* const pE = a.ws + (isA ? WS_EPA : WS_EPB);
  u32 E = 0;
  const __amdgpu_buffer_rsrc_t rw = __builtin_amdgcn_make_buffer_rsrc((void*)a.ws, 0, (int)WS_BYTES, 0x00020000);
  const u32 flagoff = WS_FLAG + (isA ? 0u : 4096u) + tt * 256;

  if (w >= 4) {
    const int w4 = w - 4;
    const u32 lr8 = l >> 3;
    if (isA) {
      const u32x4 rs = srd(w4 < 2 ? a.in[0] : (w4 == 2 ? a.in[3] : a.in[4]), w4 < 2 ? 2097152u : 524288u);
      const u32 lp = w4 < 2 ? (32 * (lr8 >> 2) + 16 * (lr8 & 1) + ((lr8 >> 1) & 1)) : lr8;
      const u32 v0 = lp * 1024 + (((l & 7) ^ (l >> 4)) << 4), v1 = v0 ^ 64;
      const u32 rb = w4 < 2 ? (128 * tt + 64 * w4) : 64 * ct;
      const u32 pa = w4 < 2 ? 2048u : 8192u, pb = w4 < 2 ? 8192u : 32768u;
      auto issue = [&](int i) {
        const u32 so = rb * 1024 + (((s0 + i) & 7) << 7);
        const u32 dst = (i & 3) * A_SLOT + w4 * 8192;
#pragma unroll
        for (int p = 0; p < 8; ++p) glds(dst + p * 1024, (p & 1) ? v1 : v0, rs, so + (p & 3) * pa + (p >> 2) * pb);
      };
      const u32 vp = (w4 < 2 ? 512 * (g & 3) + 16 * jb + 8 * w4 + (l & 7) : 16 * jb + (l & 15)) * 1024 + ((l >> 4) & 1) * 128;
      auto pre = [&](int i) { gpre(A_PRE + w4 * 256, vp, rs, ((s0 + i) & 7) << 7); };
      issue(0); issue(1); pre(2);
      gpre(A_PAR + w4 * 256, l * 4, srd(w4 == 0 ? a.in[1] : (w4 == 1 ? a.in[2] : (w4 == 2 ? a.in[7] : a.in[8])), 1024u), ct * 128);
      const u32x4 rc = srd((w4 & 1) ? a.in[6] : a.in[5], 524288u);
      const u32 vc0 = lr8 * 1024 + (((l & 7) ^ (l >> 4)) << 4), vc1 = vc0 ^ 64;
      auto issueC = [&](int i) {
        const u32 so = 64 * ct * 1024 + (((ct + i) & 7) << 7);
        const u32 dst = (i < 2 ? 131072u + i * 16384u : (i * 16384u)) + (w4 & 1) * 8192;
#pragma unroll
        for (int p = 0; p < 8; ++p) glds(dst + p * 1024, (p & 1) ? vc1 : vc0, rc, so + p * 8192);
      };
      LST(0); WAITV(10); LST(1); bar(); LST(2); pre(4); issue(2);
      LST(3); WAITV(9); LST(4); bar(); LST(5); pre(6); issue(3);
      const float lr = bf2f(*(const LDS3 u16*)(A_PAR + l * 2)), li = bf2f(*(const LDS3 u16*)(A_PAR + 256 + l * 2));
      float dk = bf2f(*(const LDS3 u16*)(A_PAR + 768 + l * 2));
      const float dt = bf2f(cvtpk(exp_acc(bf2f(*(const LDS3 u16*)(A_PAR + 512 + l * 2))), 0.f) & 0xffffu);
      float mg = exp_acc(dt * lr), th = dt * li, rd = __builtin_amdgcn_rcpf(lr * lr + li * li);
      asm volatile("" : "+v"(mg), "+v"(th), "+v"(rd), "+v"(dk));
      LST(6); WAITV(9); LST(7); bar(); LST(8); issue(4);
      gpre(A_PRE + w4 * 256, (16 * jb + 8 * (w4 >> 1) + lr8) * 1024 + (l & 7) * 128, rc, 0);
      float sn, cs;
      sincos_cw(th, sn, cs);
      asm volatile("" : "+v"(sn), "+v"(cs));
      LST(9); WAITV(9); LST(10); bar(); LST(11); issue(5);
      cpx lam = {mg * cs, mg * sn}, coef;
      coef.r = ((lam.r - 1.f) * lr + lam.i * li) * rd; coef.i = (lam.i * lr - (lam.r - 1.f) * li) * rd;
      cpx l16 = csq(csq(csq(csq(lam))));
      asm volatile("" : "+v"(lam.r), "+v"(lam.i), "+v"(coef.r), "+v"(coef.i), "+v"(l16.r), "+v"(l16.i));
      LST(12); WAITV(8); LST(13); bar(); LST(14); issue(6); LST(15);
      cpx l128 = csq(csq(csq(l16)));
      cpx l256 = csq(l128), l512 = csq(l256);
      asm volatile("" : "+v"(l128.r), "+v"(l128.i), "+v"(l256.r), "+v"(l256.i), "+v"(l512.r), "+v"(l512.i));
      WAITV(8); bar(); issue(7);
      if (w4 == 0) { *(LDS3 f32x4*)(A_LAM + l * 16) = f32x4{lam.r, lam.i, coef.r, coef.i}; *(LDS3 float*)(A_DK + l * 4) = dk; }
      asm volatile("s_waitcnt lgkmcnt(0)" ::: "memory");
      WAITV(8); bar(); issueC(w4 >> 1);
      WAITV(8); bar(); issueC(2 + (w4 >> 1));
      E = load_epoch(pE);
      const u64 T = (u64)E * 0x9E3779B97F4A7C15ull + 0x632BE59BD9B4E019ull;
      const u32 thi = (u32)(T >> 32), tlo = (u32)(T >> 24) & 0xffu, tw = tlo << 24;
      STAMP(1);
      bar();
      if (w4 == 3) {
        cpx t = {0.f, 0.f};
#pragma unroll
        for (int s = 0; s < 8; ++s) { f32x2 e = *(const LDS3 f32x2*)(A_SEG + s * 512 + l * 8); t = cfma(l16, t, cpx{e.x, e.y}); }
        __builtin_amdgcn_raw_buffer_store_b128(gran2(t.r, t.i, thi, tlo), rw, WS_GA + (tt * 512 + 64 * ct + l) * 16, 0, AUX_SC1);
      }
      STAMP(2);
      cpx P = {0.f, 0.f};
      if (w4 < tt) {
        const int jl = w4 + ((tt - 1 - w4) & ~3), ex = tt - 1 - jl;
        u32x4 gq[4];
        for (int it = 0; it < SPIN_MAX; ++it) {
          fence();
          u32 bad = 0;
#pragma unroll
          for (int m = 0; m < 4; ++m) {
            const int j = jl - 4 * (3 - m);
            if (j >= 0) gq[m] = __builtin_amdgcn_raw_buffer_load_b128(rw, WS_GA + (64 * ct + l) * 16, j * 8192, AUX_SC1);
            else gq[m] = u32x4{tw, thi, tw, thi};
          }
#pragma unroll
          for (int m = 0; m < 4; ++m) bad |= granbad(gq[m], thi, tw);
          if (__builtin_amdgcn_ballot_w64(bad != 0) == 0) break;
        }
#pragma unroll
        for (int m = 0; m < 4; ++m) P = cfma(l512, P, cpx{granval(gq[m].x), granval(gq[m].z)});
        cpx f = {1.f, 0.f};
        if (ex & 1) f = l128;
        if (ex & 2) f = cmul(f, l256);
        P = cmul(P, f);
      }
      *(LDS3 f32x2*)(A_PART + l * 32 + w4 * 8) = f32x2{P.r, P.i};
      STAMP(3);
      bar_l();
      issueC(4 + (w4 >> 1));
      issueC(6 + (w4 >> 1));
      WAITV(0);
      bar();
      STAMP(4);
    } else {
      const u32x4 rx = srd(a.in[9], 2097152u), rq = srd(a.in[10], 1572864u);
      const u32 vx0 = lr8 * 1024 + (((l & 7) ^ (l >> 4)) << 4), vx1 = vx0 ^ 64;
      const u32 vw = (l >> 2) * 3072 + (l & 3) * 16;
      auto issue = [&](int i) {
        const u32 ks = (s0 + i) & 7;
        const u32 dst = (i % 3) * B_SLOT;
        const u32 sx = (256 * tb + 64 * w4) * 1024 + (ks << 7);
        const u32 sw = ks * 64 * 3072 + 64 * cb;
#pragma unroll
        for (int p = 0; p < 3; ++p) { const u32 pc = 3 * w4 + p; glds(dst + 32768 + pc * 1024, vw, rq, sw + (pc & 3) * 49152 + (pc >> 2) * 1024); }
#pragma unroll
        for (int p = 0; p < 8; ++p) glds(dst + (8 * w4 + p) * 1024, (p & 1) ? vx1 : vx0, rx, sx + p * 8192);
      };
      if (w4 == 0) glds(B_WPHI, l * 16, srd(a.in[11], 16384u), cb * 1024);
      const u32x4 rp = w4 == 0 ? rx : rq;
      const u32 vp = w4 == 0 ? (512 * (g & 3) + 16 * jb + (l & 15)) * 1024 + ((l >> 4) & 1) * 128
                             : (((l >> 4) & 1) * 64 + 2 * jb + ((l >> 3) & 1)) * 3072 + (w4 - 1) * 1024 + (l & 7) * 128;
      const u32 pst = w4 == 0 ? 128u : 64u * 3072u;
      auto pre = [&](int i) { gpre(B_PRE + w4 * 256, vp, rp, ((s0 + i) & 7) * pst); };
      issue(0); issue(1); pre(2);
      const u32x4 ro = srd(a.in[12], 524288u);
      const u32 vo = lr8 * 1024 + (((l & 7) ^ (((l >> 4) & 1) << 2)) << 4);
      auto issueO = [&](int i) {
        const u32 so = (((ct + i) & 7) << 16) + 128 * ct;
#pragma unroll
        for (int p = 0; p < 8; ++p) glds(B_WO + i * 8192 + p * 1024, vo, ro, so + p * 8192);
      };
      WAITV(12); bar(); pre(4); issue(2);
      WAITV(13); bar(); pre(6); issue(3);
      WAITV(12); bar(); issue(4);
      gpre(B_PRE + w4 * 256, (16 * jb + 4 * w4 + (lr8 & 3)) * 1024 + (l & 7) * 128, ro, 0);
      WAITV(12); bar(); issue(5);
      WAITV(11); bar(); issue(6);
      WAITV(11); bar(); issue(7);
      WAITV(11); bar(); issueO(w4);
      WAITV(8); bar(); issueO(4 + w4);
      E = load_epoch(pE);
      STAMP(1);
    }
  }
  u32 thi = 0, tl32 = 0, tlo = 0, tw = 0;
  auto tags = [&]() {
    const u64 T = (u64)E * 0x9E3779B97F4A7C15ull + 0x632BE59BD9B4E019ull;
    thi = (u32)(T >> 32); tl32 = (u32)T; tlo = (u32)(T >> 24) & 0xffu; tw = tlo << 24;
  };
  const int wm = (w >> 1) & 1, wn = w & 1;
  const int G = l >> 4, tr_r = (l >> 2) & 3, tr_c = l & 3;
  const u32 fragrow = l31 * 128;
  const u32 fsw = (l31 >> 1) & 7;

  if (isA) {
    if (w < 4) {
      u32 ax[2][4], aw[2][4];
#pragma unroll
      for (int k = 0; k < 4; ++k) {
        const u32 t = fragrow + (((2 * k + h) ^ fsw) << 4);
        ax[0][k] = t + wm * 8192; aw[0][k] = t + 16384 + wn * 4096;
        ax[1][k] = ax[0][k] + 65536; aw[1][k] = aw[0][k] + 65536;
        asm volatile("" : "+v"(ax[0][k]), "+v"(ax[1][k]), "+v"(aw[0][k]), "+v"(aw[1][k]));
      }
      const u32 rho = 64 * (w >> 1) + 16 * (G >> 1) + 8 * (tr_r >> 1) + 4 * (w & 1) + 2 * (tr_r & 1);
      u32 xs0 = rho * 128 + (((2 * (G & 1) + (tr_c >> 1)) ^ ((rho >> 1) & 7)) << 4) + (tr_c & 1) * 8 + (((ct - s0) & 3) << 15);
      u32 xs1 = xs0 ^ 64;
      asm volatile("" : "+v"(xs0), "+v"(xs1));
      f32x16 aR[2], aI[2];
#pragma unroll
      for (int i = 0; i < 16; ++i) { aR[0][i] = 0.f; aR[1][i] = 0.f; aI[0][i] = 0.f; aI[1][i] = 0.f; }
      s16x4 xk[8];
#pragma unroll
      for (int i = 0; i < 8; ++i) xk[i] = s16x4{0, 0, 0, 0};
      const int sx = (ct - s0) & 7;
      bf16x8 fx0[2], fx1[2], fwr[2], fwi[2];
      bar();
      STAMP(1);
      fx0[0] = rd128(ax[0][0]); fwr[0] = rd128(aw[0][0]); fx1[0] = rd128(ax[0][0] + 4096); fwi[0] = rd128(aw[0][0] + 8192);
      sb();
      f32x4 lc;
#pragma unroll
      for (int n = 0; n < 32; ++n) {
        const int s = n >> 2, k = n & 3, c = n & 1, m = n + 1, hb = (m >> 3) & 1, so = ((m >> 2) & 1) * 32768;
        if (k == 0) CST(s);
        aR[0] = mfma(fx0[c], fwr[c], aR[0]); sb();
        if (k == 3 && s < 7) { bar_l(); sb(); }
        if (n < 31) { fx0[c ^ 1] = rd128(ax[hb][m & 3] + so); fwr[c ^ 1] = rd128(aw[hb][m & 3] + so); sb(); }
        else { lc = *(const LDS3 f32x4*)(A_LAM + (32 * wn + l31) * 16); sb(); }
        aR[1] = mfma(fx1[c], fwr[c], aR[1]); sb();
        if (n < 31) { fx1[c ^ 1] = rd128(ax[hb][m & 3] + so + 4096); fwi[c ^ 1] = rd128(aw[hb][m & 3] + so + 8192); sb(); }
        aI[0] = mfma(fx0[c], fwi[c], aI[0]); sb();
        if (k == 0 && __builtin_expect(sx == s, 0)) {
#pragma unroll
          for (int q = 0; q < 4; ++q) {
            const u32 o = (32 * (q & 1) + (q >> 1)) * 128;
            xk[q] = __builtin_amdgcn_ds_read_tr16_b64_v4i16((LDS3 s16x4*)(o + xs0));
            xk[4 + q] = __builtin_amdgcn_ds_read_tr16_b64_v4i16((LDS3 s16x4*)(o + xs1));
          }
        }
        sb();
        aI[1] = mfma(fx1[c], fwi[c], aI[1]); sb();
      }
      E = early_epoch(pE);
      const cpx lam = {lc.x, lc.y}, coef = {lc.z, lc.w};
      CST(8);
      STAMP(2);
      const f32x2 Lr = bc(lam.r), Li = bc(lam.i), nLi = bc(-lam.i);
      {
        f32x2 hr = {aR[0][0], aR[0][1]}, hi = {aI[0][0], aI[0][1]};
#pragma unroll
        for (int p = 1; p < 16; ++p) {
          const f32x2 ur = {aR[p >> 3][2 * (p & 7)], aR[p >> 3][2 * (p & 7) + 1]}, ui = {aI[p >> 3][2 * (p & 7)], aI[p >> 3][2 * (p & 7) + 1]};
          const f32x2 nr = fma2(Lr, hr, fma2(nLi, hi, ur)), ni = fma2(Lr, hi, fma2(Li, hr, ui));
          hr = nr; hi = ni;
        }
        const u32 sa = A_SEG + (4 * wm + 2 * h) * 512 + (32 * wn + l31) * 8;
        *(LDS3 f32x2*)sa = f32x2{hr.x, hi.x};
        *(LDS3 f32x2*)(sa + 512) = f32x2{hr.y, hi.y};
      }
      asm volatile("s_waitcnt lgkmcnt(0)\n\ts_barrier" : "+s"(E) :: "memory");
      STAMP(3);
      const float dk0 = *(const LDS3 float*)(A_DK + l31 * 4), dk1 = *(const LDS3 float*)(A_DK + 128 + l31 * 4);
      const cpx l16 = csq(csq(csq(csq(lam))));
      const cpx c2 = {2.f * coef.r, 2.f * coef.i};
      cpx pre0 = {0.f, 0.f}, pre1;
      {
        const int sg = 4 * wm + 2 * h;
        cpx e0 = {0.f, 0.f};
#pragma unroll
        for (int s = 0; s < 7; ++s) {
          f32x2 e = *(const LDS3 f32x2*)(A_SEG + s * 512 + (32 * wn + l31) * 8);
          cpx t = cfma(l16, pre0, cpx{e.x, e.y});
          if (s < sg) pre0 = t;
          if (s == sg) e0 = cpx{e.x, e.y};
        }
        pre1 = cfma(l16, pre0, e0);
      }
      pre0 = cmul(c2, pre0); pre1 = cmul(c2, pre1);
      cpx m0 = c2;
      {
        const cpx l32 = csq(l16), l64 = csq(l32);
        if (h) m0 = cmul(m0, l32);
        if (wm) m0 = cmul(m0, l64);
      }
      const cpx m1 = cmul(m0, l16);
#pragma unroll
      for (int p = 0; p < 16; ++p) {
        const f32x2 ur = {aR[p >> 3][2 * (p & 7)], aR[p >> 3][2 * (p & 7) + 1]}, ui = {aI[p >> 3][2 * (p & 7)], aI[p >> 3][2 * (p & 7) + 1]};
        const f32x2 vr = fma2(bc(c2.r), ur, bc(-c2.i) * ui), vi = fma2(bc(c2.r), ui, bc(c2.i) * ur);
        aR[p >> 3][2 * (p & 7)] = vr.x; aR[p >> 3][2 * (p & 7) + 1] = vr.y;
        aI[p >> 3][2 * (p & 7)] = vi.x; aI[p >> 3][2 * (p & 7) + 1] = vi.y;
      }
      f32x16 y0, y1;
#pragma unroll
      for (int q = 0; q < 4; ++q) {
        const u32x2 t0 = __builtin_bit_cast(u32x2, xk[q]), t1 = __builtin_bit_cast(u32x2, xk[4 + q]);
#pragma unroll
        for (int e = 0; e < 2; ++e) {
          const u32 r0 = cvtpk(bf2f(t0[e] & 0xffffu) * dk0, __uint_as_float(t0[e] & 0xffff0000u) * dk0);
          const u32 r1 = cvtpk(bf2f(t1[e] & 0xffffu) * dk1, __uint_as_float(t1[e] & 0xffff0000u) * dk1);
          y0[4 * q + 2 * e] = __uint_as_float(r0 << 16); y0[4 * q + 2 * e + 1] = __uint_as_float(r0 & 0xffff0000u);
          y1[4 * q + 2 * e] = __uint_as_float(r1 << 16); y1[4 * q + 2 * e + 1] = __uint_as_float(r1 & 0xffff0000u);
        }
      }
      asm volatile("" : "+v"(y0), "+v"(y1), "+v"(aR[0]), "+v"(aR[1]), "+v"(aI[0]), "+v"(aI[1]));
      tags();
      asm volatile("" : "+s"(thi), "+s"(tl32));
      STAMP(4);
      bar();
      STAMP(5);
      {
        const f32x4 pa = *(const LDS3 f32x4*)(A_PART + (32 * wn + l31) * 32), pb = *(const LDS3 f32x4*)(A_PART + (32 * wn + l31) * 32 + 16);
        const float cr = (pa.x + pa.z) + (pb.x + pb.z), ci = (pa.y + pa.w) + (pb.y + pb.w);
        const f32x2 Mr = {m0.r, m1.r}, Mi = {m0.i, m1.i};
        f32x2 hr = fma2(Mr, bc(cr), fma2(-Mi, bc(ci), f32x2{pre0.r, pre1.r}));
        f32x2 hi = fma2(Mr, bc(ci), fma2(Mi, bc(cr), f32x2{pre0.i, pre1.i}));
#pragma unroll
        for (int p = 0; p < 16; ++p) {
          const f32x2 ur = {aR[p >> 3][2 * (p & 7)], aR[p >> 3][2 * (p & 7) + 1]}, ui = {aI[p >> 3][2 * (p & 7)], aI[p >> 3][2 * (p & 7) + 1]};
          const f32x2 nr = fma2(Lr, hr, fma2(nLi, hi, ur)), ni = fma2(Lr, hi, fma2(Li, hr, ui));
          hr = nr; hi = ni;
          aR[p >> 3][2 * (p & 7)] = hr.x; aR[p >> 3][2 * (p & 7) + 1] = hr.y;
          aI[p >> 3][2 * (p & 7)] = hi.x; aI[p >> 3][2 * (p & 7) + 1] = hi.y;
        }
      }
      {
        const u32 f = (l31 & 1) | ((l31 & 2) << 1) | ((l31 & 4) >> 1);
        const u32 wb = A_STG + w * 4096 + l31 * 128;
        const u32 chr = 8 * (G >> 1) + tr_r;
        const u32 fr = (tr_r & 1) | ((tr_r & 2) << 1);
        const u32 rb0 = A_STG + w * 4096 + chr * 128 + (tr_c & 1) * 8;
        const u32 cq = 2 * (G & 1) + (tr_c >> 1);
        const u32 hoff = WS_HA + ((tt * 4 + 2 * wm) * 64 + ct * 8 + 2 * wn) * 1024 + l * 16;
#pragma unroll
        for (int ri = 0; ri < 2; ++ri) {
          const f32x16* src = ri ? aI : aR;
          const float sg = ri ? -1.f : 1.f;
#pragma unroll
          for (int c = 0; c < 2; ++c)
#pragma unroll
            for (int i = 0; i < 2; ++i) {
              u32x4 v = {cvtpk(sg * src[i][c], sg * src[i][2 + c]), cvtpk(sg * src[i][4 + c], sg * src[i][6 + c]),
                         cvtpk(sg * src[i][8 + c], sg * src[i][10 + c]), cvtpk(sg * src[i][12 + c], sg * src[i][14 + c])};
              *(LDS3 u32x4*)(wb + (((4 * h + 2 * c + i) ^ f) << 4)) = v;
            }
#pragma unroll
          for (int mt = 0; mt < 2; ++mt)
#pragma unroll
            for (int ks = 0; ks < 2; ++ks) {
              const bf16x8 fr8 = rdtr(rb0 + ks * 2048 + (((4 * mt + cq) ^ fr) << 4), rb0 + ks * 2048 + 512 + (((4 * mt + cq) ^ (fr | 2)) << 4));
              __builtin_amdgcn_raw_buffer_store_b128(__builtin_bit_cast(u32x4, fr8), rw, hoff + (mt * 64 + ri * 4 + ks) * 1024, 0, AUX_SC1);
            }
        }
      }
      STAMP(6);
      WAITV(0);
      if (l == 0) __builtin_amdgcn_raw_buffer_store_b64(u32x2{tl32, thi}, rw, flagoff + wm * 128 + (ct * 2 + wn) * 8, 0, AUX_SC1);
      STAMP(7);
      bar();
      wait_flags(rw, flagoff + wm * 128 + (l & 15) * 8, thi, tl32);
      STAMP(8);
      {
        const u32 ha = WS_HA + (tt * 4 + w) * 65536 + l * 16;
        u32 ca[4], cb[4];
#pragma unroll
        for (int k = 0; k < 4; ++k) { const u32 t = fragrow + (((2 * k + h) ^ fsw) << 4); ca[k] = t + 98304; cb[k] = t + 32768; }
        u32x4 af[4][8];
        auto ld = [&](int i) {
          const u32 so = ((ct + i) & 7) * 8192;
#pragma unroll
          for (int k = 0; k < 8; ++k) af[i & 3][k] = __builtin_amdgcn_raw_buffer_load_b128(rw, ha + k * 1024, so, AUX_SC1);
        };
        auto cad = [&](int n) { const int i = n >> 3, k = n & 7; return (i < 2 ? ca[k & 3] + 32768 + i * 16384 : (i < 6 ? cb[k & 3] + (i - 2) * 16384 : ca[k & 3] + (i - 6) * 16384)) + (k >> 2) * 8192; };
        ld(0); ld(1); ld(2);
        bf16x8 bq[3][2];
        bq[0][0] = rd128(cad(0)); bq[0][1] = rd128(cad(0) + 4096);
        bq[1][0] = rd128(cad(1)); bq[1][1] = rd128(cad(1) + 4096);
        sb();
#pragma unroll
        for (int n = 0; n < 64; ++n) {
          if ((n & 7) == 0) CST(10 + (n >> 3));
          const bf16x8 av = __builtin_bit_cast(bf16x8, af[(n >> 3) & 3][n & 7]);
          y0 = mfma(av, bq[n % 3][0], y0); sb();
          if (n + 2 < 64) bq[(n + 2) % 3][0] = rd128(cad(n + 2));
          if (n + 24 < 64) af[((n >> 3) + 3) & 3][n & 7] = __builtin_amdgcn_raw_buffer_load_b128(rw, ha + (n & 7) * 1024, ((ct + (n >> 3) + 3) & 7) * 8192, AUX_SC1);
          sb();
          y1 = mfma(av, bq[n % 3][1], y1); sb();
          if (n + 2 < 64) bq[(n + 2) % 3][1] = rd128(cad(n + 2) + 4096);
          sb();
        }
      }
      CST(18);
      STAMP(9);
      store_out(a.out, y0, y1, 128 * tt + 32 * w, 64 * ct, l);
      if (tt == 15 && ct == 0 && w == 0 && l == 0) __builtin_amdgcn_raw_buffer_store_b32(E + 1, rw, WS_EPA, 0, AUX_SC1);
      STAMP(10);
    }
  } else {
    u32x4 pq[2][2][2];
    auto publish = [&](u32 ad, u32 o) {
      const f32x4 t0 = (*(const LDS3 f32x4*)ad + *(const LDS3 f32x4*)(ad + 8192)) + (*(const LDS3 f32x4*)(ad + 16384) + *(const LDS3 f32x4*)(ad + 24576));
      const f32x4 t1 = (*(const LDS3 f32x4*)(ad + 16) + *(const LDS3 f32x4*)(ad + 8208)) + (*(const LDS3 f32x4*)(ad + 16400) + *(const LDS3 f32x4*)(ad + 24592));
      const u32x4 r = {umin_(cvtpk(t0.x, t0.y), ~1u), umin_(cvtpk(t0.z, t0.w), ~1u), umin_(cvtpk(t1.x, t1.y), ~1u), umin_(cvtpk(t1.z, t1.w), ~1u)};
      __builtin_amdgcn_raw_buffer_store_b128(r, rw, WS_G1 + cb * 24576 + tb * 1024 + o, 0, AUX_SC1);
    };
    if (w < 4) {
      u32 ax[2][4], aw[2];
#pragma unroll
      for (int k = 0; k < 4; ++k) {
        ax[0][k] = fragrow + (((2 * k + h) ^ fsw) << 4) + w * 8192; ax[1][k] = ax[0][k] + 2 * B_SLOT;
        asm volatile("" : "+v"(ax[0][k]), "+v"(ax[1][k]));
      }
      aw[0] = 32768 + (8 * (G >> 1) + tr_r) * 64 + (2 * (G & 1) + (tr_c >> 1)) * 16 + (tr_c & 1) * 8;
      aw[1] = aw[0] + 2 * B_SLOT;
      asm volatile("" : "+v"(aw[0]), "+v"(aw[1]));
      f32x16 q[2], kk[2], v[2];
      bf16x8 wf[2];
#pragma unroll
      for (int i = 0; i < 16; ++i) { q[0][i] = 0.f; q[1][i] = 0.f; kk[0][i] = 0.f; kk[1][i] = 0.f; v[0][i] = 0.f; v[1][i] = 0.f; }
      bf16x8 fx0[2], fx1[2], fwq[2], fwk[2], fwv[2];
      E = early_epoch(pE);
      {
        const u32x4 pz = {~0u, ~0u, ~0u, ~0u};
        if (tid < 136) __builtin_amdgcn_raw_buffer_store_b128(pz, rw, WS_G1 + cb * 24576 + w * 8192 + tb * 1024 + l * 16, 0, AUX_SC1);
      }
      bar();
      STAMP(1);
      fwk[0] = rdtr(aw[0] + 4096, aw[0] + 4352); fx0[0] = rd128(ax[0][0]); fx1[0] = rd128(ax[0][0] + 4096);
      fwv[0] = rdtr(aw[0] + 8192, aw[0] + 8448); fwq[0] = rdtr(aw[0], aw[0] + 256);
      {
        const u32 pa = B_WPHI + (4 * (G >> 1) + tr_r) * 32 + (2 * (G & 1) + (tr_c >> 1)) * 8;
#pragma unroll
        for (int hd = 0; hd < 2; ++hd) wf[hd] = rdtr(pa + hd * 512, pa + hd * 512 + 256);
      }
      sb();
#pragma unroll
      for (int n = 0; n < 32; ++n) {
        const int s = n >> 2, k = n & 3, c = n & 1, m = n + 1, sl = (m >> 2) % 3, hb = sl == 2, so = sl == 1 ? B_SLOT : 0, wo_ = so + (m & 3) * 1024;
        if (k == 0) CST(s);
        kk[0] = mfma(fwk[c], fx0[c], kk[0]); sb();
        if (n == 11) { asm volatile("s_waitcnt vmcnt(0) lgkmcnt(0)\n\ts_barrier" : "+s"(E) :: "memory"); sb(); }
        else if (k == 3 && s < 7) { bar_l(); sb(); }
        if (n < 31) { fwk[c ^ 1] = rdtr(aw[hb] + wo_ + 4096, aw[hb] + wo_ + 4352); sb(); }
        kk[1] = mfma(fwk[c], fx1[c], kk[1]); sb();
        if (n < 31) { fx0[c ^ 1] = rd128(ax[hb][m & 3] + so); fx1[c ^ 1] = rd128(ax[hb][m & 3] + so + 4096); sb(); }
        v[0] = mfma(fx0[c], fwv[c], v[0]); sb();
        if (n < 31) { fwv[c ^ 1] = rdtr(aw[hb] + wo_ + 8192, aw[hb] + wo_ + 8448); sb(); }
        v[1] = mfma(fx1[c], fwv[c], v[1]); sb();
        if (n < 31) { fwq[c ^ 1] = rdtr(aw[hb] + wo_, aw[hb] + wo_ + 256); sb(); }
        q[0] = mfma(fwq[c], fx0[c], q[0]); sb();
        if (n == 11) { tags(); asm volatile("" : "+s"(thi), "+s"(tl32)); sb(); }
        if (n == 12 && tid == 0) __builtin_amdgcn_raw_buffer_store_b64(u32x2{tl32, thi}, rw, WS_PF + (cb * 8 + tb) * 8, 0, AUX_SC1);
        sb();
        q[1] = mfma(fwq[c], fx1[c], q[1]); sb();
      }
      {
        const u32 sgn = ((l >> 2) & 1) ? 0x80008000u : 0u;
#pragma unroll
        for (int hd = 0; hd < 2; ++hd) {
          u32x4 t = __builtin_bit_cast(u32x4, wf[hd]);
          t.x ^= sgn; t.y ^= sgn; t.z ^= sgn; t.w ^= sgn;
          wf[hd] = __builtin_bit_cast(bf16x8, t);
        }
      }
      CST(8);
      STAMP(2);
      f32x16 zero;
#pragma unroll
      for (int i = 0; i < 16; ++i) zero[i] = 0.f;
      const u32 stg = w * 8192;
      {
        const u32 wb = stg + l31 * 64, sw = (l31 >> 1) & 3;
#pragma unroll
        for (int mt = 0; mt < 2; ++mt)
#pragma unroll
          for (int hd = 0; hd < 2; ++hd) {
            const f32x16 p = mfma(wf[hd], pack8(kk[mt], 8 * hd), zero);
            u32x4 o0, o1;
            softmax16(p, o0, o1);
            *(LDS3 u32x4*)(wb + hd * 4096 + mt * 2048 + (((2 * h) ^ sw) << 4)) = o0;
            *(LDS3 u32x4*)(wb + hd * 4096 + mt * 2048 + (((2 * h + 1) ^ sw) << 4)) = o1;
          }
      }
      {
        const u32 ra = stg + (4 * (G >> 1) + tr_r) * 64 + (((2 * (G & 1) + (tr_c >> 1)) ^ (2 * (G >> 1) + (tr_r >> 1))) << 4) + (tr_c & 1) * 8;
        f32x16 kv[2] = {zero, zero};
        const bool lo = l31 < 16;
#pragma unroll
        for (int mt = 0; mt < 2; ++mt)
#pragma unroll
          for (int k2 = 0; k2 < 2; ++k2) {
            const u32x4 vb = __builtin_bit_cast(u32x4, pack8(v[mt], 8 * k2));
            const u32 one = 0x3F803F80u;
            const u32x4 v0 = {lo ? vb.x : one, lo ? vb.y : one, lo ? vb.z : one, lo ? vb.w : one};
            const u32x4 v1 = {lo ? one : vb.x, lo ? one : vb.y, lo ? one : vb.z, lo ? one : vb.w};
            const u32 o = ra + mt * 2048 + k2 * 1024;
            kv[0] = mfma(rdtr(o, o + 512), __builtin_bit_cast(bf16x8, v0), kv[0]);
            kv[1] = mfma(rdtr(o + 4096, o + 4096 + 512), __builtin_bit_cast(bf16x8, v1), kv[1]);
          }
#pragma unroll
        for (int hd = 0; hd < 2; ++hd) {
          const bool own = (l31 >> 4) == hd;
          if (own || (l31 & 15) == 0) {
            const u32 pa = stg + hd * 2448 + (own ? (l31 & 15) : 16) * 144 + h * 16;
#pragma unroll
            for (int qq = 0; qq < 4; ++qq)
              *(LDS3 f32x4*)(pa + qq * 32) = f32x4{kv[hd][4 * qq], kv[hd][4 * qq + 1], kv[hd][4 * qq + 2], kv[hd][4 * qq + 3]};
          }
        }
      }
      STAMP(3);
      bar_l();
      if (w == 0) publish((l >> 2 & 1) * 2448 + 16 * 144 + (l & 3) * 32, l < 8 ? 16384 + l * 16 : 0x80000000u);
      STAMP(4);
#pragma unroll
      for (int mt = 0; mt < 2; ++mt)
#pragma unroll
        for (int hd = 0; hd < 2; ++hd) {
          const f32x16 p = mfma(wf[hd], pack8(q[mt], 8 * hd), zero);
          softmax16(p, pq[mt][hd][0], pq[mt][hd][1]);
        }
      asm volatile("" : "+v"(pq[0][0][0]), "+v"(pq[0][0][1]), "+v"(pq[0][1][0]), "+v"(pq[0][1][1]), "+v"(pq[1][0][0]), "+v"(pq[1][0][1]), "+v"(pq[1][1][0]), "+v"(pq[1][1][1]));
      STAMP(5);
    } else {
      tags();
      wait_flags(rw, WS_PF + cb * 64 + (l & 7) * 8, thi, tl32);
      STAMP(2);
      bar();
      if (w < 6) publish((w & 1) * 2448 + (l >> 2) * 144 + (l & 3) * 32, (w & 1) * 8192 + l * 16);
      STAMP(3);
    }
    if (w == 3 || w >= 6) {
      const u32 off = WS_G1 + cb * 24576 + (w == 3 ? 16384 + (l & 7) * 16 : (w & 1) * 8192 + l * 16);
      u32x4 gq[8];
      const u32x4 rg = srd(a.ws, WS_BYTES);
      u32 pend = 0xffu;
      for (int it = 0; it < SPIN_MAX; ++it) {
        asm volatile(
          "s_bitcmp1_b32 %8, 7\n\ts_cselect_b64 exec, -1, 0\n\tbuffer_load_dwordx4 %0, %9, %10, %11 offen offset:0 sc1\n\t"
          "s_bitcmp1_b32 %8, 6\n\ts_cselect_b64 exec, -1, 0\n\tbuffer_load_dwordx4 %1, %9, %10, %11 offen offset:1024 sc1\n\t"
          "s_bitcmp1_b32 %8, 5\n\ts_cselect_b64 exec, -1, 0\n\tbuffer_load_dwordx4 %2, %9, %10, %11 offen offset:2048 sc1\n\t"
          "s_bitcmp1_b32 %8, 4\n\ts_cselect_b64 exec, -1, 0\n\tbuffer_load_dwordx4 %3, %9, %10, %11 offen offset:3072 sc1\n\t"
          "s_bitcmp1_b32 %8, 3\n\ts_cselect_b64 exec, -1, 0\n\tbuffer_load_dwordx4 %4, %9, %10, %12 offen offset:0 sc1\n\t"
          "s_bitcmp1_b32 %8, 2\n\ts_cselect_b64 exec, -1, 0\n\tbuffer_load_dwordx4 %5, %9, %10, %12 offen offset:1024 sc1\n\t"
          "s_bitcmp1_b32 %8, 1\n\ts_cselect_b64 exec, -1, 0\n\tbuffer_load_dwordx4 %6, %9, %10, %12 offen offset:2048 sc1\n\t"
          "s_bitcmp1_b32 %8, 0\n\ts_cselect_b64 exec, -1, 0\n\tbuffer_load_dwordx4 %7, %9, %10, %12 offen offset:3072 sc1\n\t"
          "s_mov_b64 exec, -1\n\ts_waitcnt vmcnt(0)"
          : "+v"(gq[0]), "+v"(gq[1]), "+v"(gq[2]), "+v"(gq[3]), "+v"(gq[4]), "+v"(gq[5]), "+v"(gq[6]), "+v"(gq[7]), "+s"(pend)
          : "v"(off), "s"(rg), "s"(0u), "s"(4096u) : "scc", "memory");
        pend = 0;
#pragma unroll
        for (int m = 0; m < 8; ++m) {
          const u32 mx = umax_(umax_(umax_(gq[m].x, gq[m].y), gq[m].z), gq[m].w);
          asm volatile("v_cmp_eq_u32 vcc, -1, %1\n\ts_cmp_lg_u64 vcc, 0\n\ts_addc_u32 %0, %0, %0" : "+s"(pend) : "v"(mx) : "vcc", "scc");
        }
        if (pend == 0) break;
      }
      if (w >= 4) STAMP(10);
      float ac[8];
#pragma unroll
      for (int i = 0; i < 8; ++i) ac[i] = 0.f;
#pragma unroll
      for (int m = 0; m < 8; ++m)
#pragma unroll
        for (int i = 0; i < 4; ++i) {
          const u32 t = gq[m][i];
          asm("v_dot2c_f32_bf16 %0, %1, %2" : "+v"(ac[2 * i]) : "s"(0x00003F80u), "v"(t));
          asm("v_dot2c_f32_bf16 %0, %1, %2" : "+v"(ac[2 * i + 1]) : "s"(0x3F800000u), "v"(t));
        }
      asm volatile("s_nop 2" : "+v"(ac[0]), "+v"(ac[1]), "+v"(ac[2]), "+v"(ac[3]), "+v"(ac[4]), "+v"(ac[5]), "+v"(ac[6]), "+v"(ac[7]));
      const u32 ka = B_KVT + (w == 3 ? (((l >> 2) & 1) * 17 + 16) * 64 : ((w & 1) * 17 + (l >> 2)) * 64) + (l & 3) * 16;
      *(LDS3 u32x4*)ka = u32x4{cvtpk(ac[0], ac[1]), cvtpk(ac[2], ac[3]), cvtpk(ac[4], ac[5]), cvtpk(ac[6], ac[7])};
      if (w >= 4) STAMP(4);
    }
    bar_l();
    if (w < 4) {
      STAMP(6);
      {
        const u32 rowd = l31 < 16 ? (8 * ((l31 >> 2) & 1) + 4 * (l31 >> 3) + (l31 & 3)) : 16;
        const u32 ka = B_KVT + rowd * 64 + h * 32;
        f32x16 zero;
#pragma unroll
        for (int i = 0; i < 16; ++i) zero[i] = 0.f;
        const u32 ooff = WS_OB + ((tb * 8 + 2 * w) * 32 + 2 * cb) * 1024 + l * 16;
#pragma unroll
        for (int hd = 0; hd < 2; ++hd) {
          const bf16x8 k0 = rd128(ka + hd * 1088), k1 = rd128(ka + hd * 1088 + 16);
#pragma unroll
          for (int mt = 0; mt < 2; ++mt) {
            f32x16 o = mfma(k0, __builtin_bit_cast(bf16x8, pq[mt][hd][0]), zero);
            o = mfma(k1, __builtin_bit_cast(bf16x8, pq[mt][hd][1]), o);
            const float r = __builtin_amdgcn_rcpf(o[8] + 1e-6f);
            const u32x4 ov = {cvtpk(o[0] * r, o[1] * r), cvtpk(o[2] * r, o[3] * r), cvtpk(o[4] * r, o[5] * r), cvtpk(o[6] * r, o[7] * r)};
            __builtin_amdgcn_raw_buffer_store_b128(ov, rw, ooff + (mt * 32 + hd) * 1024, 0, AUX_SC1);
          }
        }
      }
      WAITV(0);
      if (l == 0) __builtin_amdgcn_raw_buffer_store_b64(u32x2{tl32, thi}, rw, WS_FLAG + 4096 + tb * 512 + w * 128 + cb * 8, 0, AUX_SC1);
      STAMP(7);
      wait_flags(rw, flagoff + wm * 128 + (l & 15) * 8, thi, tl32);
      STAMP(8);
      f32x16 y0, y1;
#pragma unroll
      for (int i = 0; i < 16; ++i) { y0[i] = 0.f; y1[i] = 0.f; }
      {
        const u32 oa = WS_OB + (tt * 4 + w) * 32768 + l * 16;
        const u32 b0a = B_WO + (8 * (G >> 1) + tr_r) * 128 + (((2 * (G & 1) + (tr_c >> 1)) ^ ((tr_r >> 1) << 2)) << 4) + (tr_c & 1) * 8;
        const u32 b1a = b0a ^ 64;
        u32x4 af[8][4];
#pragma unroll
        for (int i = 0; i < 8; ++i) {
          const u32 so = ((ct + i) & 7) * 4096;
#pragma unroll
          for (int k = 0; k < 4; ++k) af[i][k] = __builtin_amdgcn_raw_buffer_load_b128(rw, oa + k * 1024, so, AUX_SC1);
        }
        bf16x8 bq[3][2];
        bq[0][0] = rdtr(b0a, b0a + 512); bq[0][1] = rdtr(b1a, b1a + 512);
        bq[1][0] = rdtr(b0a + 2048, b0a + 2560); bq[1][1] = rdtr(b1a + 2048, b1a + 2560);
        sb();
#pragma unroll
        for (int n = 0; n < 32; ++n) {
          const u32 o = (n + 2) * 2048;
          const bf16x8 av = __builtin_bit_cast(bf16x8, af[n >> 2][n & 3]);
          y0 = mfma(av, bq[n % 3][0], y0); sb();
          if (n + 2 < 32) bq[(n + 2) % 3][0] = rdtr(b0a + o, b0a + o + 512);
          sb();
          y1 = mfma(av, bq[n % 3][1], y1); sb();
          if (n + 2 < 32) bq[(n + 2) % 3][1] = rdtr(b1a + o, b1a + o + 512);
          sb();
        }
      }
      STAMP(9);
      store_out(a.out, y0, y1, 128 * tt + 32 * w, 512 + 64 * ct, l);
      if (tt == 15 && ct == 0 && w == 0 && l == 0) __builtin_amdgcn_raw_buffer_store_b32(E + 1, rw, WS_EPB, 0, AUX_SC1);
      STAMP(10);
    }
  }
}

#define BENCH_LAUNCH(first, last, k, grid, block, shm, st, ...) hipLaunchKernelGGL(k, dim3(grid), dim3(block), shm, st, __VA_ARGS__)

extern "C" void kernel_launch(void* const* d_in, const int* in_sizes, int n_in, void* d_out, int out_size, void* d_ws, size_t ws_size, hipStream_t stream) {
  Args a;
  for (int i = 0; i < 13; ++i) a.in[i] = (const u16*)d_in[i];
  a.out = (float*)d_out;
  a.ws = (unsigned char*)d_ws;
  (void)hipFuncSetAttribute((const void*)fused_k, hipFuncAttributeMaxDynamicSharedMemorySize, 163840);
  BENCH_LAUNCH(true, true, fused_k, 256, 512, 163840, stream, a);
}
```

```cpp
#include <hip/hip_runtime.h>
#include <hip/hip_bf16.h>

#pragma clang diagnostic ignored "-Winline-asm"
typedef unsigned short u16;
typedef unsigned int u32;
typedef unsigned long long u64;
typedef __bf16 bf16x8 __attribute__((ext_vector_type(8)));
typedef __bf16 bf16x2 __attribute__((ext_vector_type(2)));
typedef short s16x4 __attribute__((ext_vector_type(4)));
typedef float f32x2 __attribute__((ext_vector_type(2)));
typedef float f32x4 __attribute__((ext_vector_type(4)));
typedef float f32x16 __attribute__((ext_vector_type(16)));
typedef u32 u32x2 __attribute__((ext_vector_type(2)));
typedef u32 u32x4 __attribute__((ext_vector_type(4)));
#define LDS3 __attribute__((address_space(3)))
#define DI __device__ __forceinline__
#define KARG __attribute__((address_space(4)))

#define WS_EPA   0u
#define WS_EPB   256u
#define WS_FLAG  4096u
#define WS_GA    65536u
#define WS_PF    16384u
#define WS_G1    524288u
#define WS_HA    4194304u
#define WS_OB    8388608u
#define WS_BYTES 16777216u
#define WS_DIAG  16777216u
#define AUX_SC1 16
#define SPIN_MAX (1 << 18)

struct Args { const u16 *xb, *wqkv, *wphi, *wo; float* out; unsigned char* ws; const u16 *xa, *bre, *bim, *cre, *cim, *lre, *lim, *ldt, *dsk; };

#define STAMP(i)

#define CST(i)
#define LST(i)
DI u32 umin_(u32 a, u32 b) { return __builtin_elementwise_min(a, b); }
DI u32 umax_(u32 a, u32 b) { return __builtin_elementwise_max(a, b); }
DI float bf2f(u32 v) { return __uint_as_float(v << 16); }
DI u32 cvtpk(float a, float b) { f32x2 t = {a, b}; return __builtin_bit_cast(u32, __builtin_convertvector(t, bf16x2)); }
DI u32x4 srd(const void* p, u32 bytes) { u64 a = (u64)p; u32x4 r = {(u32)a, (u32)(a >> 32) & 0xffffu, bytes, 0x00020000u}; return r; }
DI void glds(u32 ldsdst, u32 voff, u32x4 rs, u32 soff) {
  asm volatile("s_mov_b32 m0, %0\n\ts_nop 0\n\tbuffer_load_dwordx4 %1, %2, %3 offen lds" :: "s"(ldsdst), "v"(voff), "s"(rs), "s"(soff) : "m0", "memory");
}
DI void gpre(u32 ldsdst, u32 voff, u32x4 rs, u32 soff) {
  asm volatile("s_mov_b32 m0, %0\n\ts_nop 0\n\tbuffer_load_dword %1, %2, %3 offen lds" :: "s"(ldsdst), "v"(voff), "s"(rs), "s"(soff) : "m0", "memory");
}
DI u32 load_epoch(const void* p) { u32 e; asm volatile("s_load_dword %0, %1, 0x0\n\ts_waitcnt lgkmcnt(0)" : "=s"(e) : "s"(p) : "memory"); return e; }
#define WAITV(n) asm volatile("s_waitcnt vmcnt(%0)" :: "n"(n) : "memory")
DI void bar_l() { asm volatile("s_waitcnt lgkmcnt(0)\n\ts_barrier" ::: "memory"); }
DI void bar() { asm volatile("s_barrier" ::: "memory"); }
DI void fence() { asm volatile("" ::: "memory"); }
DI void sb() { __builtin_amdgcn_sched_barrier(0); }
DI bf16x8 rd128(u32 a) { return *(const LDS3 bf16x8*)a; }
DI bf16x8 rdtr(u32 a0, u32 a1) {
  s16x4 x = __builtin_amdgcn_ds_read_tr16_b64_v4i16((LDS3 s16x4*)a0);
  s16x4 y = __builtin_amdgcn_ds_read_tr16_b64_v4i16((LDS3 s16x4*)a1);
  return __builtin_bit_cast(bf16x8, __builtin_shufflevector(x, y, 0, 1, 2, 3, 4, 5, 6, 7));
}
DI f32x16 mfma(bf16x8 a, bf16x8 b, f32x16 c) { return __builtin_amdgcn_mfma_f32_32x32x16_bf16(a, b, c, 0, 0, 0); }
DI f32x2 fma2(f32x2 a, f32x2 b, f32x2 c) { return __builtin_elementwise_fma(a, b, c); }
DI f32x2 bc(float a) { f32x2 r = {a, a}; return r; }
struct cpx { float r, i; };
DI cpx cmul(cpx a, cpx b) { cpx c; c.r = a.r * b.r - a.i * b.i; c.i = a.r * b.i + a.i * b.r; return c; }
DI cpx cfma(cpx a, cpx b, cpx c) { cpx d; d.r = fmaf(a.r, b.r, fmaf(-a.i, b.i, c.r)); d.i = fmaf(a.r, b.i, fmaf(a.i, b.r, c.i)); return d; }
DI cpx csq(cpx a) { return cmul(a, a); }
DI u32x4 gran2(float a, float b, u32 thi, u32 tlo) {
  u32x4 g = {__builtin_amdgcn_alignbit(tlo, __float_as_uint(a), 8), thi, __builtin_amdgcn_alignbit(tlo, __float_as_uint(b), 8), thi};
  return g;
}
DI u32 granbad(u32x4 g, u32 thi, u32 tw) { return ((g.y ^ thi) | (g.w ^ thi)) | (((g.x ^ tw) | (g.z ^ tw)) >> 24); }
DI float granval(u32 lo) { return __uint_as_float(lo << 8); }
DI bf16x8 pack8(const f32x16& v, int o) {
  u32x4 r = {cvtpk(v[o], v[o + 1]), cvtpk(v[o + 2], v[o + 3]), cvtpk(v[o + 4], v[o + 5]), cvtpk(v[o + 6], v[o + 7])};
  return __builtin_bit_cast(bf16x8, r);
}
DI void sincos_cw(float x, float& sn, float& cs) {
  const float k = __builtin_rintf(x * 0.636619772367581343f);
  float r = fmaf(k, -1.57079601287841796875f, x);
  r = fmaf(k, -3.1391647326017846353e-07f, r);
  r = fmaf(k, -5.3903025299577647655e-15f, r);
  const float z = r * r;
  const float s = fmaf(r * z, fmaf(z, fmaf(z, -1.95152959e-4f, 8.33216087e-3f), -1.66666546e-1f), r);
  const float c = fmaf(z, fmaf(z, fmaf(z, fmaf(z, 2.44331571e-5f, -1.38873162e-3f), 4.16666457e-2f), -0.5f), 1.f);
  const int q = (int)k;
  const float ss = (q & 1) ? c : s, cc = (q & 1) ? s : c;
  sn = (q & 2) ? -ss : ss;
  cs = ((q + 1) & 2) ? -cc : cc;
}
DI float exp_acc(float x) {
  const float t = x * 1.44269502162933349609f;
  const float r = fmaf(x, 1.925963033500011e-8f, fmaf(x, 1.44269502162933349609f, -t));
  const float e = __builtin_amdgcn_exp2f(t);
  return fmaf(e, r * 0.693147182464599609f, e);
}
DI u32 early_epoch(const void* p) { u32 e; asm volatile("s_load_dword %0, %1, 0x0" : "=s"(e) : "s"(p) : "memory"); return e; }
DI u32 late_epoch(const void* p) { asm volatile("" : "+s"(p)); return *(const __attribute__((address_space(4))) u32*)p; }
DI void wait_flags(__amdgpu_buffer_rsrc_t rw, u32 off, u32 thi, u32 tl32) {
  for (int it = 0; it < SPIN_MAX; ++it) {
    fence();
    u32x2 f = __builtin_amdgcn_raw_buffer_load_b64(rw, off, 0, AUX_SC1);
    if (__builtin_amdgcn_ballot_w64((f.x != tl32) | (f.y != thi)) == 0) break;
    __builtin_amdgcn_s_sleep(1);
  }
  fence();
}
DI void softmax16(const f32x16& p, u32x4& o0, u32x4& o1) {
  float m = fmaxf(p[0], p[1]);
#pragma unroll
  for (int i = 2; i < 16; i += 2) m = fmaxf(fmaxf(m, p[i]), p[i + 1]);
  const float L2E = 1.44269504088896340736f;
  f32x2 nm = bc(-m * L2E), e[8];
#pragma unroll
  for (int i = 0; i < 8; ++i) {
    f32x2 t = {p[2 * i], p[2 * i + 1]};
    t = fma2(t, bc(L2E), nm);
    e[i].x = __builtin_amdgcn_exp2f(t.x); e[i].y = __builtin_amdgcn_exp2f(t.y);
  }
  f32x2 s = ((e[0] + e[1]) + (e[2] + e[3])) + ((e[4] + e[5]) + (e[6] + e[7]));
  f32x2 r = bc(__builtin_amdgcn_rcpf(s.x + s.y));
  u32 w[8];
#pragma unroll
  for (int i = 0; i < 8; ++i) { f32x2 t = e[i] * r; w[i] = cvtpk(t.x, t.y); }
  o0 = u32x4{w[0], w[1], w[2], w[3]}; o1 = u32x4{w[4], w[5], w[6], w[7]};
}
#define OUT_AUX 16
DI void store_out(float* out, const f32x16& a0, const f32x16& a1, int row0, int col0, int l) {
  const __amdgpu_buffer_rsrc_t ro = __builtin_amdgcn_make_buffer_rsrc((void*)out, 0, 8388608, 0x00020000);
  const u32 p = ((row0 + 4 * (l >> 5)) * 1024 + col0 + (l & 31)) * 4;
#pragma unroll
  for (int r = 0; r < 16; ++r) {
    __builtin_amdgcn_raw_buffer_store_b32(__float_as_uint(a0[r]), ro, p, (8 * (r >> 2) + (r & 3)) * 4096, OUT_AUX);
    __builtin_amdgcn_raw_buffer_store_b32(__float_as_uint(a1[r]), ro, p + 128, (8 * (r >> 2) + (r & 3)) * 4096, OUT_AUX);
  }
}

#define A_SLOT 32768u
#define A_STG  0u
#define A_SEG  16384u
#define A_PART 20480u
#define A_LAM  22528u
#define A_DK   23552u
#define A_PAR  134144u
#define A_PRE  132096u
#define A_PRE2 24576u
#define B_SLOT 45056u
#define B_KVT  81920u
#define B_WO   98304u
#define B_WPHI 135168u
#define B_PRE  136192u

__global__ __launch_bounds__(512, 2) void fused_k(Args) {
  extern __shared__ __attribute__((aligned(16))) char smem_[];
  const int b = blockIdx.x, g = b & 7, jb = b >> 3;
  const int tid = threadIdx.x, l = tid & 63;
  const int w = __builtin_amdgcn_readfirstlane(tid >> 6);
  const bool isA = g >= 4;
  const KARG Args* ka = (const KARG Args*)__builtin_amdgcn_kernarg_segment_ptr();
  u64 kp, kq;
  asm volatile("s_load_dwordx2 %0, %2, %3\n\ts_load_dwordx2 %1, %2, 0x8" : "=&s"(kp), "=&s"(kq) : "s"(ka), "s"(isA ? (w > 5 ? 8 * w + 8 : 48) : 0));
  const int tt = 4 * (g & 3) + (jb >> 3), ct = jb & 7;
  const int tb = tt >> 1, cb = jb & 15;
#define ROT 0
  const int s0 = ROT == 0 ? 0 : (ROT == 2 ? ((ct ^ (jb >> 3)) & 1) * 4 : ((ct + (jb >> 3)) & 3) * 2);
  const int h = l >> 5, l31 = l & 31;
  STAMP(0);
  unsigned char* ws;
  float* out = nullptr;
#define pE ((const void*)(ws + (isA ? WS_EPA : WS_EPB)))
#define rw __builtin_amdgcn_make_buffer_rsrc((void*)ws, 0, (int)WS_BYTES, 0x00020000)
  u32 E = 0;
  const u32 flagoff = WS_FLAG + (isA ? 0u : 4096u) + tt * 256;

  if (w < 4) {
    u64 pc; u32 len;
    asm volatile("s_getpc_b64 %0\nfk_code_lo:\n\ts_mov_b32 %1, fk_code_hi-fk_code_lo" : "=s"(pc), "=s"(len));
    gpre((isA ? A_PRE : B_PRE) + 1024 + w * 256, ((w + 4 * (jb & 1)) * 64 + l) * 128, srd((const void*)pc, len), 0);
  }
  if (w >= 4) {
    const int w4 = w - 4;
    const u32 lr8 = l >> 3;
    if (isA) {
      const u32 lp = w4 < 2 ? (32 * (lr8 >> 2) + 16 * (lr8 & 1) + ((lr8 >> 1) & 1)) : lr8;
      const u32 rot = w4 < 2 ? ct : 2 * (jb >> 3);
      u32 v0 = (lp * 1024 + (((l & 7) ^ (l >> 4)) << 4)) ^ ((rot & 1) << 6), v1 = v0 ^ 64;
      const u32 rb = w4 < 2 ? (128 * tt + 64 * w4) : 64 * ct;
      const u32 pa = w4 < 2 ? 2048u : 8192u;
      u32 qs[8], qd[8];
#pragma unroll
      for (int p = 0; p < 8; ++p) { const u32 q = (p + rot) & 7; qd[p] = w4 * 8192 + q * 1024; qs[p] = rb * 1024 + q * pa; asm volatile("" : "+s"(qd[p]), "+s"(qs[p])); }
      asm volatile("" : "+v"(v0), "+v"(v1));
      u32 nb = w4 < 2 ? 2097152u : 524288u;
      asm volatile("" : "+s"(nb));
      u64 p0;
      asm volatile("s_waitcnt lgkmcnt(0)\n\ts_mov_b64 %0, %1" : "=&s"(p0) : "s"(kp), "s"(kq));
      const u32x4 rs = srd((const void*)p0, nb);
      auto issue = [&](int i) {
#pragma unroll
        for (int p = 0; p < 8; ++p) glds((i & 3) * A_SLOT + qd[p], (p & 1) ? v1 : v0, rs, qs[p] + (((s0 + i) & 7) << 7));
      };
      issue(0);
      asm volatile("" : "+s"(ka));
      ws = ka->ws;
      issue(1);
      gpre(A_PAR + w4 * 256, l * 4, srd(w4 == 0 ? ka->lre : (w4 == 1 ? ka->lim : (w4 == 2 ? ka->ldt : ka->dsk)), 1024u), ct * 128);
      const u32x4 rc = srd((w4 & 1) ? ka->cim : ka->cre, 524288u);
      const u32 vc0 = lr8 * 1024 + (((l & 7) ^ (l >> 4)) << 4), vc1 = vc0 ^ 64;
      u32 rc2 = 2 * (jb >> 3);
      auto issueC = [&](int i) {
        const u32 so = 64 * ct * 1024 + (((ct + i) & 7) << 7);
        const u32 dst = (i < 2 ? 131072u + i * 16384u : (i * 16384u)) + (w4 & 1) * 8192;
#pragma unroll
        for (int p = 0; p < 8; ++p) { const u32 q = (p + rc2) & 7; glds(dst + q * 1024, (p & 1) ? vc1 : vc0, rc, so + q * 8192); }
      };
      LST(0); WAITV(9); LST(1); bar(); LST(2); issue(2);
      LST(3); WAITV(8); LST(4); bar(); LST(5); issue(3);
      const float lr = bf2f(*(const LDS3 u16*)(A_PAR + l * 2)), li = bf2f(*(const LDS3 u16*)(A_PAR + 256 + l * 2));
      float dk = bf2f(*(const LDS3 u16*)(A_PAR + 768 + l * 2));
      const float dt = bf2f(cvtpk(exp_acc(bf2f(*(const LDS3 u16*)(A_PAR + 512 + l * 2))), 0.f) & 0xffffu);
      float mg = exp_acc(dt * lr), th = dt * li, rd = __builtin_amdgcn_rcpf(lr * lr + li * li);
      asm volatile("" : "+v"(mg), "+v"(th), "+v"(rd), "+v"(dk));
      LST(6); WAITV(8); LST(7); bar(); LST(8); issue(4);
#define CPF 3
      if (CPF == 1) gpre(A_PRE + w4 * 256, (16 * jb + 8 * (w4 >> 1) + lr8) * 1024 + (l & 7) * 128, rc, 0);
      float sn, cs;
      sincos_cw(th, sn, cs);
      asm volatile("" : "+v"(sn), "+v"(cs));
      LST(9); WAITV(8 + (CPF == 1)); LST(10); bar(); LST(11); issue(5);
      cpx lam = {mg * cs, mg * sn}, coef;
      coef.r = ((lam.r - 1.f) * lr + lam.i * li) * rd; coef.i = (lam.i * lr - (lam.r - 1.f) * li) * rd;
      cpx l16 = csq(csq(csq(csq(lam))));
      asm volatile("" : "+v"(lam.r), "+v"(lam.i), "+v"(coef.r), "+v"(coef.i), "+v"(l16.r), "+v"(l16.i));
      LST(12); WAITV(8); LST(13); bar(); LST(14); issue(6); LST(15);
      cpx l128 = csq(csq(csq(l16)));
      cpx l256 = csq(l128), l512 = csq(l256);
      asm volatile("" : "+v"(l128.r), "+v"(l128.i), "+v"(l256.r), "+v"(l256.i), "+v"(l512.r), "+v"(l512.i));
      WAITV(8); bar(); issue(7);
      if (w4 == 0) { *(LDS3 f32x4*)(A_LAM + l * 16) = f32x4{lam.r, lam.i, coef.r, coef.i}; *(LDS3 float*)(A_DK + l * 4) = dk; }
      asm volatile("s_waitcnt lgkmcnt(0)" ::: "memory");
      asm volatile("" : "+s"(rc2));
      WAITV(8); bar(); issueC(w4 >> 1);
      WAITV(8); bar(); issueC(2 + (w4 >> 1));
      if (CPF == 3) gpre(A_PRE2 + w4 * 256, (64 * ct + 16 * (jb >> 3) + 8 * (w4 >> 1) + (l & 7)) * 1024 + ((ct + 4 + (lr8 & 3)) & 7) * 128, rc, 0);
      E = load_epoch(pE);
      const u64 T = (u64)E * 0x9E3779B97F4A7C15ull + 0x632BE59BD9B4E019ull;
      const u32 thi = (u32)(T >> 32), tlo = (u32)(T >> 24) & 0xffu, tw = tlo << 24;
      STAMP(1);
      bar();
      if (w4 == 3) {
        cpx t = {0.f, 0.f};
#pragma unroll
        for (int s = 0; s < 8; ++s) { f32x2 e = *(const LDS3 f32x2*)(A_SEG + s * 512 + l * 8); t = cfma(l16, t, cpx{e.x, e.y}); }
        __builtin_amdgcn_raw_buffer_store_b128(gran2(t.r, t.i, thi, tlo), rw, WS_GA + (tt * 512 + 64 * ct + l) * 16, 0, AUX_SC1);
      }
      STAMP(2);
      cpx P = {0.f, 0.f};
      if (w4 < tt) {
        const int jl = w4 + ((tt - 1 - w4) & ~3), ex = tt - 1 - jl;
        u32x4 gq[4];
        for (int it = 0; it < SPIN_MAX; ++it) {
          fence();
          u32 bad = 0;
#pragma unroll
          for (int m = 0; m < 4; ++m) {
            const int j = jl - 4 * (3 - m);
            if (j >= 0) gq[m] = __builtin_amdgcn_raw_buffer_load_b128(rw, WS_GA + (64 * ct + l) * 16, j * 8192, AUX_SC1);
            else gq[m] = u32x4{tw, thi, tw, thi};
          }
#pragma unroll
          for (int m = 0; m < 4; ++m) bad |= granbad(gq[m], thi, tw);
          if (__builtin_amdgcn_ballot_w64(bad != 0) == 0) break;
        }
#pragma unroll
        for (int m = 0; m < 4; ++m) P = cfma(l512, P, cpx{granval(gq[m].x), granval(gq[m].z)});
        cpx f = {1.f, 0.f};
        if (ex & 1) f = l128;
        if (ex & 2) f = cmul(f, l256);
        P = cmul(P, f);
      }
      *(LDS3 f32x2*)(A_PART + l * 32 + w4 * 8) = f32x2{P.r, P.i};
      STAMP(3);
      bar_l();
      issueC(4 + (w4 >> 1));
      issueC(6 + (w4 >> 1));
      WAITV(0);
      bar();
      STAMP(4);
    } else {
      const u32 rot = cb >> 1, rw3 = (cb & 1) + (tb & 1);
      u32 vx0 = (lr8 * 1024 + (((l & 7) ^ (l >> 4)) << 4)) ^ ((rot & 1) << 6), vx1 = vx0 ^ 64;
      u32 vw = (l >> 2) * 3072 + (l & 3) * 16;
      u32 qs[11], qd[11];
#pragma unroll
      for (int p = 0; p < 8; ++p) { const u32 q = 8 * w4 + ((p + rot) & 7); qd[p] = q * 1024; qs[p] = (256 * tb + 8 * q) * 1024; asm volatile("" : "+s"(qd[p]), "+s"(qs[p])); }
#pragma unroll
      for (int p = 0; p < 3; ++p) { const u32 t = p + rw3, pc = 3 * w4 + (t >= 3 ? t - 3 : t); qd[8 + p] = 32768 + pc * 1024; qs[8 + p] = 64 * cb + (pc & 3) * 49152 + (pc >> 2) * 1024; asm volatile("" : "+s"(qd[8 + p]), "+s"(qs[8 + p])); }
      asm volatile("" : "+v"(vx0), "+v"(vx1), "+v"(vw));
      u64 p0, p1;
      asm volatile("s_waitcnt lgkmcnt(0)\n\ts_mov_b64 %0, %2\n\ts_mov_b64 %1, %3" : "=&s"(p0), "=&s"(p1) : "s"(kp), "s"(kq));
      const u32x4 rx = srd((const void*)p0, 2097152u), rq = srd((const void*)p1, 1572864u);
      auto issue = [&](int i) {
        const u32 ks = (s0 + i) & 7, dst = (i % 3) * B_SLOT;
#pragma unroll
        for (int p = 0; p < 8; ++p) {
          glds(dst + qd[p], (p & 1) ? vx1 : vx0, rx, qs[p] + (ks << 7));
          if (p % 3 == 0) glds(dst + qd[8 + p / 3], vw, rq, qs[8 + p / 3] + ks * 64 * 3072);
        }
      };
      issue(0);
      asm volatile("" : "+s"(ka));
      ws = ka->ws;
      if (w4 == 0) glds(B_WPHI, l * 16, srd(ka->wphi, 16384u), cb * 1024);
      issue(1);
      const u32x4 ro = srd(ka->wo, 524288u);
      const u32 vo = lr8 * 1024 + (((l & 7) ^ (((l >> 4) & 1) << 2)) << 4);
      u32 ro2 = 2 * (jb >> 3);
      auto issueO = [&](int i) {
        const u32 so = (((ct + i) & 7) << 16) + 128 * ct;
#pragma unroll
        for (int p = 0; p < 8; ++p) { const u32 q = (p + ro2) & 7; glds(B_WO + i * 8192 + q * 1024, vo, ro, so + q * 8192); }
      };
#define OPF 0
      WAITV(11); bar(); issue(2);
      WAITV(11); bar(); issue(3);
      WAITV(11); bar(); issue(4);
      if (OPF == 1) gpre(B_PRE + w4 * 256, (16 * jb + 4 * w4 + (lr8 & 3)) * 1024 + (l & 7) * 128, ro, 0);
      if (OPF == 2) gpre(B_PRE + w4 * 256, (16 * jb + 4 * w4 + (lr8 & 3)) * 1024 + (l & 7) * 128 + h * 64, ro, 0);
      WAITV(11 + (OPF > 0)); bar(); issue(5);
      WAITV(11); bar(); issue(6);
      WAITV(11); bar(); issue(7);
      asm volatile("" : "+s"(ro2));
      WAITV(11); bar(); issueO(w4);
      WAITV(8); bar(); issueO(4 + w4);
      E = load_epoch(pE);
      STAMP(1);
    }
  } else {
    ws = ka->ws; out = ka->out;
    asm volatile("s_waitcnt lgkmcnt(0)" : "+s"(ws), "+s"(out) : "s"(kp), "s"(kq));
  }
  u32 thi = 0, tl32 = 0, tlo = 0, tw = 0;
  auto tags = [&]() {
    const u64 T = (u64)E * 0x9E3779B97F4A7C15ull + 0x632BE59BD9B4E019ull;
    thi = (u32)(T >> 32); tl32 = (u32)T; tlo = (u32)(T >> 24) & 0xffu; tw = tlo << 24;
  };
  const int wm = (w >> 1) & 1, wn = w & 1;
  const int G = l >> 4, tr_r = (l >> 2) & 3, tr_c = l & 3;
  const u32 fragrow = l31 * 128;
  const u32 fsw = (l31 >> 1) & 7;

  if (isA) {
    if (w < 4) {
      u32 ax[2][4], aw[2][4];
#pragma unroll
      for (int k = 0; k < 4; ++k) {
        const u32 t = fragrow + (((2 * k + h) ^ fsw) << 4);
        ax[0][k] = t + wm * 8192; aw[0][k] = t + 16384 + wn * 4096;
        ax[1][k] = ax[0][k] + 65536; aw[1][k] = aw[0][k] + 65536;
        asm volatile("" : "+v"(ax[0][k]), "+v"(ax[1][k]), "+v"(aw[0][k]), "+v"(aw[1][k]));
      }
      const u32 rho = 64 * (w >> 1) + 16 * (G >> 1) + 8 * (tr_r >> 1) + 4 * (w & 1) + 2 * (tr_r & 1);
      u32 xs0 = rho * 128 + (((2 * (G & 1) + (tr_c >> 1)) ^ ((rho >> 1) & 7)) << 4) + (tr_c & 1) * 8 + (((ct - s0) & 3) << 15);
      u32 xs1 = xs0 ^ 64;
      asm volatile("" : "+v"(xs0), "+v"(xs1));
      f32x16 aR[2], aI[2];
#pragma unroll
      for (int i = 0; i < 16; ++i) { aR[0][i] = 0.f; aR[1][i] = 0.f; aI[0][i] = 0.f; aI[1][i] = 0.f; }
      s16x4 xk[8];
#pragma unroll
      for (int i = 0; i < 8; ++i) xk[i] = s16x4{0, 0, 0, 0};
      const int sx = (ct - s0) & 7;
      bf16x8 fx0[2], fx1[2], fwr[2], fwi[2];
      bar();
      STAMP(1);
      fx0[0] = rd128(ax[0][0]); fwr[0] = rd128(aw[0][0]); fx1[0] = rd128(ax[0][0] + 4096); fwi[0] = rd128(aw[0][0] + 8192);
      sb();
      f32x4 lc;
#pragma unroll
      for (int n = 0; n < 32; ++n) {
        const int s = n >> 2, k = n & 3, c = n & 1, m = n + 1, hb = (m >> 3) & 1, so = ((m >> 2) & 1) * 32768;
        if (k == 0) CST(s);
        aR[0] = mfma(fx0[c], fwr[c], aR[0]); sb();
        if (n == 15) { asm volatile("s_waitcnt vmcnt(0) lgkmcnt(0)\n\ts_barrier" ::: "memory"); sb(); }
        else if (k == 3 && s < 7) { bar_l(); sb(); }
        if (n < 31) { fx0[c ^ 1] = rd128(ax[hb][m & 3] + so); fwr[c ^ 1] = rd128(aw[hb][m & 3] + so); sb(); }
        else { lc = *(const LDS3 f32x4*)(A_LAM + (32 * wn + l31) * 16); sb(); }
        aR[1] = mfma(fx1[c], fwr[c], aR[1]); sb();
        if (n < 31) { fx1[c ^ 1] = rd128(ax[hb][m & 3] + so + 4096); fwi[c ^ 1] = rd128(aw[hb][m & 3] + so + 8192); sb(); }
        aI[0] = mfma(fx0[c], fwi[c], aI[0]); sb();
        if (k == 0 && __builtin_expect(sx == s, 0)) {
#pragma unroll
          for (int q = 0; q < 4; ++q) {
            const u32 o = (32 * (q & 1) + (q >> 1)) * 128;
            xk[q] = __builtin_amdgcn_ds_read_tr16_b64_v4i16((LDS3 s16x4*)(o + xs0));
            xk[4 + q] = __builtin_amdgcn_ds_read_tr16_b64_v4i16((LDS3 s16x4*)(o + xs1));
          }
        }
        sb();
        aI[1] = mfma(fx1[c], fwi[c], aI[1]); sb();
      }
      E = early_epoch(pE);
      const cpx lam = {lc.x, lc.y}, coef = {lc.z, lc.w};
      CST(8);
      STAMP(2);
      const f32x2 Lr = bc(lam.r), Li = bc(lam.i), nLi = bc(-lam.i);
      {
        f32x2 hr = {aR[0][0], aR[0][1]}, hi = {aI[0][0], aI[0][1]};
#pragma unroll
        for (int p = 1; p < 16; ++p) {
          const f32x2 ur = {aR[p >> 3][2 * (p & 7)], aR[p >> 3][2 * (p & 7) + 1]}, ui = {aI[p >> 3][2 * (p & 7)], aI[p >> 3][2 * (p & 7) + 1]};
          const f32x2 nr = fma2(Lr, hr, fma2(nLi, hi, ur)), ni = fma2(Lr, hi, fma2(Li, hr, ui));
          hr = nr; hi = ni;
        }
        const u32 sa = A_SEG + (4 * wm + 2 * h) * 512 + (32 * wn + l31) * 8;
        *(LDS3 f32x2*)sa = f32x2{hr.x, hi.x};
        *(LDS3 f32x2*)(sa + 512) = f32x2{hr.y, hi.y};
      }
      asm volatile("s_waitcnt lgkmcnt(0)\n\ts_barrier" : "+s"(E) :: "memory");
      STAMP(3);
      const float dk0 = *(const LDS3 float*)(A_DK + l31 * 4), dk1 = *(const LDS3 float*)(A_DK + 128 + l31 * 4);
      const cpx l16 = csq(csq(csq(csq(lam))));
      const cpx c2 = {2.f * coef.r, 2.f * coef.i};
      cpx pre0 = {0.f, 0.f}, pre1;
      {
        const int sg = 4 * wm + 2 * h;
        cpx e0 = {0.f, 0.f};
#pragma unroll
        for (int s = 0; s < 7; ++s) {
          f32x2 e = *(const LDS3 f32x2*)(A_SEG + s * 512 + (32 * wn + l31) * 8);
          cpx t = cfma(l16, pre0, cpx{e.x, e.y});
          if (s < sg) pre0 = t;
          if (s == sg) e0 = cpx{e.x, e.y};
        }
        pre1 = cfma(l16, pre0, e0);
      }
      pre0 = cmul(c2, pre0); pre1 = cmul(c2, pre1);
      cpx m0 = c2;
      {
        const cpx l32 = csq(l16), l64 = csq(l32);
        if (h) m0 = cmul(m0, l32);
        if (wm) m0 = cmul(m0, l64);
      }
      const cpx m1 = cmul(m0, l16);
#pragma unroll
      for (int p = 0; p < 16; ++p) {
        const f32x2 ur = {aR[p >> 3][2 * (p & 7)], aR[p >> 3][2 * (p & 7) + 1]}, ui = {aI[p >> 3][2 * (p & 7)], aI[p >> 3][2 * (p & 7) + 1]};
        const f32x2 vr = fma2(bc(c2.r), ur, bc(-c2.i) * ui), vi = fma2(bc(c2.r), ui, bc(c2.i) * ur);
        aR[p >> 3][2 * (p & 7)] = vr.x; aR[p >> 3][2 * (p & 7) + 1] = vr.y;
        aI[p >> 3][2 * (p & 7)] = vi.x; aI[p >> 3][2 * (p & 7) + 1] = vi.y;
      }
      f32x16 y0, y1;
#pragma unroll
      for (int q = 0; q < 4; ++q) {
        const u32x2 t0 = __builtin_bit_cast(u32x2, xk[q]), t1 = __builtin_bit_cast(u32x2, xk[4 + q]);
#pragma unroll
        for (int e = 0; e < 2; ++e) {
          const u32 r0 = cvtpk(bf2f(t0[e] & 0xffffu) * dk0, __uint_as_float(t0[e] & 0xffff0000u) * dk0);
          const u32 r1 = cvtpk(bf2f(t1[e] & 0xffffu) * dk1, __uint_as_float(t1[e] & 0xffff0000u) * dk1);
          y0[4 * q + 2 * e] = __uint_as_float(r0 << 16); y0[4 * q + 2 * e + 1] = __uint_as_float(r0 & 0xffff0000u);
          y1[4 * q + 2 * e] = __uint_as_float(r1 << 16); y1[4 * q + 2 * e + 1] = __uint_as_float(r1 & 0xffff0000u);
        }
      }
      asm volatile("" : "+v"(y0), "+v"(y1), "+v"(aR[0]), "+v"(aR[1]), "+v"(aI[0]), "+v"(aI[1]));
      tags();
      asm volatile("" : "+s"(thi), "+s"(tl32));
      STAMP(4);
      bar();
      STAMP(5);
      {
        const f32x4 pa = *(const LDS3 f32x4*)(A_PART + (32 * wn + l31) * 32), pb = *(const LDS3 f32x4*)(A_PART + (32 * wn + l31) * 32 + 16);
        const float cr = (pa.x + pa.z) + (pb.x + pb.z), ci = (pa.y + pa.w) + (pb.y + pb.w);
        const f32x2 Mr = {m0.r, m1.r}, Mi = {m0.i, m1.i};
        f32x2 hr = fma2(Mr, bc(cr), fma2(-Mi, bc(ci), f32x2{pre0.r, pre1.r}));
        f32x2 hi = fma2(Mr, bc(ci), fma2(Mi, bc(cr), f32x2{pre0.i, pre1.i}));
#pragma unroll
        for (int p = 0; p < 16; ++p) {
          const f32x2 ur = {aR[p >> 3][2 * (p & 7)], aR[p >> 3][2 * (p & 7) + 1]}, ui = {aI[p >> 3][2 * (p & 7)], aI[p >> 3][2 * (p & 7) + 1]};
          const f32x2 nr = fma2(Lr, hr, fma2(nLi, hi, ur)), ni = fma2(Lr, hi, fma2(Li, hr, ui));
          hr = nr; hi = ni;
          aR[p >> 3][2 * (p & 7)] = hr.x; aR[p >> 3][2 * (p & 7) + 1] = hr.y;
          aI[p >> 3][2 * (p & 7)] = hi.x; aI[p >> 3][2 * (p & 7) + 1] = hi.y;
        }
      }
      {
        const u32 f = (l31 & 1) | ((l31 & 2) << 1) | ((l31 & 4) >> 1);
        const u32 wb = A_STG + w * 4096 + l31 * 128;
        const u32 chr = 8 * (G >> 1) + tr_r;
        const u32 fr = (tr_r & 1) | ((tr_r & 2) << 1);
        const u32 rb0 = A_STG + w * 4096 + chr * 128 + (tr_c & 1) * 8;
        const u32 cq = 2 * (G & 1) + (tr_c >> 1);
        const u32 hoff = WS_HA + ((tt * 4 + 2 * wm) * 64 + ct * 8 + 2 * wn) * 1024 + l * 16;
#pragma unroll
        for (int ri = 0; ri < 2; ++ri) {
          const f32x16* src = ri ? aI : aR;
          const float sg = ri ? -1.f : 1.f;
#pragma unroll
          for (int c = 0; c < 2; ++c)
#pragma unroll
            for (int i = 0; i < 2; ++i) {
              u32x4 v = {cvtpk(sg * src[i][c], sg * src[i][2 + c]), cvtpk(sg * src[i][4 + c], sg * src[i][6 + c]),
                         cvtpk(sg * src[i][8 + c], sg * src[i][10 + c]), cvtpk(sg * src[i][12 + c], sg * src[i][14 + c])};
              *(LDS3 u32x4*)(wb + (((4 * h + 2 * c + i) ^ f) << 4)) = v;
            }
#pragma unroll
          for (int mt = 0; mt < 2; ++mt)
#pragma unroll
            for (int ks = 0; ks < 2; ++ks) {
              const bf16x8 fr8 = rdtr(rb0 + ks * 2048 + (((4 * mt + cq) ^ fr) << 4), rb0 + ks * 2048 + 512 + (((4 * mt + cq) ^ (fr | 2)) << 4));
              __builtin_amdgcn_raw_buffer_store_b128(__builtin_bit_cast(u32x4, fr8), rw, hoff + (mt * 64 + ri * 4 + ks) * 1024, 0, AUX_SC1);
            }
        }
      }
      STAMP(6);
      WAITV(0);
      if (l == 0) __builtin_amdgcn_raw_buffer_store_b64(u32x2{tl32, thi}, rw, flagoff + wm * 128 + (ct * 2 + wn) * 8, 0, AUX_SC1);
      STAMP(7);
      bar();
      wait_flags(rw, flagoff + wm * 128 + (l & 15) * 8, thi, tl32);
      STAMP(8);
      {
        const u32 ha = WS_HA + (tt * 4 + w) * 65536 + l * 16;
        u32 ca[4], cb[4];
#pragma unroll
        for (int k = 0; k < 4; ++k) { const u32 t = fragrow + (((2 * k + h) ^ fsw) << 4); ca[k] = t + 98304; cb[k] = t + 32768; }
        u32x4 af[4][8];
        auto ld = [&](int i) {
          const u32 so = ((ct + i) & 7) * 8192;
#pragma unroll
          for (int k = 0; k < 8; ++k) af[i & 3][k] = __builtin_amdgcn_raw_buffer_load_b128(rw, ha + k * 1024, so, AUX_SC1);
        };
        auto cad = [&](int n) { const int i = n >> 3, k = n & 7; return (i < 2 ? ca[k & 3] + 32768 + i * 16384 : (i < 6 ? cb[k & 3] + (i - 2) * 16384 : ca[k & 3] + (i - 6) * 16384)) + (k >> 2) * 8192; };
        ld(0); ld(1); ld(2);
        bf16x8 bq[3][2];
        bq[0][0] = rd128(cad(0)); bq[0][1] = rd128(cad(0) + 4096);
        bq[1][0] = rd128(cad(1)); bq[1][1] = rd128(cad(1) + 4096);
        sb();
#pragma unroll
        for (int n = 0; n < 64; ++n) {
          if ((n & 7) == 0) CST(10 + (n >> 3));
          const bf16x8 av = __builtin_bit_cast(bf16x8, af[(n >> 3) & 3][n & 7]);
          y0 = mfma(av, bq[n % 3][0], y0); sb();
          if (n + 2 < 64) bq[(n + 2) % 3][0] = rd128(cad(n + 2));
          if (n + 24 < 64) af[((n >> 3) + 3) & 3][n & 7] = __builtin_amdgcn_raw_buffer_load_b128(rw, ha + (n & 7) * 1024, ((ct + (n >> 3) + 3) & 7) * 8192, AUX_SC1);
          sb();
          y1 = mfma(av, bq[n % 3][1], y1); sb();
          if (n + 2 < 64) bq[(n + 2) % 3][1] = rd128(cad(n + 2) + 4096);
          sb();
        }
      }
      CST(18);
      STAMP(9);
      store_out(out, y0, y1, 128 * tt + 32 * w, 64 * ct, l);
      if (tt == 15 && ct == 0 && w == 0 && l == 0) __builtin_amdgcn_raw_buffer_store_b32(E + 1, rw, WS_EPA, 0, AUX_SC1);
      STAMP(10);
    }
  } else {
    u32x4 pq[2][2][2];
    auto publish = [&](u32 ad, u32 o) {
      const f32x4 t0 = (*(const LDS3 f32x4*)ad + *(const LDS3 f32x4*)(ad + 8192)) + (*(const LDS3 f32x4*)(ad + 16384) + *(const LDS3 f32x4*)(ad + 24576));
      const f32x4 t1 = (*(const LDS3 f32x4*)(ad + 16) + *(const LDS3 f32x4*)(ad + 8208)) + (*(const LDS3 f32x4*)(ad + 16400) + *(const LDS3 f32x4*)(ad + 24592));
      const u32x4 r = {umin_(cvtpk(t0.x, t0.y), ~1u), umin_(cvtpk(t0.z, t0.w), ~1u), umin_(cvtpk(t1.x, t1.y), ~1u), umin_(cvtpk(t1.z, t1.w), ~1u)};
      __builtin_amdgcn_raw_buffer_store_b128(r, rw, WS_G1 + cb * 24576 + tb * 1024 + o, 0, AUX_SC1);
    };
    if (w < 4) {
      u32 ax[2][4], aw[2];
#pragma unroll
      for (int k = 0; k < 4; ++k) {
        ax[0][k] = fragrow + (((2 * k + h) ^ fsw) << 4) + w * 8192; ax[1][k] = ax[0][k] + 2 * B_SLOT;
        asm volatile("" : "+v"(ax[0][k]), "+v"(ax[1][k]));
      }
      aw[0] = 32768 + (8 * (G >> 1) + tr_r) * 64 + (2 * (G & 1) + (tr_c >> 1)) * 16 + (tr_c & 1) * 8;
      aw[1] = aw[0] + 2 * B_SLOT;
      asm volatile("" : "+v"(aw[0]), "+v"(aw[1]));
      f32x16 q[2], kk[2], v[2];
      bf16x8 wf[2];
#pragma unroll
      for (int i = 0; i < 16; ++i) { q[0][i] = 0.f; q[1][i] = 0.f; kk[0][i] = 0.f; kk[1][i] = 0.f; v[0][i] = 0.f; v[1][i] = 0.f; }
      bf16x8 fx0[2], fx1[2], fwq[2], fwk[2], fwv[2];
      E = early_epoch(pE);
      {
        const u32x4 pz = {~0u, ~0u, ~0u, ~0u};
        if (tid < 136) __builtin_amdgcn_raw_buffer_store_b128(pz, rw, WS_G1 + cb * 24576 + w * 8192 + tb * 1024 + l * 16, 0, AUX_SC1);
      }
      bar();
      STAMP(1);
      fwk[0] = rdtr(aw[0] + 4096, aw[0] + 4352); fx0[0] = rd128(ax[0][0]); fx1[0] = rd128(ax[0][0] + 4096);
      fwv[0] = rdtr(aw[0] + 8192, aw[0] + 8448); fwq[0] = rdtr(aw[0], aw[0] + 256);
      {
        const u32 pa = B_WPHI + (4 * (G >> 1) + tr_r) * 32 + (2 * (G & 1) + (tr_c >> 1)) * 8;
#pragma unroll
        for (int hd = 0; hd < 2; ++hd) wf[hd] = rdtr(pa + hd * 512, pa + hd * 512 + 256);
      }
      sb();
#pragma unroll
      for (int n = 0; n < 32; ++n) {
        const int s = n >> 2, k = n & 3, c = n & 1, m = n + 1, sl = (m >> 2) % 3, hb = sl == 2, so = sl == 1 ? B_SLOT : 0, wo_ = so + (m & 3) * 1024;
        if (k == 0) CST(s);
        kk[0] = mfma(fwk[c], fx0[c], kk[0]); sb();
        if (n == 11) { asm volatile("s_waitcnt vmcnt(0) lgkmcnt(0)\n\ts_barrier" : "+s"(E) :: "memory"); sb(); }
        else if (k == 3 && s < 7) { bar_l(); sb(); }
        if (n < 31) { fwk[c ^ 1] = rdtr(aw[hb] + wo_ + 4096, aw[hb] + wo_ + 4352); sb(); }
        kk[1] = mfma(fwk[c], fx1[c], kk[1]); sb();
        if (n < 31) { fx0[c ^ 1] = rd128(ax[hb][m & 3] + so); fx1[c ^ 1] = rd128(ax[hb][m & 3] + so + 4096); sb(); }
        v[0] = mfma(fx0[c], fwv[c], v[0]); sb();
        if (n < 31) { fwv[c ^ 1] = rdtr(aw[hb] + wo_ + 8192, aw[hb] + wo_ + 8448); sb(); }
        v[1] = mfma(fx1[c], fwv[c], v[1]); sb();
        if (n < 31) { fwq[c ^ 1] = rdtr(aw[hb] + wo_, aw[hb] + wo_ + 256); sb(); }
        q[0] = mfma(fwq[c], fx0[c], q[0]); sb();
        if (n == 11) { tags(); asm volatile("" : "+s"(thi), "+s"(tl32)); sb(); }
        if (n == 12 && tid == 0) __builtin_amdgcn_raw_buffer_store_b64(u32x2{tl32, thi}, rw, WS_PF + (cb * 8 + tb) * 8, 0, AUX_SC1);
        sb();
        q[1] = mfma(fwq[c], fx1[c], q[1]); sb();
      }
      {
        const u32 sgn = ((l >> 2) & 1) ? 0x80008000u : 0u;
#pragma unroll
        for (int hd = 0; hd < 2; ++hd) {
          u32x4 t = __builtin_bit_cast(u32x4, wf[hd]);
          t.x ^= sgn; t.y ^= sgn; t.z ^= sgn; t.w ^= sgn;
          wf[hd] = __builtin_bit_cast(bf16x8, t);
        }
      }
      CST(8);
      STAMP(2);
      f32x16 zero;
#pragma unroll
      for (int i = 0; i < 16; ++i) zero[i] = 0.f;
      const u32 stg = w * 8192;
      {
        const u32 wb = stg + l31 * 64, sw = (l31 >> 1) & 3;
#pragma unroll
        for (int mt = 0; mt < 2; ++mt)
#pragma unroll
          for (int hd = 0; hd < 2; ++hd) {
            const f32x16 p = mfma(wf[hd], pack8(kk[mt], 8 * hd), zero);
            u32x4 o0, o1;
            softmax16(p, o0, o1);
            *(LDS3 u32x4*)(wb + hd * 4096 + mt * 2048 + (((2 * h) ^ sw) << 4)) = o0;
            *(LDS3 u32x4*)(wb + hd * 4096 + mt * 2048 + (((2 * h + 1) ^ sw) << 4)) = o1;
          }
      }
      {
        const u32 ra = stg + (4 * (G >> 1) + tr_r) * 64 + (((2 * (G & 1) + (tr_c >> 1)) ^ (2 * (G >> 1) + (tr_r >> 1))) << 4) + (tr_c & 1) * 8;
        f32x16 kv[2] = {zero, zero};
        const bool lo = l31 < 16;
#pragma unroll
        for (int mt = 0; mt < 2; ++mt)
#pragma unroll
          for (int k2 = 0; k2 < 2; ++k2) {
            const u32x4 vb = __builtin_bit_cast(u32x4, pack8(v[mt], 8 * k2));
            const u32 one = 0x3F803F80u;
            const u32x4 v0 = {lo ? vb.x : one, lo ? vb.y : one, lo ? vb.z : one, lo ? vb.w : one};
            const u32x4 v1 = {lo ? one : vb.x, lo ? one : vb.y, lo ? one : vb.z, lo ? one : vb.w};
            const u32 o = ra + mt * 2048 + k2 * 1024;
            kv[0] = mfma(rdtr(o, o + 512), __builtin_bit_cast(bf16x8, v0), kv[0]);
            kv[1] = mfma(rdtr(o + 4096, o + 4096 + 512), __builtin_bit_cast(bf16x8, v1), kv[1]);
          }
#pragma unroll
        for (int hd = 0; hd < 2; ++hd) {
          const bool own = (l31 >> 4) == hd;
          if (own || (l31 & 15) == 0) {
            const u32 pa = stg + hd * 2448 + (own ? (l31 & 15) : 16) * 144 + h * 16;
#pragma unroll
            for (int qq = 0; qq < 4; ++qq)
              *(LDS3 f32x4*)(pa + qq * 32) = f32x4{kv[hd][4 * qq], kv[hd][4 * qq + 1], kv[hd][4 * qq + 2], kv[hd][4 * qq + 3]};
          }
        }
      }
      STAMP(3);
      bar_l();
      if (w == 0) publish((l >> 2 & 1) * 2448 + 16 * 144 + (l & 3) * 32, l < 8 ? 16384 + l * 16 : 0x80000000u);
      STAMP(4);
#pragma unroll
      for (int mt = 0; mt < 2; ++mt)
#pragma unroll
        for (int hd = 0; hd < 2; ++hd) {
          const f32x16 p = mfma(wf[hd], pack8(q[mt], 8 * hd), zero);
          softmax16(p, pq[mt][hd][0], pq[mt][hd][1]);
        }
      asm volatile("" : "+v"(pq[0][0][0]), "+v"(pq[0][0][1]), "+v"(pq[0][1][0]), "+v"(pq[0][1][1]), "+v"(pq[1][0][0]), "+v"(pq[1][0][1]), "+v"(pq[1][1][0]), "+v"(pq[1][1][1]));
      STAMP(5);
    } else {
      tags();
      wait_flags(rw, WS_PF + cb * 64 + (l & 7) * 8, thi, tl32);
      STAMP(2);
      bar();
      if (w < 6) publish((w & 1) * 2448 + (l >> 2) * 144 + (l & 3) * 32, (w & 1) * 8192 + l * 16);
      STAMP(3);
    }
    if (w == 3 || w >= 6) {
      const u32 off = WS_G1 + cb * 24576 + (w == 3 ? 16384 + (l & 7) * 16 : (w & 1) * 8192 + l * 16);
      u32x4 gq[8];
      const u32x4 rg = srd(ws, WS_BYTES);
      u32 pend = 0xffu;
      for (int it = 0; it < SPIN_MAX; ++it) {
        asm volatile(
          "s_bitcmp1_b32 %8, 7\n\ts_cselect_b64 exec, -1, 0\n\tbuffer_load_dwordx4 %0, %9, %10, %11 offen offset:0 sc1\n\t"
          "s_bitcmp1_b32 %8, 6\n\ts_cselect_b64 exec, -1, 0\n\tbuffer_load_dwordx4 %1, %9, %10, %11 offen offset:1024 sc1\n\t"
          "s_bitcmp1_b32 %8, 5\n\ts_cselect_b64 exec, -1, 0\n\tbuffer_load_dwordx4 %2, %9, %10, %11 offen offset:2048 sc1\n\t"
          "s_bitcmp1_b32 %8, 4\n\ts_cselect_b64 exec, -1, 0\n\tbuffer_load_dwordx4 %3, %9, %10, %11 offen offset:3072 sc1\n\t"
          "s_bitcmp1_b32 %8, 3\n\ts_cselect_b64 exec, -1, 0\n\tbuffer_load_dwordx4 %4, %9, %10, %12 offen offset:0 sc1\n\t"
          "s_bitcmp1_b32 %8, 2\n\ts_cselect_b64 exec, -1, 0\n\tbuffer_load_dwordx4 %5, %9, %10, %12 offen offset:1024 sc1\n\t"
          "s_bitcmp1_b32 %8, 1\n\ts_cselect_b64 exec, -1, 0\n\tbuffer_load_dwordx4 %6, %9, %10, %12 offen offset:2048 sc1\n\t"
          "s_bitcmp1_b32 %8, 0\n\ts_cselect_b64 exec, -1, 0\n\tbuffer_load_dwordx4 %7, %9, %10, %12 offen offset:3072 sc1\n\t"
          "s_mov_b64 exec, -1\n\ts_waitcnt vmcnt(0)"
          : "+v"(gq[0]), "+v"(gq[1]), "+v"(gq[2]), "+v"(gq[3]), "+v"(gq[4]), "+v"(gq[5]), "+v"(gq[6]), "+v"(gq[7]), "+s"(pend)
          : "v"(off), "s"(rg), "s"(0u), "s"(4096u) : "scc", "memory");
        pend = 0;
#pragma unroll
        for (int m = 0; m < 8; ++m) {
          const u32 mx = umax_(umax_(umax_(gq[m].x, gq[m].y), gq[m].z), gq[m].w);
          asm volatile("v_cmp_eq_u32 vcc, -1, %1\n\ts_cmp_lg_u64 vcc, 0\n\ts_addc_u32 %0, %0, %0" : "+s"(pend) : "v"(mx) : "vcc", "scc");
        }
        if (pend == 0) break;
      }
      if (w >= 4) STAMP(10);
      float ac[8];
#pragma unroll
      for (int i = 0; i < 8; ++i) ac[i] = 0.f;
#pragma unroll
      for (int m = 0; m < 8; ++m)
#pragma unroll
        for (int i = 0; i < 4; ++i) {
          const u32 t = gq[m][i];
          asm("v_dot2c_f32_bf16 %0, %1, %2" : "+v"(ac[2 * i]) : "s"(0x00003F80u), "v"(t));
          asm("v_dot2c_f32_bf16 %0, %1, %2" : "+v"(ac[2 * i + 1]) : "s"(0x3F800000u), "v"(t));
        }
      asm volatile("s_nop 2" : "+v"(ac[0]), "+v"(ac[1]), "+v"(ac[2]), "+v"(ac[3]), "+v"(ac[4]), "+v"(ac[5]), "+v"(ac[6]), "+v"(ac[7]));
      const u32 ka = B_KVT + (w == 3 ? (((l >> 2) & 1) * 17 + 16) * 64 : ((w & 1) * 17 + (l >> 2)) * 64) + (l & 3) * 16;
      *(LDS3 u32x4*)ka = u32x4{cvtpk(ac[0], ac[1]), cvtpk(ac[2], ac[3]), cvtpk(ac[4], ac[5]), cvtpk(ac[6], ac[7])};
      if (w >= 4) STAMP(4);
    }
    bar_l();
    if (w < 4) {
      STAMP(6);
      {
        const u32 rowd = l31 < 16 ? (8 * ((l31 >> 2) & 1) + 4 * (l31 >> 3) + (l31 & 3)) : 16;
        const u32 ka = B_KVT + rowd * 64 + h * 32;
        f32x16 zero;
#pragma unroll
        for (int i = 0; i < 16; ++i) zero[i] = 0.f;
        const u32 ooff = WS_OB + ((tb * 8 + 2 * w) * 32 + 2 * cb) * 1024 + l * 16;
#pragma unroll
        for (int hd = 0; hd < 2; ++hd) {
          const bf16x8 k0 = rd128(ka + hd * 1088), k1 = rd128(ka + hd * 1088 + 16);
#pragma unroll
          for (int mt = 0; mt < 2; ++mt) {
            f32x16 o = mfma(k0, __builtin_bit_cast(bf16x8, pq[mt][hd][0]), zero);
            o = mfma(k1, __builtin_bit_cast(bf16x8, pq[mt][hd][1]), o);
            const float r = __builtin_amdgcn_rcpf(o[8] + 1e-6f);
            const u32x4 ov = {cvtpk(o[0] * r, o[1] * r), cvtpk(o[2] * r, o[3] * r), cvtpk(o[4] * r, o[5] * r), cvtpk(o[6] * r, o[7] * r)};
            __builtin_amdgcn_raw_buffer_store_b128(ov, rw, ooff + (mt * 32 + hd) * 1024, 0, AUX_SC1);
          }
        }
      }
      WAITV(0);
      if (l == 0) __builtin_amdgcn_raw_buffer_store_b64(u32x2{tl32, thi}, rw, WS_FLAG + 4096 + tb * 512 + w * 128 + cb * 8, 0, AUX_SC1);
      STAMP(7);
      wait_flags(rw, flagoff + wm * 128 + (l & 15) * 8, thi, tl32);
      STAMP(8);
      f32x16 y0, y1;
#pragma unroll
      for (int i = 0; i < 16; ++i) { y0[i] = 0.f; y1[i] = 0.f; }
      {
        const u32 oa = WS_OB + (tt * 4 + w) * 32768 + l * 16;
        const u32 b0a = B_WO + (8 * (G >> 1) + tr_r) * 128 + (((2 * (G & 1) + (tr_c >> 1)) ^ ((tr_r >> 1) << 2)) << 4) + (tr_c & 1) * 8;
        const u32 b1a = b0a ^ 64;
        u32x4 af[8][4];
#pragma unroll
        for (int i = 0; i < 8; ++i) {
          const u32 so = ((ct + i) & 7) * 4096;
#pragma unroll
          for (int k = 0; k < 4; ++k) af[i][k] = __builtin_amdgcn_raw_buffer_load_b128(rw, oa + k * 1024, so, AUX_SC1);
        }
        bf16x8 bq[3][2];
        bq[0][0] = rdtr(b0a, b0a + 512); bq[0][1] = rdtr(b1a, b1a + 512);
        bq[1][0] = rdtr(b0a + 2048, b0a + 2560); bq[1][1] = rdtr(b1a + 2048, b1a + 2560);
        sb();
#pragma unroll
        for (int n = 0; n < 32; ++n) {
          const u32 o = (n + 2) * 2048;
          const bf16x8 av = __builtin_bit_cast(bf16x8, af[n >> 2][n & 3]);
          y0 = mfma(av, bq[n % 3][0], y0); sb();
          if (n + 2 < 32) bq[(n + 2) % 3][0] = rdtr(b0a + o, b0a + o + 512);
          sb();
          y1 = mfma(av, bq[n % 3][1], y1); sb();
          if (n + 2 < 32) bq[(n + 2) % 3][1] = rdtr(b1a + o, b1a + o + 512);
          sb();
        }
      }
      STAMP(9);
      store_out(out, y0, y1, 128 * tt + 32 * w, 512 + 64 * ct, l);
      if (tt == 15 && ct == 0 && w == 0 && l == 0) __builtin_amdgcn_raw_buffer_store_b32(E + 1, rw, WS_EPB, 0, AUX_SC1);
      STAMP(10);
    }
  }
  asm volatile("fk_code_hi:");
}

#define BENCH_LAUNCH(first, last, k, grid, block, shm, st, ...) hipLaunchKernelGGL(k, dim3(grid), dim3(block), shm, st, __VA_ARGS__)

extern "C" void kernel_launch(void* const* d_in, const int* in_sizes, int n_in, void* d_out, int out_size, void* d_ws, size_t ws_size, hipStream_t stream) {
  Args a;
  a.xa = (const u16*)d_in[0];
  a.lre = (const u16*)d_in[1];
  a.lim = (const u16*)d_in[2];
  a.bre = (const u16*)d_in[3];
  a.bim = (const u16*)d_in[4];
  a.cre = (const u16*)d_in[5];
  a.cim = (const u16*)d_in[6];
  a.ldt = (const u16*)d_in[7];
  a.dsk = (const u16*)d_in[8];
  a.xb = (const u16*)d_in[9];
  a.wqkv = (const u16*)d_in[10];
  a.wphi = (const u16*)d_in[11];
  a.wo = (const u16*)d_in[12];
  a.out = (float*)d_out;
  a.ws = (unsigned char*)d_ws;
  (void)hipFuncSetAttribute((const void*)fused_k, hipFuncAttributeMaxDynamicSharedMemorySize, 163840);
  BENCH_LAUNCH(true, true, fused_k, 256, 512, 163840, stream, a);
}
```

```cpp
#include <hip/hip_runtime.h>
#include <hip/hip_bf16.h>

#pragma clang diagnostic ignored "-Winline-asm"
typedef unsigned short u16;
typedef unsigned int u32;
typedef unsigned long long u64;
typedef __bf16 bf16x8 __attribute__((ext_vector_type(8)));
typedef __bf16 bf16x2 __attribute__((ext_vector_type(2)));
typedef short s16x4 __attribute__((ext_vector_type(4)));
typedef float f32x2 __attribute__((ext_vector_type(2)));
typedef float f32x4 __attribute__((ext_vector_type(4)));
typedef float f32x16 __attribute__((ext_vector_type(16)));
typedef u32 u32x2 __attribute__((ext_vector_type(2)));
typedef u32 u32x4 __attribute__((ext_vector_type(4)));
#define LDS3 __attribute__((address_space(3)))
#define DI __device__ __forceinline__
#define KARG __attribute__((address_space(4)))

#define WS_EPA   0u
#define WS_EPB   256u
#define FS 256u
#define PSL 1
#define PDA 6
#define PD 12
#define WS_FLAG  1048576u
#define WS_GA    65536u
#define WS_PF    16384u
#define WS_G1    524288u
#define WS_HA    4194304u
#define WS_OB    8388608u
#define WS_BYTES 16777216u
#define WS_DIAG  16777216u
#define AUX_SC1 16
#define SPIN_MAX (1 << 18)

struct Args { const u16 *xb, *wqkv, *wphi, *wo; float* out; unsigned char* ws; const u16 *xa, *bre, *bim, *cre, *cim, *lre, *lim, *ldt, *dsk; };

#define STAMP(i)

#define CST(i)
#define LST(i)
DI u32 umin_(u32 a, u32 b) { return __builtin_elementwise_min(a, b); }
DI u32 umax_(u32 a, u32 b) { return __builtin_elementwise_max(a, b); }
DI float bf2f(u32 v) { return __uint_as_float(v << 16); }
DI u32 cvtpk(float a, float b) { f32x2 t = {a, b}; return __builtin_bit_cast(u32, __builtin_convertvector(t, bf16x2)); }
DI u32x4 srd(const void* p, u32 bytes) { u64 a = (u64)p; u32x4 r = {(u32)a, (u32)(a >> 32) & 0xffffu, bytes, 0x00020000u}; return r; }
DI void glds(u32 ldsdst, u32 voff, u32x4 rs, u32 soff) {
  asm volatile("s_mov_b32 m0, %0\n\ts_nop 0\n\tbuffer_load_dwordx4 %1, %2, %3 offen lds" :: "s"(ldsdst), "v"(voff), "s"(rs), "s"(soff) : "m0", "memory");
}
DI void gpre(u32 ldsdst, u32 voff, u32x4 rs, u32 soff) {
  asm volatile("s_mov_b32 m0, %0\n\ts_nop 0\n\tbuffer_load_dword %1, %2, %3 offen lds" :: "s"(ldsdst), "v"(voff), "s"(rs), "s"(soff) : "m0", "memory");
}
DI u32 load_epoch(const void* p) { u32 e; asm volatile("s_load_dword %0, %1, 0x0\n\ts_waitcnt lgkmcnt(0)" : "=s"(e) : "s"(p) : "memory"); return e; }
#define WAITV(n) asm volatile("s_waitcnt vmcnt(%0)" :: "n"(n) : "memory")
DI void bar_l() { asm volatile("s_waitcnt lgkmcnt(0)\n\ts_barrier" ::: "memory"); }
DI void bar() { asm volatile("s_barrier" ::: "memory"); }
DI void fence() { asm volatile("" ::: "memory"); }
DI void sb() { __builtin_amdgcn_sched_barrier(0); }
DI bf16x8 rd128(u32 a) { return *(const LDS3 bf16x8*)a; }
DI bf16x8 rdtr(u32 a0, u32 a1) {
  s16x4 x = __builtin_amdgcn_ds_read_tr16_b64_v4i16((LDS3 s16x4*)a0);
  s16x4 y = __builtin_amdgcn_ds_read_tr16_b64_v4i16((LDS3 s16x4*)a1);
  return __builtin_bit_cast(bf16x8, __builtin_shufflevector(x, y, 0, 1, 2, 3, 4, 5, 6, 7));
}
DI f32x16 mfma(bf16x8 a, bf16x8 b, f32x16 c) { return __builtin_amdgcn_mfma_f32_32x32x16_bf16(a, b, c, 0, 0, 0); }
DI f32x2 fma2(f32x2 a, f32x2 b, f32x2 c) { return __builtin_elementwise_fma(a, b, c); }
DI f32x2 bc(float a) { f32x2 r = {a, a}; return r; }
struct cpx { float r, i; };
DI cpx cmul(cpx a, cpx b) { cpx c; c.r = a.r * b.r - a.i * b.i; c.i = a.r * b.i + a.i * b.r; return c; }
DI cpx cfma(cpx a, cpx b, cpx c) { cpx d; d.r = fmaf(a.r, b.r, fmaf(-a.i, b.i, c.r)); d.i = fmaf(a.r, b.i, fmaf(a.i, b.r, c.i)); return d; }
DI cpx csq(cpx a) { return cmul(a, a); }
DI u32x4 gran2(float a, float b, u32 thi, u32 tlo) {
  u32x4 g = {__builtin_amdgcn_alignbit(tlo, __float_as_uint(a), 8), thi, __builtin_amdgcn_alignbit(tlo, __float_as_uint(b), 8), thi};
  return g;
}
DI u32 granbad(u32x4 g, u32 thi, u32 tw) { return ((g.y ^ thi) | (g.w ^ thi)) | (((g.x ^ tw) | (g.z ^ tw)) >> 24); }
DI float granval(u32 lo) { return __uint_as_float(lo << 8); }
DI bf16x8 pack8(const f32x16& v, int o) {
  u32x4 r = {cvtpk(v[o], v[o + 1]), cvtpk(v[o + 2], v[o + 3]), cvtpk(v[o + 4], v[o + 5]), cvtpk(v[o + 6], v[o + 7])};
  return __builtin_bit_cast(bf16x8, r);
}
DI void sincos_cw(float x, float& sn, float& cs) {
  const float k = __builtin_rintf(x * 0.636619772367581343f);
  float r = fmaf(k, -1.57079601287841796875f, x);
  r = fmaf(k, -3.1391647326017846353e-07f, r);
  r = fmaf(k, -5.3903025299577647655e-15f, r);
  const float z = r * r;
  const float s = fmaf(r * z, fmaf(z, fmaf(z, -1.95152959e-4f, 8.33216087e-3f), -1.66666546e-1f), r);
  const float c = fmaf(z, fmaf(z, fmaf(z, fmaf(z, 2.44331571e-5f, -1.38873162e-3f), 4.16666457e-2f), -0.5f), 1.f);
  const int q = (int)k;
  const float ss = (q & 1) ? c : s, cc = (q & 1) ? s : c;
  sn = (q & 2) ? -ss : ss;
  cs = ((q + 1) & 2) ? -cc : cc;
}
DI float exp_acc(float x) {
  const float t = x * 1.44269502162933349609f;
  const float r = fmaf(x, 1.925963033500011e-8f, fmaf(x, 1.44269502162933349609f, -t));
  const float e = __builtin_amdgcn_exp2f(t);
  return fmaf(e, r * 0.693147182464599609f, e);
}
DI u32 early_epoch(const void* p) { u32 e; asm volatile("s_load_dword %0, %1, 0x0" : "=s"(e) : "s"(p) : "memory"); return e; }
DI u32 late_epoch(const void* p) { asm volatile("" : "+s"(p)); return *(const __attribute__((address_space(4))) u32*)p; }
DI void wait_flags(__amdgpu_buffer_rsrc_t rw, u32 off, u32 thi, u32 tl32) {
  for (int it = 0; it < SPIN_MAX; ++it) {
    fence();
    u32x2 f = __builtin_amdgcn_raw_buffer_load_b64(rw, off, 0, AUX_SC1);
    if (__builtin_amdgcn_ballot_w64((f.x != tl32) | (f.y != thi)) == 0) break;
    __builtin_amdgcn_s_sleep(PSL);
  }
  fence();
}
DI void scan_step(int p, f32x2& hr, f32x2& hi, f32x2 ur, f32x2 ui, f32x2 Lr, f32x2 La, f32x2 Lb) {
  f32x2 a, b;
  if (p & 1) { a = fma2(La, hi, ur); sb(); b = fma2(Lb, hr, ui); sb(); hr = fma2(Lr, hr, a); sb(); hi = fma2(Lr, hi, b); sb(); }
  else       { b = fma2(Lb, hr, ui); sb(); a = fma2(La, hi, ur); sb(); hi = fma2(Lr, hi, b); sb(); hr = fma2(Lr, hr, a); sb(); }
}
DI float mx3(float a, float b, float c) { return __builtin_elementwise_maximum(__builtin_elementwise_maximum(a, b), c); }
template <class F> DI void softmax2(const f32x16& pa, const f32x16& pb, u32x4* oa, u32x4* ob, F hook) {
  const float L2E = 1.44269504088896340736f;
  const f32x16* p[2] = {&pa, &pb};
  float m[2][5];
  f32x2 nm[2], e[2][8], r[2];
#pragma unroll
  for (int i = 0; i < 5; ++i)
#pragma unroll
    for (int j = 0; j < 2; ++j) m[j][i] = mx3((*p[j])[3 * i], (*p[j])[3 * i + 1], (*p[j])[3 * i + 2]);
  sb();
#pragma unroll
  for (int j = 0; j < 2; ++j) { m[j][0] = mx3(m[j][0], m[j][1], m[j][2]); m[j][3] = mx3(m[j][3], m[j][4], (*p[j])[15]); }
  sb();
#pragma unroll
  for (int j = 0; j < 2; ++j) m[j][0] = __builtin_elementwise_maximum(m[j][0], m[j][3]);
  asm volatile("" : "+v"(m[0][0]), "+v"(m[1][0]));
  hook(0);
  sb();
#pragma unroll
  for (int j = 0; j < 2; ++j) nm[j] = bc(-m[j][0] * L2E);
  sb();
#pragma unroll
  for (int i = 0; i < 8; i += 2) {
#pragma unroll
    for (int j = 0; j < 4; ++j) e[j & 1][i + (j >> 1)] = fma2(f32x2{(*p[j & 1])[2 * i + (j & 2)], (*p[j & 1])[2 * i + (j & 2) + 1]}, bc(L2E), nm[j & 1]);
    asm volatile("" : "+v"(e[0][i]), "+v"(e[1][i]), "+v"(e[0][i + 1]), "+v"(e[1][i + 1]));
    hook(10 + (i >> 1));
    sb();
#pragma unroll
    for (int j = 0; j < 4; ++j) { f32x2& t = e[j & 1][i + (j >> 1)]; t.x = __builtin_amdgcn_exp2f(t.x); t.y = __builtin_amdgcn_exp2f(t.y); }
    asm volatile("" : "+v"(e[0][i]), "+v"(e[1][i]), "+v"(e[0][i + 1]), "+v"(e[1][i + 1]));
    hook(1 + (i >> 1));
    sb();
  }
  f32x2 s[2][4];
#pragma unroll
  for (int i = 0; i < 4; ++i)
#pragma unroll
    for (int j = 0; j < 2; ++j) s[j][i] = e[j][2 * i] + e[j][2 * i + 1];
  sb();
#pragma unroll
  for (int j = 0; j < 2; ++j) { s[j][0] += s[j][1]; s[j][2] += s[j][3]; }
  sb();
#pragma unroll
  for (int j = 0; j < 2; ++j) s[j][0] += s[j][2];
  asm volatile("" : "+v"(s[0][0]), "+v"(s[1][0]));
  hook(5);
  sb();
#pragma unroll
  for (int j = 0; j < 2; ++j) s[j][0].x += s[j][0].y;
  hook(6);
  sb();
#pragma unroll
  for (int j = 0; j < 2; ++j) r[j] = bc(__builtin_amdgcn_rcpf(s[j][0].x));
  hook(7);
  sb();
#pragma unroll
  for (int i = 0; i < 8; i += 4) {
#pragma unroll
    for (int j = 0; j < 8; ++j) e[j & 1][i + (j >> 1)] *= r[j & 1];
    sb();
    oa[i >> 2] = u32x4{cvtpk(e[0][i].x, e[0][i].y), cvtpk(e[0][i + 1].x, e[0][i + 1].y), cvtpk(e[0][i + 2].x, e[0][i + 2].y), cvtpk(e[0][i + 3].x, e[0][i + 3].y)};
    ob[i >> 2] = u32x4{cvtpk(e[1][i].x, e[1][i].y), cvtpk(e[1][i + 1].x, e[1][i + 1].y), cvtpk(e[1][i + 2].x, e[1][i + 2].y), cvtpk(e[1][i + 3].x, e[1][i + 3].y)};
    hook(8 + (i >> 2));
    sb();
  }
}
#define OUT_AUX 16
DI void store_out(float* out, const f32x16& a0, const f32x16& a1, int row0, int col0, int l) {
  const __amdgpu_buffer_rsrc_t ro = __builtin_amdgcn_make_buffer_rsrc((void*)out, 0, 8388608, 0x00020000);
  const u32 p = ((row0 + 4 * (l >> 5)) * 1024 + col0 + (l & 31)) * 4;
#pragma unroll
  for (int r = 0; r < 16; ++r) {
    __builtin_amdgcn_raw_buffer_store_b32(__float_as_uint(a0[r]), ro, p, (8 * (r >> 2) + (r & 3)) * 4096, OUT_AUX);
    __builtin_amdgcn_raw_buffer_store_b32(__float_as_uint(a1[r]), ro, p + 128, (8 * (r >> 2) + (r & 3)) * 4096, OUT_AUX);
  }
}

#define A_SLOT 32768u
#define A_STG  0u
#define A_SEG  0u
#define A_PART 7168u
#define A_LAM  8192u
#define A_DK   9216u
#define A_PAR  134144u
#define A_PRE  132096u
#define A_PRE2 16384u
#define B_SLOT 45056u
#define B_KVT  81920u
#define B_WO   98304u
#define B_WPHI 135168u
#define B_PRE  136192u
#define B_STG  126976u
#define B_PRE2 159744u
#define YQ (QD == 5 ? 23 : 4 * QD)
#define LPF 3
#define CE 4
#define OPOLL 0
#define GAD 0
#define GAS 0
#define XE 1
#define QD 5

__global__ __launch_bounds__(512, 2) void fused_k(Args) {
  extern __shared__ __attribute__((aligned(16))) char smem_[];
  const int b = blockIdx.x, g = b & 7, jb = b >> 3;
  const int tid = threadIdx.x, l = tid & 63;
  const int w = __builtin_amdgcn_readfirstlane(tid >> 6);
  const bool isA = g >= 4;
  const KARG Args* ka = (const KARG Args*)__builtin_amdgcn_kernarg_segment_ptr();
  u64 kp, kq;
  asm volatile("s_load_dwordx2 %0, %2, %3\n\ts_load_dwordx2 %1, %2, 0x8" : "=&s"(kp), "=&s"(kq) : "s"(ka), "s"(isA ? (w > 5 ? 8 * w + 8 : 48) : 0));
  const int tt = 4 * (g & 3) + (jb >> 3), ct = jb & 7;
  const int tb = tt >> 1, cb = jb & 15;
#define ROT 0
  const int s0 = ROT == 0 ? 0 : (ROT == 2 ? ((ct ^ (jb >> 3)) & 1) * 4 : ((ct + (jb >> 3)) & 3) * 2);
  const int h = l >> 5, l31 = l & 31;
  STAMP(0);
  unsigned char* ws;
  float* out = nullptr;
#define pE ((const void*)(ws + (isA ? WS_EPA : WS_EPB)))
#define rw __builtin_amdgcn_make_buffer_rsrc((void*)ws, 0, (int)WS_BYTES, 0x00020000)
  u32 E = 0;
  const u32 flagoff = WS_FLAG + ((isA ? 0u : 512u) + tt * 32) * FS;

  if (w < 4) {
    u64 pc; u32 len;
    asm volatile("s_getpc_b64 %0\nfk_code_lo:\n\ts_mov_b32 %1, fk_code_hi-fk_code_lo" : "=s"(pc), "=s"(len));
    gpre((isA ? A_PRE : B_PRE) + 1024 + w * 256, ((w + 4 * (jb & 1)) * 64 + l) * 128, srd((const void*)pc, len), 0);
  }
  if (w >= 4) {
    const int w4 = w - 4;
    const u32 lr8 = l >> 3;
    if (isA) {
      const u32 lp = w4 < 2 ? (32 * (lr8 >> 2) + 16 * (lr8 & 1) + ((lr8 >> 1) & 1)) : lr8;
      const u32 rot = w4 < 2 ? ct : 2 * (jb >> 3);
      u32 v0 = (lp * 1024 + (((l & 7) ^ (l >> 4)) << 4)) ^ ((rot & 1) << 6), v1 = v0 ^ 64;
      const u32 rb = w4 < 2 ? (128 * tt + 64 * w4) : 64 * ct;
      const u32 pa = w4 < 2 ? 2048u : 8192u;
      u32 qs[8], qd[8];
#pragma unroll
      for (int p = 0; p < 8; ++p) { const u32 q = (p + rot) & 7; qd[p] = w4 * 8192 + q * 1024; qs[p] = rb * 1024 + q * pa; asm volatile("" : "+s"(qd[p]), "+s"(qs[p])); }
      asm volatile("" : "+v"(v0), "+v"(v1));
      u32 nb = w4 < 2 ? 2097152u : 524288u;
      asm volatile("" : "+s"(nb));
      u64 p0;
      asm volatile("s_waitcnt lgkmcnt(0)\n\ts_mov_b64 %0, %1" : "=&s"(p0) : "s"(kp), "s"(kq));
      const u32x4 rs = srd((const void*)p0, nb);
      auto issue = [&](int i) {
#pragma unroll
        for (int p = 0; p < 8; ++p) glds((i & 3) * A_SLOT + qd[p], (p & 1) ? v1 : v0, rs, qs[p] + (((s0 + i) & 7) << 7));
      };
      issue(0);
      asm volatile("" : "+s"(ka));
      ws = ka->ws;
      issue(1);
      gpre(A_PAR + w4 * 256, l * 4, srd(w4 == 0 ? ka->lre : (w4 == 1 ? ka->lim : (w4 == 2 ? ka->ldt : ka->dsk)), 1024u), ct * 128);
      const u32x4 rc = srd((w4 & 1) ? ka->cim : ka->cre, 524288u);
      const u32 vc0 = lr8 * 1024 + (((l & 7) ^ (l >> 4)) << 4), vc1 = vc0 ^ 64;
      u32 rc2 = 2 * (jb >> 3);
      auto issueC = [&](int i) {
        const u32 so = 64 * ct * 1024 + (((ct + i) & 7) << 7);
        const u32 dst = (i < 2 ? 131072u + i * 16384u : (i * 16384u)) + (w4 & 1) * 8192;
#pragma unroll
        for (int p = 0; p < 8; ++p) { const u32 q = (p + rc2) & 7; glds(dst + q * 1024, (p & 1) ? vc1 : vc0, rc, so + q * 8192); }
      };
      LST(0); WAITV(9); LST(1); bar(); LST(2); issue(2);
      E = load_epoch(pE);
      const u64 T = (u64)E * 0x9E3779B97F4A7C15ull + 0x632BE59BD9B4E019ull;
      u32 thi = (u32)(T >> 32), tlo = (u32)(T >> 24) & 0xffu, tw = tlo << 24;
      asm volatile("" : "+s"(thi), "+s"(tlo), "+s"(tw));
      LST(3); WAITV(8); LST(4); bar(); LST(5); issue(3);
      const float lr = bf2f(*(const LDS3 u16*)(A_PAR + l * 2)), li = bf2f(*(const LDS3 u16*)(A_PAR + 256 + l * 2));
      float dk = bf2f(*(const LDS3 u16*)(A_PAR + 768 + l * 2));
      const float dt = bf2f(cvtpk(exp_acc(bf2f(*(const LDS3 u16*)(A_PAR + 512 + l * 2))), 0.f) & 0xffffu);
      float mg = exp_acc(dt * lr), th = dt * li, rd = __builtin_amdgcn_rcpf(lr * lr + li * li);
      asm volatile("" : "+v"(mg), "+v"(th), "+v"(rd), "+v"(dk));
      LST(6); WAITV(8); LST(7); bar(); LST(8); issue(4);
#define CPF 3
      if (CPF == 1) gpre(A_PRE + w4 * 256, (16 * jb + 8 * (w4 >> 1) + lr8) * 1024 + (l & 7) * 128, rc, 0);
      float sn, cs;
      sincos_cw(th, sn, cs);
      asm volatile("" : "+v"(sn), "+v"(cs));
      LST(9); WAITV(8 + (CPF == 1)); LST(10); bar(); LST(11); issue(5);
      cpx lam = {mg * cs, mg * sn}, coef;
      coef.r = ((lam.r - 1.f) * lr + lam.i * li) * rd; coef.i = (lam.i * lr - (lam.r - 1.f) * li) * rd;
      cpx l16 = csq(csq(csq(csq(lam))));
      asm volatile("" : "+v"(lam.r), "+v"(lam.i), "+v"(coef.r), "+v"(coef.i), "+v"(l16.r), "+v"(l16.i));
      LST(12); WAITV(8); LST(13); bar(); LST(14); issue(6); LST(15);
      cpx l128 = csq(csq(csq(l16)));
      cpx l256 = csq(l128), l512 = csq(l256);
      asm volatile("" : "+v"(l128.r), "+v"(l128.i), "+v"(l256.r), "+v"(l256.i), "+v"(l512.r), "+v"(l512.i));
      WAITV(8); bar(); issue(7);
      const int jl = w4 + ((tt - 1 - w4) & ~3), ex = tt - 1 - jl;
      cpx f[4], l32 = csq(l16), l64 = csq(l32);
      f[3] = cpx{1.f, 0.f};
      if (ex & 1) f[3] = l128;
      if (ex & 2) f[3] = cmul(f[3], l256);
#pragma unroll
      for (int m = 2; m >= 0; --m) f[m] = cmul(f[m + 1], l512);
      u32 pw = A_PART + (l >> 5) * 8192 + l31 * 32 + w4 * 8, ga = WS_GA + (64 * ct + l) * 16;
      asm volatile("" : "+v"(f[0].r), "+v"(f[0].i), "+v"(f[1].r), "+v"(f[1].i), "+v"(f[2].r), "+v"(f[2].i), "+v"(f[3].r), "+v"(f[3].i));
      asm volatile("" : "+v"(l32.r), "+v"(l32.i), "+v"(l64.r), "+v"(l64.i), "+v"(pw), "+v"(ga));
      if (w4 == 0) { *(LDS3 f32x4*)(A_LAM + l * 16) = f32x4{lam.r, lam.i, coef.r, coef.i}; *(LDS3 float*)(A_DK + l * 4) = dk; }
      asm volatile("s_waitcnt lgkmcnt(0)" ::: "memory");
      asm volatile("" : "+s"(rc2));
      WAITV(8); bar(); issueC(w4 >> 1);
      WAITV(8); bar(); issueC(2 + (w4 >> 1));
      if (CPF == 3) gpre(A_PRE2 + w4 * 256, (64 * ct + 16 * (jb >> 3) + 8 * (w4 >> 1) + (l & 7)) * 1024 + ((ct + 4 + (lr8 & 3)) & 7) * 128, rc, 0);
      STAMP(1);
#define APRIO 0
      __builtin_amdgcn_s_setprio(APRIO);
      bar();
      if (w4 == 3) {
        cpx t[8];
#pragma unroll
        for (int s = 0; s < 8; ++s) { f32x2 e = *(const LDS3 f32x2*)(A_SEG + s * 512 + l * 8); t[s] = cpx{e.x, e.y}; }
#pragma unroll
        for (int s = 0; s < 4; ++s) t[s] = cfma(l16, t[2 * s], t[2 * s + 1]);
        t[0] = cfma(l64, cfma(l32, t[0], t[1]), cfma(l32, t[2], t[3]));
        __builtin_amdgcn_raw_buffer_store_b128(gran2(t[0].r, t[0].i, thi, tlo), rw, ga + tt * 8192, 0, AUX_SC1);
      }
      if (CE) {
        if (w4 != 3) __builtin_amdgcn_s_sleep(CE);
        issueC(4 + (w4 >> 1));
        issueC(6 + (w4 >> 1));
      }
      STAMP(2);
      cpx P = {0.f, 0.f};
      if (w4 < tt) {
        u32x4 gq[4];
        if (GAD) __builtin_amdgcn_s_sleep(GAD);
        for (int it = 0; it < SPIN_MAX; ++it) {
          fence();
          u32 bad = 0;
#pragma unroll
          for (int m = 0; m < 4; ++m) {
            const int j = jl - 4 * (3 - m);
            if (j >= 0) gq[m] = __builtin_amdgcn_raw_buffer_load_b128(rw, ga, j * 8192, AUX_SC1);
            else gq[m] = u32x4{tw, thi, tw, thi};
          }
#pragma unroll
          for (int m = 0; m < 4; ++m) bad |= granbad(gq[m], thi, tw);
          if (__builtin_amdgcn_ballot_w64(bad != 0) == 0) break;
          if (GAS) __builtin_amdgcn_s_sleep(GAS);
        }
        const cpx Q = cfma(f[1], cpx{granval(gq[1].x), granval(gq[1].z)}, cmul(f[0], cpx{granval(gq[0].x), granval(gq[0].z)}));
        P = cfma(f[3], cpx{granval(gq[3].x), granval(gq[3].z)}, cmul(f[2], cpx{granval(gq[2].x), granval(gq[2].z)}));
        P.r += Q.r; P.i += Q.i;
      }
      *(LDS3 f32x2*)pw = f32x2{P.r, P.i};
      *(LDS3 f32x2*)(pw + 16384) = f32x2{P.r, P.i};
      STAMP(3);
      asm volatile("s_waitcnt vmcnt(0) lgkmcnt(0)\n\ts_barrier" ::: "memory");
      if (!CE) {
        issueC(4 + (w4 >> 1));
        issueC(6 + (w4 >> 1));
        bar();
        WAITV(0);
        bar();
      }
      STAMP(4);
    } else {
      const u32 rot = cb >> 1, rw3 = (cb & 1) + (tb & 1);
      u32 vx0 = (lr8 * 1024 + (((l & 7) ^ (l >> 4)) << 4)) ^ ((rot & 1) << 6), vx1 = vx0 ^ 64;
      u32 vw = (l >> 2) * 3072 + (l & 3) * 16;
      u32 qs[11], qd[11];
#pragma unroll
      for (int p = 0; p < 8; ++p) { const u32 q = 8 * w4 + ((p + rot) & 7); qd[p] = q * 1024; qs[p] = (256 * tb + 8 * q) * 1024; asm volatile("" : "+s"(qd[p]), "+s"(qs[p])); }
#pragma unroll
      for (int p = 0; p < 3; ++p) { const u32 t = p + rw3, pc = 3 * w4 + (t >= 3 ? t - 3 : t); qd[8 + p] = 32768 + pc * 1024; qs[8 + p] = 64 * cb + (pc & 3) * 49152 + (pc >> 2) * 1024; asm volatile("" : "+s"(qd[8 + p]), "+s"(qs[8 + p])); }
      asm volatile("" : "+v"(vx0), "+v"(vx1), "+v"(vw));
      u64 p0, p1;
      asm volatile("s_waitcnt lgkmcnt(0)\n\ts_mov_b64 %0, %2\n\ts_mov_b64 %1, %3" : "=&s"(p0), "=&s"(p1) : "s"(kp), "s"(kq));
      const u32x4 rx = srd((const void*)p0, 2097152u), rq = srd((const void*)p1, 1572864u);
      auto issue = [&](int i) {
        const u32 ks = (s0 + i) & 7, dst = (i % 3) * B_SLOT;
#pragma unroll
        for (int p = 0; p < 8; ++p) {
          glds(dst + qd[p], (p & 1) ? vx1 : vx0, rx, qs[p] + (ks << 7));
          if (p % 3 == 0) glds(dst + qd[8 + p / 3], vw, rq, qs[8 + p / 3] + ks * 64 * 3072);
        }
      };
      issue(0);
      asm volatile("" : "+s"(ka));
      ws = ka->ws;
      if (w4 == 0) glds(B_WPHI, l * 16, srd(ka->wphi, 16384u), cb * 1024);
      issue(1);
      WAITV(11); bar(); issue(2);
      WAITV(11); bar(); issue(3);
      WAITV(11); bar(); issue(4);
      WAITV(11); bar(); issue(5);
      WAITV(11); bar(); issue(6);
      WAITV(11); bar(); issue(7);
      WAITV(11); bar();
      WAITV(0); bar();
#define OPF 1
      if (OPF == 1) gpre(B_PRE2 + w4 * 256, (16 * jb + 4 * w4 + (lr8 & 3)) * 1024 + (l & 7) * 128, srd(ka->wo, 524288u), 0);
      E = load_epoch(pE);
      STAMP(1);
    }
  } else {
    ws = ka->ws; out = ka->out;
    asm volatile("s_waitcnt lgkmcnt(0)" : "+s"(ws), "+s"(out) : "s"(kp), "s"(kq));
  }
  u32 thi = 0, tl32 = 0, tlo = 0, tw = 0;
  auto tags = [&]() {
    const u64 T = (u64)E * 0x9E3779B97F4A7C15ull + 0x632BE59BD9B4E019ull;
    thi = (u32)(T >> 32); tl32 = (u32)T; tlo = (u32)(T >> 24) & 0xffu; tw = tlo << 24;
  };
  const int wm = (w >> 1) & 1, wn = w & 1;
  const int G = l >> 4, tr_r = (l >> 2) & 3, tr_c = l & 3;
  const u32 fragrow = l31 * 128;
  const u32 fsw = (l31 >> 1) & 7;

  if (isA) {
    if (w < 4) {
      u32 ax[2][4], aw[2][4];
#pragma unroll
      for (int k = 0; k < 4; ++k) {
        const u32 t = fragrow + (((2 * k + h) ^ fsw) << 4);
        ax[0][k] = t + wm * 8192; aw[0][k] = t + 16384 + wn * 4096;
        ax[1][k] = ax[0][k] + 65536; aw[1][k] = aw[0][k] + 65536;
        asm volatile("" : "+v"(ax[0][k]), "+v"(ax[1][k]), "+v"(aw[0][k]), "+v"(aw[1][k]));
      }
      const u32 rho = 64 * (w >> 1) + 16 * (G >> 1) + 8 * (tr_r >> 1) + 4 * (w & 1) + 2 * (tr_r & 1);
      u32 xs0 = rho * 128 + (((2 * (G & 1) + (tr_c >> 1)) ^ ((rho >> 1) & 7)) << 4) + (tr_c & 1) * 8 + (((ct - s0) & 3) << 15);
      u32 xs1 = xs0 ^ 64;
      asm volatile("" : "+v"(xs0), "+v"(xs1));
      f32x16 aR[2], aI[2];
#pragma unroll
      for (int i = 0; i < 16; ++i) { aR[0][i] = 0.f; aR[1][i] = 0.f; aI[0][i] = 0.f; aI[1][i] = 0.f; }
      s16x4 xk[8];
#pragma unroll
      for (int i = 0; i < 8; ++i) xk[i] = s16x4{0, 0, 0, 0};
      const int sx = (ct - s0) & 7;
      bf16x8 fx0[2], fx1[2], fwr[2], fwi[2];
      bar();
      STAMP(1);
      fx0[0] = rd128(ax[0][0]); fwr[0] = rd128(aw[0][0]); fx1[0] = rd128(ax[0][0] + 4096); fwi[0] = rd128(aw[0][0] + 8192);
      sb();
      f32x4 lc;
      u32 sa = A_SEG + (4 * wm + 2 * h) * 512 + (32 * wn + l31) * 8;
      asm volatile("" : "+v"(sa));
#pragma unroll
      for (int n = 0; n < 32; ++n) {
        const int s = n >> 2, k = n & 3, c = n & 1, m = n + 1, hb = (m >> 3) & 1, so = ((m >> 2) & 1) * 32768;
        if (k == 0) CST(s);
        aR[0] = mfma(fx0[c], fwr[c], aR[0]); sb();
        if (n == 15) { asm volatile("s_waitcnt vmcnt(0) lgkmcnt(0)\n\ts_barrier" ::: "memory"); sb(); }
        else if (k == 3 && s < 7) { bar_l(); sb(); }
        if (n < 31) { fx0[c ^ 1] = rd128(ax[hb][m & 3] + so); fwr[c ^ 1] = rd128(aw[hb][m & 3] + so); sb(); }
        else { lc = *(const LDS3 f32x4*)(A_LAM + (32 * wn + l31) * 16); sb(); }
        aR[1] = mfma(fx1[c], fwr[c], aR[1]); sb();
        if (n < 31) { fx1[c ^ 1] = rd128(ax[hb][m & 3] + so + 4096); fwi[c ^ 1] = rd128(aw[hb][m & 3] + so + 8192); sb(); }
        aI[0] = mfma(fx0[c], fwi[c], aI[0]); sb();
        if (k == 0 && __builtin_expect(sx == s, XE)) {
#pragma unroll
          for (int q = 0; q < 4; ++q) {
            const u32 o = (32 * (q & 1) + (q >> 1)) * 128;
            xk[q] = __builtin_amdgcn_ds_read_tr16_b64_v4i16((LDS3 s16x4*)(o + xs0));
            xk[4 + q] = __builtin_amdgcn_ds_read_tr16_b64_v4i16((LDS3 s16x4*)(o + xs1));
          }
        }
        sb();
        aI[1] = mfma(fx1[c], fwi[c], aI[1]); sb();
      }
      E = early_epoch(pE);
      const cpx lam = {lc.x, lc.y}, coef = {lc.z, lc.w};
      CST(8);
      STAMP(2);
      const f32x2 Lr = bc(lam.r), Li = bc(lam.i), nLi = bc(-lam.i);
      {
        f32x2 hr = {aR[0][0], aR[0][1]}, hi = {aI[0][0], aI[0][1]};
#pragma unroll
        for (int p = 1; p < 16; ++p) {
          const f32x2 ur = {aR[p >> 3][2 * (p & 7)], aR[p >> 3][2 * (p & 7) + 1]}, ui = {aI[p >> 3][2 * (p & 7)], aI[p >> 3][2 * (p & 7) + 1]};
          scan_step(p, hr, hi, ur, ui, Lr, nLi, Li);
        }
        *(LDS3 f32x2*)sa = f32x2{hr.x, hi.x};
        *(LDS3 f32x2*)(sa + 512) = f32x2{hr.y, hi.y};
      }
      asm volatile("s_waitcnt lgkmcnt(0)\n\ts_barrier" : "+s"(E) :: "memory");
      STAMP(3);
      const float dk0 = *(const LDS3 float*)(A_DK + l31 * 4), dk1 = *(const LDS3 float*)(A_DK + 128 + l31 * 4);
      const cpx l16 = csq(csq(csq(csq(lam))));
      const cpx c2 = {2.f * coef.r, 2.f * coef.i};
      cpx pre0 = {0.f, 0.f}, pre1;
      {
        const int sg = 4 * wm + 2 * h;
        cpx e0 = {0.f, 0.f};
#pragma unroll
        for (int s = 0; s < 7; ++s) {
          f32x2 e = *(const LDS3 f32x2*)(A_SEG + s * 512 + (32 * wn + l31) * 8);
          cpx t = cfma(l16, pre0, cpx{e.x, e.y});
          if (s < sg) pre0 = t;
          if (s == sg) e0 = cpx{e.x, e.y};
        }
        pre1 = cfma(l16, pre0, e0);
      }
      pre0 = cmul(c2, pre0); pre1 = cmul(c2, pre1);
      cpx m0 = c2;
      {
        const cpx l32 = csq(l16), l64 = csq(l32);
        if (h) m0 = cmul(m0, l32);
        if (wm) m0 = cmul(m0, l64);
      }
      const cpx m1 = cmul(m0, l16);
#pragma unroll
      for (int p = 0; p < 16; ++p) {
        const f32x2 ur = {aR[p >> 3][2 * (p & 7)], aR[p >> 3][2 * (p & 7) + 1]}, ui = {aI[p >> 3][2 * (p & 7)], aI[p >> 3][2 * (p & 7) + 1]};
        const f32x2 vr = fma2(bc(c2.r), ur, bc(-c2.i) * ui), vi = fma2(bc(-c2.r), ui, bc(-c2.i) * ur);
        aR[p >> 3][2 * (p & 7)] = vr.x; aR[p >> 3][2 * (p & 7) + 1] = vr.y;
        aI[p >> 3][2 * (p & 7)] = vi.x; aI[p >> 3][2 * (p & 7) + 1] = vi.y;
      }
      f32x16 y0, y1;
#pragma unroll
      for (int q = 0; q < 4; ++q) {
        const u32x2 t0 = __builtin_bit_cast(u32x2, xk[q]), t1 = __builtin_bit_cast(u32x2, xk[4 + q]);
#pragma unroll
        for (int e = 0; e < 2; ++e) {
          const u32 r0 = cvtpk(bf2f(t0[e] & 0xffffu) * dk0, __uint_as_float(t0[e] & 0xffff0000u) * dk0);
          const u32 r1 = cvtpk(bf2f(t1[e] & 0xffffu) * dk1, __uint_as_float(t1[e] & 0xffff0000u) * dk1);
          y0[4 * q + 2 * e] = __uint_as_float(r0 << 16); y0[4 * q + 2 * e + 1] = __uint_as_float(r0 & 0xffff0000u);
          y1[4 * q + 2 * e] = __uint_as_float(r1 << 16); y1[4 * q + 2 * e + 1] = __uint_as_float(r1 & 0xffff0000u);
        }
      }
      f32x2 Mr = {m0.r, m1.r}, nMr = -Mr, nMi = {-m0.i, -m1.i}, Pr = {pre0.r, pre1.r}, nPi = {-pre0.i, -pre1.i};
      const u32 chr = 8 * (G >> 1) + tr_r;
      const u32 fr = (tr_r & 1) | ((tr_r & 2) << 1);
      const u32 cq = 4 * wn + 2 * (G & 1) + (tr_c >> 1);
      u32 rb0 = A_STG + w * 8192 + chr * 128 + (tr_c & 1) * 8 + ((cq ^ fr) << 4), rb1 = (rb0 + 512) ^ 32, rb2 = rb0 ^ 64, rb3 = rb1 ^ 64;
      u32 wb[4];
#pragma unroll
      for (int j = 0; j < 4; ++j) wb[j] = A_STG + w * 8192 + l31 * 128 + (((4 * h + j) ^ ((l31 & 1) | ((l31 & 2) << 1) | ((l31 & 4) >> 1))) << 4);
      u32 hoff = WS_HA + ((tt * 4 + 2 * wm + wn) * 64 + ct * 8 + 2 * wn) * 1024 + l * 16, hof2 = hoff + 4096, hof3 = hoff ^ 65536, hof4 = hof2 ^ 65536;
      u32 pa = A_PART + w * 8192 + l31 * 32;
      u32 fo = l == 0 ? flagoff + (wm * 16 + ct * 2 + wn) * FS : 0x80000000u;
      u32 lp = WS_HA + ((tt * 4 + 2 * wm + (l >> 5)) * 64 + ct * 8 + 2 * wn + ((l >> 4) & 1) * 4 + ((l >> 3) & 1)) * 1024 + (l & 7) * 128;
      asm volatile("" : "+v"(y0), "+v"(y1), "+v"(aR[0]), "+v"(aR[1]), "+v"(aI[0]), "+v"(aI[1]));
      asm volatile("" : "+v"(Mr), "+v"(nMr), "+v"(nMi), "+v"(Pr), "+v"(nPi), "+v"(rb0), "+v"(rb1), "+v"(rb2), "+v"(rb3), "+v"(pa), "+v"(fo), "+v"(lp));
      asm volatile("" : "+v"(wb[0]), "+v"(wb[1]), "+v"(wb[2]), "+v"(wb[3]), "+v"(hoff), "+v"(hof2), "+v"(hof3), "+v"(hof4));
      tags();
      u32x2 tg = {tl32, thi};
      asm volatile("" : "+s"(thi), "+s"(tl32), "+v"(tg));
      STAMP(4);
      bar();
      STAMP(5);
      bf16x8 own[8];
      {
        const f32x4 p0 = *(const LDS3 f32x4*)pa, p1 = *(const LDS3 f32x4*)(pa + 16);
        sb();
        const f32x2 cc = (f32x2{p0.x, p0.y} + f32x2{p0.z, p0.w}) + (f32x2{p1.x, p1.y} + f32x2{p1.z, p1.w});
        f32x2 hr = fma2(nMi, bc(cc.y), Pr), hi = fma2(nMi, bc(cc.x), nPi);
        sb();
        hr = fma2(Mr, bc(cc.x), hr); sb();
        hi = fma2(nMr, bc(cc.y), hi); sb();
        u32x4 v[4];
        f32x2 qr, qi;
#pragma unroll
        for (int p = 0; p < 16; ++p) {
          const f32x2 ur = {aR[p >> 3][2 * (p & 7)], aR[p >> 3][2 * (p & 7) + 1]}, ui = {aI[p >> 3][2 * (p & 7)], aI[p >> 3][2 * (p & 7) + 1]};
          scan_step(p + 1, hr, hi, ur, ui, Lr, Li, nLi);
          if (p & 1) {
            const int j = (p & 7) >> 1;
            v[2][j] = cvtpk(qi.x, hi.x); v[3][j] = cvtpk(qi.y, hi.y); v[0][j] = cvtpk(qr.x, hr.x); v[1][j] = cvtpk(qr.y, hr.y);
            sb();
          } else { qr = hr; qi = hi; }
          if ((p & 7) == 7) {
#pragma unroll
            for (int j = 0; j < 4; ++j) *(LDS3 u32x4*)(wb[2 * (j & 1) + (p >> 3)] + (j >> 1) * 4096) = v[j];
            sb();
          }
        }
        bf16x8 nb[4];
#pragma unroll
        for (int j = 0; j < 4; ++j) { own[j] = rdtr(rb0 + j * 2048, rb1 + j * 2048); nb[j] = rdtr(rb2 + j * 2048, rb3 + j * 2048); sb(); }
#pragma unroll
        for (int j = 0; j < 4; ++j) {
          __builtin_amdgcn_raw_buffer_store_b128(__builtin_bit_cast(u32x4, own[j]), rw, ((j & 2) ? hof2 : hoff) + (j & 1) * 1024, 0, AUX_SC1);
          __builtin_amdgcn_raw_buffer_store_b128(__builtin_bit_cast(u32x4, nb[j]), rw, ((j & 2) ? hof4 : hof3) + (j & 1) * 1024, 0, AUX_SC1);
          sb();
        }
      }
      STAMP(6);
      bar();
      WAITV(0);
      __builtin_amdgcn_raw_buffer_store_b64(tg, rw, fo, 0, AUX_SC1);
      const u32 warm = __builtin_amdgcn_raw_buffer_load_b32(rw, lp, 0, 0);
      STAMP(7);
      u32 ca[4], cb[4];
#pragma unroll
      for (int k = 0; k < 4; ++k) { const u32 t = fragrow + (((2 * k + h) ^ fsw) << 4); ca[k] = t + 98304; cb[k] = t + 32768; }
      bf16x8 bq[3][2];
      {
#pragma unroll
        for (int j = 0; j < 4; ++j) own[4 + j] = rdtr((rb0 ^ 8192) + j * 2048, (rb1 ^ 8192) + j * 2048);
        const u32 c0[2] = {ca[0] ^ (wn * 64), ca[1] ^ (wn * 64)};
        auto cad = [&](int n) { return (c0[n & 1] ^ ((n >> 2) * 64)) + 32768 + ((n >> 1) & 1) * 8192; };
        bq[0][0] = rd128(cad(0)); bq[0][1] = rd128(cad(0) + 4096);
        bq[1][0] = rd128(cad(1)); bq[1][1] = rd128(cad(1) + 4096);
        sb();
#pragma unroll
        for (int n = 0; n < 8; ++n) {
          y0 = mfma(own[n], bq[n % 3][0], y0); sb();
          if (n + 2 < 8) bq[(n + 2) % 3][0] = rd128(cad(n + 2));
          sb();
          y1 = mfma(own[n], bq[n % 3][1], y1); sb();
          if (n + 2 < 8) bq[(n + 2) % 3][1] = rd128(cad(n + 2) + 4096);
          sb();
        }
      }
      if (OPOLL & 1) {
        if (w == 0) { if (PDA) __builtin_amdgcn_s_sleep(PDA); wait_flags(rw, flagoff + l31 * FS, thi, tl32); }
        bar();
      } else {
        if (!CE) bar();
        if (PDA) __builtin_amdgcn_s_sleep(PDA);
        wait_flags(rw, flagoff + (wm * 16 + (l & 15)) * FS, thi, tl32);
      }
      asm volatile("" :: "v"(warm));
      STAMP(8);
      {
        const u32 ha = WS_HA + (tt * 4 + w) * 65536 + l * 16;
        u32x4 af[4][8];
        auto ld = [&](int i) {
          const u32 so = ((ct + i) & 7) * 8192;
#pragma unroll
          for (int k = 0; k < 8; ++k) af[i & 3][k] = __builtin_amdgcn_raw_buffer_load_b128(rw, ha + k * 1024, so, AUX_SC1);
        };
        auto cad = [&](int n) { const int i = n >> 3, k = n & 7; return (i < 2 ? ca[k & 3] + 32768 + i * 16384 : (i < 6 ? cb[k & 3] + (i - 2) * 16384 : ca[k & 3] + (i - 6) * 16384)) + (k >> 2) * 8192; };
        ld(1); ld(2); ld(3);
        bq[8 % 3][0] = rd128(cad(8)); bq[8 % 3][1] = rd128(cad(8) + 4096);
        bq[9 % 3][0] = rd128(cad(9)); bq[9 % 3][1] = rd128(cad(9) + 4096);
        sb();
#pragma unroll
        for (int n = 8; n < 64; ++n) {
          if ((n & 7) == 0) CST(10 + (n >> 3));
          const bf16x8 av = __builtin_bit_cast(bf16x8, af[(n >> 3) & 3][n & 7]);
          y0 = mfma(av, bq[n % 3][0], y0); sb();
          if (n + 2 < 64) bq[(n + 2) % 3][0] = rd128(cad(n + 2));
          if (n + 24 < 64) af[((n >> 3) + 3) & 3][n & 7] = __builtin_amdgcn_raw_buffer_load_b128(rw, ha + (n & 7) * 1024, ((ct + (n >> 3) + 3) & 7) * 8192, AUX_SC1);
          sb();
          y1 = mfma(av, bq[n % 3][1], y1); sb();
          if (n + 2 < 64) bq[(n + 2) % 3][1] = rd128(cad(n + 2) + 4096);
          sb();
        }
      }
      CST(18);
      STAMP(9);
      store_out(out, y0, y1, 128 * tt + 32 * w, 64 * ct, l);
      if (tt == 15 && ct == 0 && w == 0 && l == 0) __builtin_amdgcn_raw_buffer_store_b32(E + 1, rw, WS_EPA, 0, AUX_SC1);
      STAMP(10);
    }
  } else {
    u32x4 pq[2][2][2];
    u32 kva = B_KVT + (l31 < 16 ? (8 * ((l31 >> 2) & 1) + 4 * (l31 >> 3) + (l31 & 3)) : 16) * 64 + h * 32;
    u32 ooff = WS_OB + ((tb * 8 + 2 * w) * 32 + 2 * cb) * 1024 + l * 16;
    u32 ooff2 = ooff + 32768;
    if (w < 4) {
      asm volatile("" : "+v"(kva), "+v"(ooff), "+v"(ooff2));
      u32 ax[2][4], aw[2];
#pragma unroll
      for (int k = 0; k < 4; ++k) {
        ax[0][k] = fragrow + (((2 * k + h) ^ fsw) << 4) + w * 8192; ax[1][k] = ax[0][k] + 2 * B_SLOT;
        asm volatile("" : "+v"(ax[0][k]), "+v"(ax[1][k]));
      }
      aw[0] = 32768 + (8 * (G >> 1) + tr_r) * 64 + (2 * (G & 1) + (tr_c >> 1)) * 16 + (tr_c & 1) * 8;
      aw[1] = aw[0] + 2 * B_SLOT;
      asm volatile("" : "+v"(aw[0]), "+v"(aw[1]));
      const u32 stg = B_STG + w * 8192;
      u32 wb0 = stg + l31 * 64 + (((2 * h) ^ ((l31 >> 1) & 3)) << 4), wb1 = wb0 ^ 16;
      u32 ra = stg + (4 * (G >> 1) + tr_r) * 64 + (((2 * (G & 1) + (tr_c >> 1)) ^ (2 * (G >> 1) + (tr_r >> 1))) << 4) + (tr_c & 1) * 8;
      u32 pa0 = stg + (l31 < 16 ? l31 : 16) * 144 + h * 16, pa1 = stg + 2448 + (l31 >= 16 ? l31 - 16 : 16) * 144 + h * 16;
      u32 pad = B_STG + (w == 2 ? (l >> 2 & 1) * 2448 + 16 * 144 + (l & 3) * 32 : w * 2448 + (l >> 2) * 144 + (l & 3) * 32), pad2 = pad + 16384;
      u32 pof = w == 2 && l >= 8 ? 0x80000000u : WS_G1 + cb * 24576 + tb * 1024 + w * 8192 + l * 16;
      asm volatile("" : "+v"(wb0), "+v"(wb1), "+v"(ra), "+v"(pa0), "+v"(pa1), "+v"(pad), "+v"(pad2), "+v"(pof));
      f32x16 q[2], kk[2], v[2];
      bf16x8 wf[2];
#pragma unroll
      for (int i = 0; i < 16; ++i) { q[0][i] = 0.f; q[1][i] = 0.f; kk[0][i] = 0.f; kk[1][i] = 0.f; v[0][i] = 0.f; v[1][i] = 0.f; }
      bf16x8 fx0[2], fx1[2], fwq[2], fwk[2], fwv[2];
      E = early_epoch(pE);
      {
        const u32x4 pz = {~0u, ~0u, ~0u, ~0u};
        if (tid < 136) __builtin_amdgcn_raw_buffer_store_b128(pz, rw, WS_G1 + cb * 24576 + w * 8192 + tb * 1024 + l * 16, 0, AUX_SC1);
      }
      bar();
      STAMP(1);
      fwk[0] = rdtr(aw[0] + 4096, aw[0] + 4352); fx0[0] = rd128(ax[0][0]); fx1[0] = rd128(ax[0][0] + 4096);
      fwv[0] = rdtr(aw[0] + 8192, aw[0] + 8448); fwq[0] = rdtr(aw[0], aw[0] + 256);
      {
        const u32 pa = B_WPHI + (4 * (G >> 1) + tr_r) * 32 + (2 * (G & 1) + (tr_c >> 1)) * 8;
#pragma unroll
        for (int hd = 0; hd < 2; ++hd) wf[hd] = rdtr(pa + hd * 512, pa + hd * 512 + 256);
      }
      sb();
#pragma unroll
      for (int n = 0; n < 32; ++n) {
        const int s = n >> 2, k = n & 3, c = n & 1, m = n + 1, sl = (m >> 2) % 3, hb = sl == 2, so = sl == 1 ? B_SLOT : 0, wo_ = so + (m & 3) * 1024;
        if (k == 0) CST(s);
        kk[0] = mfma(fwk[c], fx0[c], kk[0]); sb();
        if (n == 11) { asm volatile("s_waitcnt vmcnt(0) lgkmcnt(0)\n\ts_barrier" : "+s"(E) :: "memory"); sb(); }
        else if (k == 3 && s < 7) { bar_l(); sb(); }
        if (n < 31) { fwk[c ^ 1] = rdtr(aw[hb] + wo_ + 4096, aw[hb] + wo_ + 4352); sb(); }
        kk[1] = mfma(fwk[c], fx1[c], kk[1]); sb();
        if (n < 31) { fx0[c ^ 1] = rd128(ax[hb][m & 3] + so); fx1[c ^ 1] = rd128(ax[hb][m & 3] + so + 4096); sb(); }
        v[0] = mfma(fx0[c], fwv[c], v[0]); sb();
        if (n < 31) { fwv[c ^ 1] = rdtr(aw[hb] + wo_ + 8192, aw[hb] + wo_ + 8448); sb(); }
        v[1] = mfma(fx1[c], fwv[c], v[1]); sb();
        if (m < 4 * QD) { fwq[c ^ 1] = rdtr(aw[hb] + wo_, aw[hb] + wo_ + 256); sb(); }
        if (s < QD) { q[0] = mfma(fwq[c], fx0[c], q[0]); sb(); }
        if (n == 11) { tags(); asm volatile("" : "+s"(thi), "+s"(tl32)); sb(); }
        if (n == 1 || n == 2) {
          const u32 sgn = ((l >> 2) & 1) ? 0x80008000u : 0u;
          u32x4 t = __builtin_bit_cast(u32x4, wf[n - 1]);
          t.x ^= sgn; t.y ^= sgn; t.z ^= sgn; t.w ^= sgn;
          asm volatile("" : "+v"(t));
          wf[n - 1] = __builtin_bit_cast(bf16x8, t);
        }
        if (n == 12 && tid == 0) __builtin_amdgcn_raw_buffer_store_b64(u32x2{tl32, thi}, rw, WS_PF + (cb * 8 + tb) * 8, 0, AUX_SC1);
        sb();
        if (s < QD) { q[1] = mfma(fwq[c], fx1[c], q[1]); sb(); }
      }
      CST(8);
      STAMP(2);
      f32x16 zero;
#pragma unroll
      for (int i = 0; i < 16; ++i) zero[i] = 0.f;
      {
        f32x16 p[4], kv[2];
        u32x4 vb[2][4], o[2][2];
        bf16x8 fa[4];
        const bool lo = l31 < 16;
        const u32 one = 0x3F803F80u;
        auto vpack = [&](int i) {
          const u32x4 t = __builtin_bit_cast(u32x4, pack8(v[i >> 1], 8 * (i & 1)));
          vb[0][i] = u32x4{lo ? t.x : one, lo ? t.y : one, lo ? t.z : one, lo ? t.w : one};
          vb[1][i] = u32x4{lo ? one : t.x, lo ? one : t.y, lo ? one : t.z, lo ? one : t.w};
        };
#pragma unroll
        for (int i = 0; i < 4; ++i) { p[i] = mfma(wf[i & 1], pack8(kk[i >> 1], 8 * (i & 1)), zero); sb(); }
        vpack(0); vpack(1);
        sb();
        CST(9);
        softmax2(p[0], p[1], o[0], o[1], [&](int i) {
          if (i == 8 || i == 9) { *(LDS3 u32x4*)((i == 8 ? wb0 : wb1)) = o[0][i - 8]; *(LDS3 u32x4*)((i == 8 ? wb0 : wb1) + 4096) = o[1][i - 8]; }
        });
        CST(10);
        softmax2(p[2], p[3], o[0], o[1], [&](int i) {
          if (i == 0) { fa[0] = rdtr(ra, ra + 512); fa[1] = rdtr(ra + 4096, ra + 4096 + 512); fa[2] = rdtr(ra + 1024, ra + 1536); fa[3] = rdtr(ra + 5120, ra + 5120 + 512); }
          if (i == 12) { kv[0] = mfma(fa[0], __builtin_bit_cast(bf16x8, vb[0][0]), zero); kv[1] = mfma(fa[1], __builtin_bit_cast(bf16x8, vb[1][0]), zero); }
          if (i == 13) { kv[0] = mfma(fa[2], __builtin_bit_cast(bf16x8, vb[0][1]), kv[0]); kv[1] = mfma(fa[3], __builtin_bit_cast(bf16x8, vb[1][1]), kv[1]); }
          if (i == 8 || i == 9) { *(LDS3 u32x4*)((i == 8 ? wb0 : wb1) + 2048) = o[0][i - 8]; *(LDS3 u32x4*)((i == 8 ? wb0 : wb1) + 6144) = o[1][i - 8]; }
        });
        CST(11);
        fa[0] = rdtr(ra + 2048, ra + 2560); fa[1] = rdtr(ra + 6144, ra + 6144 + 512); fa[2] = rdtr(ra + 3072, ra + 3584); fa[3] = rdtr(ra + 7168, ra + 7168 + 512);
        sb();
        vpack(2); vpack(3);
        sb();
        kv[0] = mfma(fa[0], __builtin_bit_cast(bf16x8, vb[0][2]), kv[0]);
        kv[1] = mfma(fa[1], __builtin_bit_cast(bf16x8, vb[1][2]), kv[1]);
        kv[0] = mfma(fa[2], __builtin_bit_cast(bf16x8, vb[0][3]), kv[0]);
        kv[1] = mfma(fa[3], __builtin_bit_cast(bf16x8, vb[1][3]), kv[1]);
        sb();
        if (l31 <= 16)
#pragma unroll
          for (int qq = 0; qq < 4; ++qq) *(LDS3 f32x4*)(pa0 + qq * 32) = f32x4{kv[0][4 * qq], kv[0][4 * qq + 1], kv[0][4 * qq + 2], kv[0][4 * qq + 3]};
        if (l31 >= 15)
#pragma unroll
          for (int qq = 0; qq < 4; ++qq) *(LDS3 f32x4*)(pa1 + qq * 32) = f32x4{kv[1][4 * qq], kv[1][4 * qq + 1], kv[1][4 * qq + 2], kv[1][4 * qq + 3]};
      }
      CST(12);
      STAMP(3);
      bar_l();
      if (w < 3) {
        f32x4 t[8];
#pragma unroll
        for (int i = 0; i < 8; ++i) t[i] = *(const LDS3 f32x4*)(((i & 2) ? pad2 : pad) + (i & 1) * 8192 + (i >> 2) * 16);
        sb();
        const f32x4 t0 = (t[0] + t[1]) + (t[2] + t[3]), t1 = (t[4] + t[5]) + (t[6] + t[7]);
        const u32x4 r = {umin_(cvtpk(t0.x, t0.y), ~1u), umin_(cvtpk(t0.z, t0.w), ~1u), umin_(cvtpk(t1.x, t1.y), ~1u), umin_(cvtpk(t1.z, t1.w), ~1u)};
        __builtin_amdgcn_raw_buffer_store_b128(r, rw, pof, 0, AUX_SC1);
      }
      CST(13);
      STAMP(4);
      {
        auto rq = [&](int m) {
          const int sl = (m >> 2) % 3, hb = sl == 2, so = sl == 1 ? B_SLOT : 0, wo_ = so + (m & 3) * 1024;
          fwq[m & 1] = rdtr(aw[hb] + wo_, aw[hb] + wo_ + 256); fx0[m & 1] = rd128(ax[hb][m & 3] + so); fx1[m & 1] = rd128(ax[hb][m & 3] + so + 4096);
        };
        if (QD < 8) rq(4 * QD);
        sb();
#pragma unroll
        for (int n = 4 * QD; n < 32; ++n) {
          if (n < 31) rq(n + 1);
          sb();
          q[0] = mfma(fwq[n & 1], fx0[n & 1], q[0]); sb();
          q[1] = mfma(fwq[n & 1], fx1[n & 1], q[1]); sb();
          if (n == YQ) { bar(); sb(); }
        }
      }
      CST(14);
      {
        f32x16 p[4];
#pragma unroll
        for (int i = 0; i < 4; ++i) { p[i] = mfma(wf[i & 1], pack8(q[i >> 1], 8 * (i & 1)), zero); sb(); }
        softmax2(p[0], p[1], pq[0][0], pq[0][1], [&](int) {});
        softmax2(p[2], p[3], pq[1][0], pq[1][1], [&](int) {});
      }
      asm volatile("" : "+v"(pq[0][0][0]), "+v"(pq[0][0][1]), "+v"(pq[0][1][0]), "+v"(pq[0][1][1]), "+v"(pq[1][0][0]), "+v"(pq[1][0][1]), "+v"(pq[1][1][0]), "+v"(pq[1][1][1]));
      CST(15);
      STAMP(5);
    } else {
      tags();
      wait_flags(rw, WS_PF + cb * 64 + (l & 7) * 8, thi, tl32);
      STAMP(2);
#define HPRIO 3
      __builtin_amdgcn_s_setprio(HPRIO);
      bar();
      bar();
      {
        const u32x4 ro = srd(ka->wo, 524288u);
        const u32 vo = (l >> 3) * 1024 + (((l & 7) ^ (((l >> 4) & 1) << 2)) << 4);
        const u32 ro2 = 2 * (jb >> 3);
#define WOW 4
        if ((w & 3) < WOW)
#pragma unroll 1
        for (u32 i = w & 3; i < 8; i += WOW) {
          const u32 so = (((ct + i) & 7) << 16) + 128 * ct;
#pragma unroll
          for (int p = 0; p < 8; ++p) { const u32 q = (p + ro2) & 7; glds(B_WO + i * 8192 + q * 1024, vo, ro, so + q * 8192); }
        }
      }
      STAMP(3);
    }
    if (w >= 5) {
      const u32 off = WS_G1 + cb * 24576 + (w == 5 ? 16384 + (l & 7) * 16 : (w & 1) * 8192 + l * 16);
      u32x4 gq[8];
      const u32x4 rg = srd(ws, WS_BYTES);
      u32 pend = 0xffu;
      for (int it = 0; it < SPIN_MAX; ++it) {
        asm volatile(
          "s_bitcmp1_b32 %8, 7\n\ts_cselect_b64 exec, -1, 0\n\tbuffer_load_dwordx4 %0, %9, %10, %11 offen offset:0 sc1\n\t"
          "s_bitcmp1_b32 %8, 6\n\ts_cselect_b64 exec, -1, 0\n\tbuffer_load_dwordx4 %1, %9, %10, %11 offen offset:1024 sc1\n\t"
          "s_bitcmp1_b32 %8, 5\n\ts_cselect_b64 exec, -1, 0\n\tbuffer_load_dwordx4 %2, %9, %10, %11 offen offset:2048 sc1\n\t"
          "s_bitcmp1_b32 %8, 4\n\ts_cselect_b64 exec, -1, 0\n\tbuffer_load_dwordx4 %3, %9, %10, %11 offen offset:3072 sc1\n\t"
          "s_bitcmp1_b32 %8, 3\n\ts_cselect_b64 exec, -1, 0\n\tbuffer_load_dwordx4 %4, %9, %10, %12 offen offset:0 sc1\n\t"
          "s_bitcmp1_b32 %8, 2\n\ts_cselect_b64 exec, -1, 0\n\tbuffer_load_dwordx4 %5, %9, %10, %12 offen offset:1024 sc1\n\t"
          "s_bitcmp1_b32 %8, 1\n\ts_cselect_b64 exec, -1, 0\n\tbuffer_load_dwordx4 %6, %9, %10, %12 offen offset:2048 sc1\n\t"
          "s_bitcmp1_b32 %8, 0\n\ts_cselect_b64 exec, -1, 0\n\tbuffer_load_dwordx4 %7, %9, %10, %12 offen offset:3072 sc1\n\t"
          "s_mov_b64 exec, -1\n\ts_waitcnt vmcnt(0)"
          : "+v"(gq[0]), "+v"(gq[1]), "+v"(gq[2]), "+v"(gq[3]), "+v"(gq[4]), "+v"(gq[5]), "+v"(gq[6]), "+v"(gq[7]), "+s"(pend)
          : "v"(off), "s"(rg), "s"(0u), "s"(4096u) : "scc", "memory");
        u32 mx[8];
#pragma unroll
        for (int m = 0; m < 8; ++m) mx[m] = umax_(umax_(gq[m].x, gq[m].y), gq[m].z);
        {
          const u32 a0 = umax_(umax_(mx[0], mx[1]), mx[2]), a1 = umax_(umax_(mx[3], mx[4]), mx[5]), a2 = umax_(umax_(mx[6], mx[7]), gq[0].w);
          const u32 a3 = umax_(umax_(gq[1].w, gq[2].w), gq[3].w), a4 = umax_(umax_(gq[4].w, gq[5].w), gq[6].w);
          const u32 a5 = umax_(umax_(umax_(umax_(a0, a1), a2), umax_(umax_(a3, a4), gq[7].w)), 0u);
          if (__builtin_amdgcn_ballot_w64(a5 == ~0u) == 0) break;
        }
        pend = 0;
#pragma unroll
        for (int m = 0; m < 8; ++m)
          asm volatile("v_cmp_eq_u32 vcc, -1, %1\n\ts_cmp_lg_u64 vcc, 0\n\ts_addc_u32 %0, %0, %0" : "+s"(pend) : "v"(umax_(mx[m], gq[m].w)) : "vcc", "scc");
      }
      if (w >= 4) STAMP(10);
      float ac[8];
#pragma unroll
      for (int i = 0; i < 8; ++i) ac[i] = 0.f;
#pragma unroll
      for (int m = 0; m < 8; ++m)
#pragma unroll
        for (int i = 0; i < 4; ++i) {
          const u32 t = gq[m][i];
          asm("v_dot2c_f32_bf16 %0, %1, %2" : "+v"(ac[2 * i]) : "s"(0x00003F80u), "v"(t));
          asm("v_dot2c_f32_bf16 %0, %1, %2" : "+v"(ac[2 * i + 1]) : "s"(0x3F800000u), "v"(t));
        }
      asm volatile("s_nop 2" : "+v"(ac[0]), "+v"(ac[1]), "+v"(ac[2]), "+v"(ac[3]), "+v"(ac[4]), "+v"(ac[5]), "+v"(ac[6]), "+v"(ac[7]));
      const u32 ka = B_KVT + (w == 5 ? (((l >> 2) & 1) * 17 + 16) * 64 : ((w & 1) * 17 + (l >> 2)) * 64) + (l & 3) * 16;
      *(LDS3 u32x4*)ka = u32x4{cvtpk(ac[0], ac[1]), cvtpk(ac[2], ac[3]), cvtpk(ac[4], ac[5]), cvtpk(ac[6], ac[7])};
      if (w >= 4) STAMP(4);
    }
    bar_l();
    if (w >= 4) {
      WAITV(0);
      bar();
    } else {
      STAMP(6);
      {
        const u32 ka = kva;
        f32x16 zero;
#pragma unroll
        for (int i = 0; i < 16; ++i) zero[i] = 0.f;
        bf16x8 kf[4];
#pragma unroll
        for (int i = 0; i < 4; ++i) kf[i] = rd128(ka + (i >> 1) * 1088 + (i & 1) * 16);
        sb();
        f32x16 o[4];
        float r[4];
        auto epi = [&](int i) {
          float t[8];
          if (i < 2) {
#pragma unroll
            for (int j = 0; j < 8; ++j) asm("v_mul_f32 %0, %1, %2" : "=v"(t[j]) : "v"(o[i][j]), "v"(r[i]));
          } else {
#pragma unroll
            for (int j = 0; j < 8; ++j) t[j] = o[i][j] * r[i];
          }
          const u32x4 ov = {cvtpk(t[0], t[1]), cvtpk(t[2], t[3]), cvtpk(t[4], t[5]), cvtpk(t[6], t[7])};
          __builtin_amdgcn_raw_buffer_store_b128(ov, rw, ((i & 1) ? ooff2 : ooff) + (i >> 1) * 1024, 0, AUX_SC1);
          sb();
        };
#pragma unroll
        for (int i = 0; i < 8; ++i) {
          const int t = (i & 1) + (i >> 2) * 2, s2 = (i >> 1) & 1;
          o[t] = mfma(kf[(t & 2) + s2], __builtin_bit_cast(bf16x8, pq[t & 1][t >> 1][s2]), s2 ? o[t] : zero);
          sb();
          if (i == 4 || i == 5) { r[i - 4] = __builtin_amdgcn_rcpf(o[i - 4][8] + 1e-6f); asm volatile("" : "+v"(r[i - 4])); sb(); }
          if (i == 6) epi(0);
        }
        epi(1);
        asm volatile("" : "+v"(o[2]), "+v"(o[3]));
        r[2] = __builtin_amdgcn_rcpf(o[2][8] + 1e-6f); r[3] = __builtin_amdgcn_rcpf(o[3][8] + 1e-6f);
        asm volatile("" : "+v"(r[2]), "+v"(r[3]));
        sb();
        epi(2); epi(3);
      }
      WAITV(0);
      if (l == 0) __builtin_amdgcn_raw_buffer_store_b64(u32x2{tl32, thi}, rw, WS_FLAG + (512 + tb * 64 + w * 16 + cb) * FS, 0, AUX_SC1);
      if (LPF & 2) gpre(w * 256, WS_OB + ((tb * 8 + 2 * w + ((l >> 4) & 1)) * 32 + 2 * cb) * 1024 + (l & 15) * 128, srd(ws, WS_BYTES), 0);
      STAMP(7);
      f32x16 y0, y1;
      u32 oa = WS_OB + (tt * 4 + w) * 32768 + l * 16;
      u32 b0a = B_WO + (8 * (G >> 1) + tr_r) * 128 + (((2 * (G & 1) + (tr_c >> 1)) ^ ((tr_r >> 1) << 2)) << 4) + (tr_c & 1) * 8;
      u32 b1a = b0a ^ 64;
      u32 so[8];
#pragma unroll
      for (int i = 0; i < 8; ++i) { so[i] = ((ct + i) & 7) * 4096; asm volatile("" : "+s"(so[i])); }
      asm volatile("" : "+v"(oa), "+v"(b0a), "+v"(b1a));
      if (OPOLL & 2) {
        if (w == 0) { if (PD) __builtin_amdgcn_s_sleep(PD); wait_flags(rw, flagoff + l31 * FS, thi, tl32); }
        bar();
      } else {
        bar();
        if (PD) __builtin_amdgcn_s_sleep(PD);
        wait_flags(rw, flagoff + (wm * 16 + (l & 15)) * FS, thi, tl32);
      }
      STAMP(8);
      {
#pragma unroll
        for (int i = 0; i < 16; ++i) { y0[i] = 0.f; y1[i] = 0.f; }
        u32x4 af[8][4];
        asm volatile("" : "+v"(oa) :: "memory");
#pragma unroll
        for (int i = 0; i < 8; ++i) {
#pragma unroll
          for (int k = 0; k < 4; ++k) af[i][k] = __builtin_amdgcn_raw_buffer_load_b128(rw, oa + k * 1024, so[i], AUX_SC1);
          sb();
        }
        bf16x8 bq[3][2];
        bq[0][0] = rdtr(b0a, b0a + 512); bq[0][1] = rdtr(b1a, b1a + 512);
        bq[1][0] = rdtr(b0a + 2048, b0a + 2560); bq[1][1] = rdtr(b1a + 2048, b1a + 2560);
        sb();
#pragma unroll
        for (int n = 0; n < 32; ++n) {
          const u32 o = (n + 2) * 2048;
          const u32x4 au = af[n >> 2][n & 3];
          const bf16x8 av = __builtin_bit_cast(bf16x8, au);
          y0 = mfma(av, bq[n % 3][0], y0); sb();
          if (n + 2 < 32) bq[(n + 2) % 3][0] = rdtr(b0a + o, b0a + o + 512);
          sb();
          y1 = mfma(av, bq[n % 3][1], y1); sb();
          if (n + 2 < 32) bq[(n + 2) % 3][1] = rdtr(b1a + o, b1a + o + 512);
          sb();
        }
      }
      STAMP(9);
      store_out(out, y0, y1, 128 * tt + 32 * w, 512 + 64 * ct, l);
      if (tt == 15 && ct == 0 && w == 0 && l == 0) __builtin_amdgcn_raw_buffer_store_b32(E + 1, rw, WS_EPB, 0, AUX_SC1);
      STAMP(10);
    }
  }
  asm volatile("fk_code_hi:");
}

#define BENCH_LAUNCH(first, last, k, grid, block, shm, st, ...) hipLaunchKernelGGL(k, dim3(grid), dim3(block), shm, st, __VA_ARGS__)

extern "C" void kernel_launch(void* const* d_in, const int* in_sizes, int n_in, void* d_out, int out_size, void* d_ws, size_t ws_size, hipStream_t stream) {
  Args a;
  a.xa = (const u16*)d_in[0];
  a.lre = (const u16*)d_in[1];
  a.lim = (const u16*)d_in[2];
  a.bre = (const u16*)d_in[3];
  a.bim = (const u16*)d_in[4];
  a.cre = (const u16*)d_in[5];
  a.cim = (const u16*)d_in[6];
  a.ldt = (const u16*)d_in[7];
  a.dsk = (const u16*)d_in[8];
  a.xb = (const u16*)d_in[9];
  a.wqkv = (const u16*)d_in[10];
  a.wphi = (const u16*)d_in[11];
  a.wo = (const u16*)d_in[12];
  a.out = (float*)d_out;
  a.ws = (unsigned char*)d_ws;
  (void)hipFuncSetAttribute((const void*)fused_k, hipFuncAttributeMaxDynamicSharedMemorySize, 163840);
  BENCH_LAUNCH(true, true, fused_k, 256, 512, 163840, stream, a);
}
```

```cpp
#include <hip/hip_runtime.h>
#include <hip/hip_bf16.h>

#pragma clang diagnostic ignored "-Winline-asm"
typedef unsigned short u16;
typedef unsigned int u32;
typedef unsigned long long u64;
typedef __bf16 bf16x8 __attribute__((ext_vector_type(8)));
typedef __bf16 bf16x2 __attribute__((ext_vector_type(2)));
typedef short s16x4 __attribute__((ext_vector_type(4)));
typedef float f32x2 __attribute__((ext_vector_type(2)));
typedef float f32x4 __attribute__((ext_vector_type(4)));
typedef float f32x16 __attribute__((ext_vector_type(16)));
typedef u32 u32x2 __attribute__((ext_vector_type(2)));
typedef u32 u32x4 __attribute__((ext_vector_type(4)));
#define LDS3 __attribute__((address_space(3)))
#define DI __device__ __forceinline__
#define KARG __attribute__((address_space(4)))

#define WS_EPA   0u
#define WS_EPB   256u
#define FS 256u
#define PSL 1
#define PDA 6
#define PD 12
#define WS_FLAG  1048576u
#define WS_GA    65536u
#define WS_PF    16384u
#define WS_G1    524288u
#define WS_HA    4194304u
#define WS_OB    8388608u
#define WS_BYTES 16777216u
#define AUX_SC1 16
#define SPIN_MAX (1 << 18)

struct Args { const u16 *xb, *wqkv, *wphi, *wo; float* out; unsigned char* ws; const u16 *xa, *bre, *bim, *cre, *cim, *lre, *lim, *ldt, *dsk; };


DI u32 umin_(u32 a, u32 b) { return __builtin_elementwise_min(a, b); }
DI u32 umax_(u32 a, u32 b) { return __builtin_elementwise_max(a, b); }
DI float bf2f(u32 v) { return __uint_as_float(v << 16); }
DI u32 cvtpk(float a, float b) { f32x2 t = {a, b}; return __builtin_bit_cast(u32, __builtin_convertvector(t, bf16x2)); }
DI u32x4 srd(const void* p, u32 bytes) { u64 a = (u64)p; u32x4 r = {(u32)a, (u32)(a >> 32) & 0xffffu, bytes, 0x00020000u}; return r; }
DI void glds(u32 ldsdst, u32 voff, u32x4 rs, u32 soff) {
  asm volatile("s_mov_b32 m0, %0\n\ts_nop 0\n\tbuffer_load_dwordx4 %1, %2, %3 offen lds" :: "s"(ldsdst), "v"(voff), "s"(rs), "s"(soff) : "m0", "memory");
}
DI void gldsw(u32 ldsdst, u32 voff, u32x4 rs, u32 soff) {
  asm volatile("s_mov_b32 m0, %0\n\ts_nop 0\n\tbuffer_load_dwordx4 %1, %2, %3 offen lds" :: "s"(ldsdst), "v"(voff), "s"(rs), "s"(soff) : "m0", "memory");
}
DI void gldsx(u32 ldsdst, u32 voff, u32x4 rs, u32 soff) {
  asm volatile("s_mov_b32 m0, %0\n\ts_nop 0\n\tbuffer_load_dwordx4 %1, %2, %3 offen lds" :: "s"(ldsdst), "v"(voff), "s"(rs), "s"(soff) : "m0", "memory");
}
DI void gpre(u32 ldsdst, u32 voff, u32x4 rs, u32 soff) {
  asm volatile("s_mov_b32 m0, %0\n\ts_nop 0\n\tbuffer_load_dword %1, %2, %3 offen lds" :: "s"(ldsdst), "v"(voff), "s"(rs), "s"(soff) : "m0", "memory");
}
DI u32 load_epoch(const void* p) { u32 e; asm volatile("s_load_dword %0, %1, 0x0\n\ts_waitcnt lgkmcnt(0)" : "=s"(e) : "s"(p) : "memory"); return e; }
#define WAITV(n) asm volatile("s_waitcnt vmcnt(%0)" :: "n"(n) : "memory")
DI void bar_l() { asm volatile("s_waitcnt lgkmcnt(0)\n\ts_barrier" ::: "memory"); }
DI void bar() { asm volatile("s_barrier" ::: "memory"); }
DI void fence() { asm volatile("" ::: "memory"); }
DI void sb() { __builtin_amdgcn_sched_barrier(0); }
DI bf16x8 rd128(u32 a) { return *(const LDS3 bf16x8*)a; }
DI bf16x8 rdtr(u32 a0, u32 a1) {
  s16x4 x = __builtin_amdgcn_ds_read_tr16_b64_v4i16((LDS3 s16x4*)a0);
  s16x4 y = __builtin_amdgcn_ds_read_tr16_b64_v4i16((LDS3 s16x4*)a1);
  return __builtin_bit_cast(bf16x8, __builtin_shufflevector(x, y, 0, 1, 2, 3, 4, 5, 6, 7));
}
DI f32x16 mfma(bf16x8 a, bf16x8 b, f32x16 c) { return __builtin_amdgcn_mfma_f32_32x32x16_bf16(a, b, c, 0, 0, 0); }
DI f32x2 fma2(f32x2 a, f32x2 b, f32x2 c) { return __builtin_elementwise_fma(a, b, c); }
DI f32x2 bc(float a) { f32x2 r = {a, a}; return r; }
struct cpx { float r, i; };
DI cpx cmul(cpx a, cpx b) { cpx c; c.r = a.r * b.r - a.i * b.i; c.i = a.r * b.i + a.i * b.r; return c; }
DI cpx cfma(cpx a, cpx b, cpx c) { cpx d; d.r = fmaf(a.r, b.r, fmaf(-a.i, b.i, c.r)); d.i = fmaf(a.r, b.i, fmaf(a.i, b.r, c.i)); return d; }
DI cpx csq(cpx a) { return cmul(a, a); }
DI u32x4 gran2(float a, float b, u32 thi, u32 tlo) {
  u32x4 g = {__builtin_amdgcn_alignbit(tlo, __float_as_uint(a), 8), thi, __builtin_amdgcn_alignbit(tlo, __float_as_uint(b), 8), thi};
  return g;
}
DI u32 granbad(u32x4 g, u32 thi, u32 tw) { return ((g.y ^ thi) | (g.w ^ thi)) | (((g.x ^ tw) | (g.z ^ tw)) >> 24); }
DI float granval(u32 lo) { return __uint_as_float(lo << 8); }
DI bf16x8 pack8(const f32x16& v, int o) {
  u32x4 r = {cvtpk(v[o], v[o + 1]), cvtpk(v[o + 2], v[o + 3]), cvtpk(v[o + 4], v[o + 5]), cvtpk(v[o + 6], v[o + 7])};
  return __builtin_bit_cast(bf16x8, r);
}
DI void sincos_cw(float x, float& sn, float& cs) {
  const float k = __builtin_rintf(x * 0.636619772367581343f);
  float r = fmaf(k, -1.57079601287841796875f, x);
  r = fmaf(k, -3.1391647326017846353e-07f, r);
  r = fmaf(k, -5.3903025299577647655e-15f, r);
  const float z = r * r;
  const float s = fmaf(r * z, fmaf(z, fmaf(z, -1.95152959e-4f, 8.33216087e-3f), -1.66666546e-1f), r);
  const float c = fmaf(z, fmaf(z, fmaf(z, fmaf(z, 2.44331571e-5f, -1.38873162e-3f), 4.16666457e-2f), -0.5f), 1.f);
  const int q = (int)k;
  const float ss = (q & 1) ? c : s, cc = (q & 1) ? s : c;
  sn = (q & 2) ? -ss : ss;
  cs = ((q + 1) & 2) ? -cc : cc;
}
DI float exp_acc(float x) {
  const float t = x * 1.44269502162933349609f;
  const float r = fmaf(x, 1.925963033500011e-8f, fmaf(x, 1.44269502162933349609f, -t));
  const float e = __builtin_amdgcn_exp2f(t);
  return fmaf(e, r * 0.693147182464599609f, e);
}
DI u32 early_epoch(const void* p) { u32 e; asm volatile("s_load_dword %0, %1, 0x0" : "=s"(e) : "s"(p) : "memory"); return e; }
DI void wait_flags(__amdgpu_buffer_rsrc_t rw, u32 off, u32 thi, u32 tl32) {
  for (int it = 0; it < SPIN_MAX; ++it) {
    fence();
    u32x2 f = __builtin_amdgcn_raw_buffer_load_b64(rw, off, 0, AUX_SC1);
    if (__builtin_amdgcn_ballot_w64((f.x != tl32) | (f.y != thi)) == 0) break;
    __builtin_amdgcn_s_sleep(PSL);
  }
  fence();
}
DI void scan_step(int p, f32x2& hr, f32x2& hi, f32x2 ur, f32x2 ui, f32x2 Lr, f32x2 La, f32x2 Lb) {
  f32x2 a, b;
  if (p & 1) { a = fma2(La, hi, ur); sb(); b = fma2(Lb, hr, ui); sb(); hr = fma2(Lr, hr, a); sb(); hi = fma2(Lr, hi, b); sb(); }
  else       { b = fma2(Lb, hr, ui); sb(); a = fma2(La, hi, ur); sb(); hi = fma2(Lr, hi, b); sb(); hr = fma2(Lr, hr, a); sb(); }
}
DI float mx3(float a, float b, float c) { return __builtin_elementwise_maximum(__builtin_elementwise_maximum(a, b), c); }
#define SMX 1
template <class F> DI void softmax2(const f32x16& pa, const f32x16& pb, u32x4* oa, u32x4* ob, F hook) {
  const float L2E = 1.44269504088896340736f;
  const f32x16* p[2] = {&pa, &pb};
  float m[2][5];
  f32x2 nm[2], e[2][8], r[2];
#pragma unroll
  for (int i = 0; i < 5; ++i)
#pragma unroll
    for (int j = 0; j < 2; ++j) m[j][i] = mx3((*p[j])[3 * i], (*p[j])[3 * i + 1], (*p[j])[3 * i + 2]);
  sb();
#pragma unroll
  for (int j = 0; j < 2; ++j) { m[j][0] = mx3(m[j][0], m[j][1], m[j][2]); m[j][3] = mx3(m[j][3], m[j][4], (*p[j])[15]); }
  sb();
#pragma unroll
  for (int j = 0; j < 2; ++j) m[j][0] = __builtin_elementwise_maximum(m[j][0], m[j][3]);
  asm volatile("" : "+v"(m[0][0]), "+v"(m[1][0]));
  hook(0);
  sb();
#pragma unroll
  for (int j = 0; j < 2; ++j) nm[j] = bc(-m[j][0] * L2E);
  sb();
  f32x2 s[2][4];
  auto FM = [&](int i, int j) { e[j & 1][i + (j >> 1)] = fma2(f32x2{(*p[j & 1])[2 * i + (j & 2)], (*p[j & 1])[2 * i + (j & 2) + 1]}, bc(L2E), nm[j & 1]); };
#pragma unroll
  for (int j = 0; j < 4; ++j) FM(0, j);
  sb();
#pragma unroll
  for (int i = 0; i < 8; i += 2) {
    hook(10 + (i >> 1));
    sb();
#pragma unroll
    for (int j = 0; j < 4; ++j) {
      f32x2& t = e[j & 1][i + (j >> 1)];
      t.x = __builtin_amdgcn_exp2f(t.x); sb();
      if (SMX == 2 && i < 6) { FM(i + 2, j); sb(); }
      t.y = __builtin_amdgcn_exp2f(t.y); sb();
      if (SMX == 1 && i < 6) { FM(i + 2, j); sb(); }
      if (i > 0 && (j & 1)) { s[j >> 1][(i >> 1) - 1] = e[j >> 1][i - 2] + e[j >> 1][i - 1]; sb(); }
    }
    hook(1 + (i >> 1));
    sb();
  }
#pragma unroll
  for (int j = 0; j < 2; ++j) s[j][3] = e[j][6] + e[j][7];
  sb();
#pragma unroll
  for (int j = 0; j < 2; ++j) { s[j][0] += s[j][1]; s[j][2] += s[j][3]; }
  sb();
#pragma unroll
  for (int j = 0; j < 2; ++j) s[j][0] += s[j][2];
  asm volatile("" : "+v"(s[0][0]), "+v"(s[1][0]));
  hook(5);
  sb();
#pragma unroll
  for (int j = 0; j < 2; ++j) s[j][0].x += s[j][0].y;
  hook(6);
  sb();
#pragma unroll
  for (int j = 0; j < 2; ++j) r[j] = bc(__builtin_amdgcn_rcpf(s[j][0].x));
  hook(7);
  sb();
#pragma unroll
  for (int i = 0; i < 8; i += 4) {
#pragma unroll
    for (int j = 0; j < 8; ++j) e[j & 1][i + (j >> 1)] *= r[j & 1];
    sb();
    oa[i >> 2] = u32x4{cvtpk(e[0][i].x, e[0][i].y), cvtpk(e[0][i + 1].x, e[0][i + 1].y), cvtpk(e[0][i + 2].x, e[0][i + 2].y), cvtpk(e[0][i + 3].x, e[0][i + 3].y)};
    ob[i >> 2] = u32x4{cvtpk(e[1][i].x, e[1][i].y), cvtpk(e[1][i + 1].x, e[1][i + 1].y), cvtpk(e[1][i + 2].x, e[1][i + 2].y), cvtpk(e[1][i + 3].x, e[1][i + 3].y)};
    hook(8 + (i >> 2));
    sb();
  }
}
#define OUT_AUX 16
DI void store_out(float* out, const f32x16& a0, const f32x16& a1, int row0, int col0, int l) {
  const __amdgpu_buffer_rsrc_t ro = __builtin_amdgcn_make_buffer_rsrc((void*)out, 0, 8388608, 0x00020000);
  const u32 p = ((row0 + 4 * (l >> 5)) * 1024 + col0 + (l & 31)) * 4;
#pragma unroll
  for (int r = 0; r < 16; ++r) {
    __builtin_amdgcn_raw_buffer_store_b32(__float_as_uint(a0[r]), ro, p, (8 * (r >> 2) + (r & 3)) * 4096, OUT_AUX);
    __builtin_amdgcn_raw_buffer_store_b32(__float_as_uint(a1[r]), ro, p + 128, (8 * (r >> 2) + (r & 3)) * 4096, OUT_AUX);
  }
}

#define A_SLOT 32768u
#define A_STG  0u
#define A_SEG  0u
#define A_PART 7168u
#define A_LAM  8192u
#define A_DK   9216u
#define A_PAR  134144u
#define A_PRE  132096u
#define A_PRE2 16384u
#define B_SLOT 45056u
#define B_KVT  81920u
#define B_WO   98304u
#define B_WPHI 135168u
#define B_PRE  136192u
#define B_STG  126976u
#define B_PRE2 159744u
#define YQ (QD == 5 ? 23 : 4 * QD)
#define LPF 3
#define AD 0
#define AB 3
#define BB 0
#define CE 4
#define OPOLL 0
#define GAD 0
#define GAS 0
#define XE 1
#define QD 5

__global__ __launch_bounds__(512, 2) void fused_k(Args) {
  extern __shared__ __attribute__((aligned(16))) char smem_[];
  const int b = blockIdx.x, g = b & 7, jb = b >> 3;
  const int tid = threadIdx.x, l = tid & 63;
  const int w = __builtin_amdgcn_readfirstlane(tid >> 6);
  const bool isA = g >= 4;
  const KARG Args* ka = (const KARG Args*)__builtin_amdgcn_kernarg_segment_ptr();
  u64 kp, kq;
  asm volatile("s_load_dwordx2 %0, %2, %3\n\ts_load_dwordx2 %1, %2, 0x8" : "=&s"(kp), "=&s"(kq) : "s"(ka), "s"(isA ? (w > 5 ? 8 * w + 8 : 48) : 0));
  const int tt = 4 * (g & 3) + (jb >> 3), ct = jb & 7;
  const int tb = tt >> 1, cb = jb & 15;
#define ROT 0
  const int s0 = ROT == 0 ? 0 : (ROT == 2 ? ((ct ^ (jb >> 3)) & 1) * 4 : ((ct + (jb >> 3)) & 3) * 2);
  const int h = l >> 5, l31 = l & 31;
  unsigned char* ws;
  float* out = nullptr;
#define pE ((const void*)(ws + (isA ? WS_EPA : WS_EPB)))
#define rw __builtin_amdgcn_make_buffer_rsrc((void*)ws, 0, (int)WS_BYTES, 0x00020000)
  u32 E = 0;
  const u32 flagoff = WS_FLAG + ((isA ? 0u : 512u) + tt * 32) * FS;

  if (w < 4) {
    u64 pc; u32 len;
    asm volatile("s_getpc_b64 %0\nfk_code_lo:\n\ts_mov_b32 %1, fk_code_hi-fk_code_lo" : "=s"(pc), "=s"(len));
    gpre((isA ? A_PRE : B_PRE) + 1024 + w * 256, ((w + 4 * (jb & 1)) * 64 + l) * 128, srd((const void*)pc, len), 0);
  }
  if (w >= 4) {
    const int w4 = w - 4;
    const u32 lr8 = l >> 3;
    if (isA) {
      const u32 lp = w4 < 2 ? (32 * (lr8 >> 2) + 16 * (lr8 & 1) + ((lr8 >> 1) & 1)) : lr8;
      const u32 rot = w4 < 2 ? ct : 2 * (jb >> 3);
      u32 v0 = (lp * 1024 + (((l & 7) ^ (l >> 4)) << 4)) ^ ((rot & 1) << 6), v1 = v0 ^ 64;
      const u32 rb = w4 < 2 ? (128 * tt + 64 * w4) : 64 * ct;
      const u32 pa = w4 < 2 ? 2048u : 8192u;
      u32 qs[8], qd[8];
#pragma unroll
      for (int p = 0; p < 8; ++p) { const u32 q = (p + rot) & 7; qd[p] = w4 * 8192 + q * 1024; qs[p] = rb * 1024 + q * pa; asm volatile("" : "+s"(qd[p]), "+s"(qs[p])); }
      asm volatile("" : "+v"(v0), "+v"(v1));
      u32 nb = w4 < 2 ? 2097152u : 524288u;
      asm volatile("" : "+s"(nb));
      u64 p0;
      asm volatile("s_waitcnt lgkmcnt(0)\n\ts_mov_b64 %0, %1" : "=&s"(p0) : "s"(kp), "s"(kq));
      const u32x4 rs = srd((const void*)p0, nb);
      auto issue = [&](int i) {
#pragma unroll
        for (int p = 0; p < 8; ++p) gldsx((i & 3) * A_SLOT + qd[p], (p & 1) ? v1 : v0, rs, qs[p] + (((s0 + i) & 7) << 7));
      };
      issue(0);
      asm volatile("" : "+s"(ka));
      ws = ka->ws;
      issue(1);
      if (AD) issue(2);
      gpre(A_PAR + w4 * 256, l * 4, srd(w4 == 0 ? ka->lre : (w4 == 1 ? ka->lim : (w4 == 2 ? ka->ldt : ka->dsk)), 1024u), ct * 128);
      const u32x4 rc = srd((w4 & 1) ? ka->cim : ka->cre, 524288u);
      const u32 vc0 = lr8 * 1024 + (((l & 7) ^ (l >> 4)) << 4), vc1 = vc0 ^ 64;
      u32 rc2 = 2 * (jb >> 3);
      auto issueC = [&](int i) {
        const u32 so = 64 * ct * 1024 + (((ct + i) & 7) << 7);
        const u32 dst = (i < 2 ? 131072u + i * 16384u : (i * 16384u)) + (w4 & 1) * 8192;
#pragma unroll
        for (int p = 0; p < 8; ++p) { const u32 q = (p + rc2) & 7; glds(dst + q * 1024, (p & 1) ? vc1 : vc0, rc, so + q * 8192); }
      };
      WAITV(9 + 8 * AD); bar(); issue(2 + AD);
      E = load_epoch(pE);
      const u64 T = (u64)E * 0x9E3779B97F4A7C15ull + 0x632BE59BD9B4E019ull;
      u32 thi = (u32)(T >> 32), tlo = (u32)(T >> 24) & 0xffu, tw = tlo << 24;
      asm volatile("" : "+s"(thi), "+s"(tlo), "+s"(tw));
      WAITV(8 + 9 * AD); bar(); issue(3 + AD); if (AD) { WAITV(16); bar(); issue(5); }
      const float lr = bf2f(*(const LDS3 u16*)(A_PAR + l * 2)), li = bf2f(*(const LDS3 u16*)(A_PAR + 256 + l * 2));
      float dk = bf2f(*(const LDS3 u16*)(A_PAR + 768 + l * 2));
      const float dt = bf2f(cvtpk(exp_acc(bf2f(*(const LDS3 u16*)(A_PAR + 512 + l * 2))), 0.f) & 0xffffu);
      float mg = exp_acc(dt * lr), th = dt * li, rd = __builtin_amdgcn_rcpf(lr * lr + li * li);
      asm volatile("" : "+v"(mg), "+v"(th), "+v"(rd), "+v"(dk));
      WAITV(8 + 8 * AD); bar(); issue(4 + 2 * AD);
#define CPF 3
      if (CPF == 1) gpre(A_PRE + w4 * 256, (16 * jb + 8 * (w4 >> 1) + lr8) * 1024 + (l & 7) * 128, rc, 0);
      float sn, cs;
      sincos_cw(th, sn, cs);
      asm volatile("" : "+v"(sn), "+v"(cs));
      WAITV(8 + (CPF == 1) + 8 * AD); bar(); issue(5 + 2 * AD);
      cpx lam = {mg * cs, mg * sn}, coef;
      coef.r = ((lam.r - 1.f) * lr + lam.i * li) * rd; coef.i = (lam.i * lr - (lam.r - 1.f) * li) * rd;
      cpx l16 = csq(csq(csq(csq(lam))));
      asm volatile("" : "+v"(lam.r), "+v"(lam.i), "+v"(coef.r), "+v"(coef.i), "+v"(l16.r), "+v"(l16.i));
      if (!AD) { WAITV(8); bar(); issue(6); }
      cpx l128 = csq(csq(csq(l16)));
      cpx l256 = csq(l128), l512 = csq(l256);
      asm volatile("" : "+v"(l128.r), "+v"(l128.i), "+v"(l256.r), "+v"(l256.i), "+v"(l512.r), "+v"(l512.i));
      if (AD) { WAITV(16); bar(); issueC(w4 >> 1); } else { WAITV(8); bar(); issue(7); }
      const int jl = w4 + ((tt - 1 - w4) & ~3), ex = tt - 1 - jl;
      cpx f[4], l32 = csq(l16), l64 = csq(l32);
      f[3] = cpx{1.f, 0.f};
      if (ex & 1) f[3] = l128;
      if (ex & 2) f[3] = cmul(f[3], l256);
#pragma unroll
      for (int m = 2; m >= 0; --m) f[m] = cmul(f[m + 1], l512);
      u32 pw = A_PART + (l >> 5) * 8192 + l31 * 32 + w4 * 8, ga = WS_GA + (64 * ct + l) * 16;
      asm volatile("" : "+v"(f[0].r), "+v"(f[0].i), "+v"(f[1].r), "+v"(f[1].i), "+v"(f[2].r), "+v"(f[2].i), "+v"(f[3].r), "+v"(f[3].i));
      asm volatile("" : "+v"(l32.r), "+v"(l32.i), "+v"(l64.r), "+v"(l64.i), "+v"(pw), "+v"(ga));
      asm volatile("" : "+s"(rc2));
      if (AD) { WAITV(16); bar(); } else { WAITV(8); bar(); issueC(w4 >> 1); }
      if (w4 == 0) { *(LDS3 f32x4*)(A_LAM + l * 16) = f32x4{lam.r, lam.i, coef.r, coef.i}; *(LDS3 float*)(A_DK + l * 4) = dk; }
      asm volatile("s_waitcnt lgkmcnt(0)" ::: "memory");
      WAITV(8); bar(); issueC(2 + (w4 >> 1));
      if (CPF == 3) gpre(A_PRE2 + w4 * 256, (64 * ct + 16 * (jb >> 3) + 8 * (w4 >> 1) + (l & 7)) * 1024 + ((ct + 4 + (lr8 & 3)) & 7) * 128, rc, 0);
#define APRIO 0
      __builtin_amdgcn_s_setprio(APRIO);
      bar();
      if (w4 == 3) {
        cpx t[8];
#pragma unroll
        for (int s = 0; s < 8; ++s) { f32x2 e = *(const LDS3 f32x2*)(A_SEG + s * 512 + l * 8); t[s] = cpx{e.x, e.y}; }
#pragma unroll
        for (int s = 0; s < 4; ++s) t[s] = cfma(l16, t[2 * s], t[2 * s + 1]);
        t[0] = cfma(l64, cfma(l32, t[0], t[1]), cfma(l32, t[2], t[3]));
        __builtin_amdgcn_raw_buffer_store_b128(gran2(t[0].r, t[0].i, thi, tlo), rw, ga + tt * 8192, 0, AUX_SC1);
      }
      if (CE) {
        if (w4 != 3) __builtin_amdgcn_s_sleep(CE);
        issueC(4 + (w4 >> 1));
        issueC(6 + (w4 >> 1));
      }
      cpx P = {0.f, 0.f};
      if (w4 < tt) {
        u32x4 gq[4];
        if (GAD) __builtin_amdgcn_s_sleep(GAD);
        for (int it = 0; it < SPIN_MAX; ++it) {
          fence();
          u32 bad = 0;
#pragma unroll
          for (int m = 0; m < 4; ++m) {
            const int j = jl - 4 * (3 - m);
            if (j >= 0) gq[m] = __builtin_amdgcn_raw_buffer_load_b128(rw, ga, j * 8192, AUX_SC1);
            else gq[m] = u32x4{tw, thi, tw, thi};
          }
#pragma unroll
          for (int m = 0; m < 4; ++m) bad |= granbad(gq[m], thi, tw);
          if (__builtin_amdgcn_ballot_w64(bad != 0) == 0) break;
          if (GAS) __builtin_amdgcn_s_sleep(GAS);
        }
        const cpx Q = cfma(f[1], cpx{granval(gq[1].x), granval(gq[1].z)}, cmul(f[0], cpx{granval(gq[0].x), granval(gq[0].z)}));
        P = cfma(f[3], cpx{granval(gq[3].x), granval(gq[3].z)}, cmul(f[2], cpx{granval(gq[2].x), granval(gq[2].z)}));
        P.r += Q.r; P.i += Q.i;
      }
      *(LDS3 f32x2*)pw = f32x2{P.r, P.i};
      *(LDS3 f32x2*)(pw + 16384) = f32x2{P.r, P.i};
      asm volatile("s_waitcnt vmcnt(0) lgkmcnt(0)\n\ts_barrier" ::: "memory");
      if (!CE) {
        issueC(4 + (w4 >> 1));
        issueC(6 + (w4 >> 1));
        bar();
        WAITV(0);
        bar();
      }
    } else {
      const u32 rot = cb >> 1, rw3 = (cb & 1) + (tb & 1);
      u32 vx0 = (lr8 * 1024 + (((l & 7) ^ (l >> 4)) << 4)) ^ ((rot & 1) << 6), vx1 = vx0 ^ 64;
      u32 vw = (l >> 2) * 3072 + (l & 3) * 16;
      u32 qs[11], qd[11];
#pragma unroll
      for (int p = 0; p < 8; ++p) { const u32 q = 8 * w4 + ((p + rot) & 7); qd[p] = q * 1024; qs[p] = (256 * tb + 8 * q) * 1024; asm volatile("" : "+s"(qd[p]), "+s"(qs[p])); }
#pragma unroll
      for (int p = 0; p < 3; ++p) { const u32 t = p + rw3, pc = 3 * w4 + (t >= 3 ? t - 3 : t); qd[8 + p] = 32768 + pc * 1024; qs[8 + p] = 64 * cb + (pc & 3) * 49152 + (pc >> 2) * 1024; asm volatile("" : "+s"(qd[8 + p]), "+s"(qs[8 + p])); }
      asm volatile("" : "+v"(vx0), "+v"(vx1), "+v"(vw));
      u64 p0, p1;
      asm volatile("s_waitcnt lgkmcnt(0)\n\ts_mov_b64 %0, %2\n\ts_mov_b64 %1, %3" : "=&s"(p0), "=&s"(p1) : "s"(kp), "s"(kq));
      const u32x4 rx = srd((const void*)p0, 2097152u), rq = srd((const void*)p1, 1572864u);
      auto issue = [&](int i) {
        const u32 ks = (s0 + i) & 7, dst = (i % 3) * B_SLOT;
#pragma unroll
        for (int p = 0; p < 8; ++p) {
          gldsx(dst + qd[p], (p & 1) ? vx1 : vx0, rx, qs[p] + (ks << 7));
          if (p % 3 == 0) gldsw(dst + qd[8 + p / 3], vw, rq, qs[8 + p / 3] + ks * 64 * 3072);
        }
      };
      issue(0);
      asm volatile("" : "+s"(ka));
      ws = ka->ws;
      if (w4 == 0) glds(B_WPHI, l * 16, srd(ka->wphi, 16384u), cb * 1024);
      issue(1);
      WAITV(11); bar(); issue(2);
      WAITV(11); bar(); if (BB) bar(); issue(3);
      WAITV(11); bar(); if (BB) bar(); issue(4);
      WAITV(11); bar(); if (BB) bar(); issue(5);
      WAITV(11); bar(); if (BB) bar(); issue(6);
      WAITV(11); bar(); if (BB) bar(); issue(7);
      WAITV(11); bar();
      WAITV(0); bar();
#define OPF 1
      if (OPF == 1) gpre(B_PRE2 + w4 * 256, (16 * jb + 4 * w4 + (lr8 & 3)) * 1024 + (l & 7) * 128, srd(ka->wo, 524288u), 0);
      E = load_epoch(pE);
    }
  } else {
    ws = ka->ws; out = ka->out;
    asm volatile("s_waitcnt lgkmcnt(0)" : "+s"(ws), "+s"(out) : "s"(kp), "s"(kq));
  }
  u32 thi = 0, tl32 = 0, tlo = 0, tw = 0;
  auto tags = [&]() {
    const u64 T = (u64)E * 0x9E3779B97F4A7C15ull + 0x632BE59BD9B4E019ull;
    thi = (u32)(T >> 32); tl32 = (u32)T; tlo = (u32)(T >> 24) & 0xffu; tw = tlo << 24;
  };
  const int wm = (w >> 1) & 1, wn = w & 1;
  const int G = l >> 4, tr_r = (l >> 2) & 3, tr_c = l & 3;
  const u32 fragrow = l31 * 128;
  const u32 fsw = (l31 >> 1) & 7;

  if (isA) {
    if (w < 4) {
      u32 ax[2][4], aw[2][4];
#pragma unroll
      for (int k = 0; k < 4; ++k) {
        const u32 t = fragrow + (((2 * k + h) ^ fsw) << 4);
        ax[0][k] = t + wm * 8192; aw[0][k] = t + 16384 + wn * 4096;
        ax[1][k] = ax[0][k] + 65536; aw[1][k] = aw[0][k] + 65536;
        asm volatile("" : "+v"(ax[0][k]), "+v"(ax[1][k]), "+v"(aw[0][k]), "+v"(aw[1][k]));
      }
      const u32 rho = 64 * (w >> 1) + 16 * (G >> 1) + 8 * (tr_r >> 1) + 4 * (w & 1) + 2 * (tr_r & 1);
      u32 xs0 = rho * 128 + (((2 * (G & 1) + (tr_c >> 1)) ^ ((rho >> 1) & 7)) << 4) + (tr_c & 1) * 8 + (((ct - s0) & 3) << 15);
      u32 xs1 = xs0 ^ 64;
      asm volatile("" : "+v"(xs0), "+v"(xs1));
      f32x16 aR[2], aI[2];
#pragma unroll
      for (int i = 0; i < 16; ++i) { aR[0][i] = 0.f; aR[1][i] = 0.f; aI[0][i] = 0.f; aI[1][i] = 0.f; }
      s16x4 xk[8];
#pragma unroll
      for (int i = 0; i < 8; ++i) xk[i] = s16x4{0, 0, 0, 0};
      const int sx = (ct - s0) & 7;
      bf16x8 fx0[2], fx1[2], fwr[2], fwi[2];
      bar();
      fx0[0] = rd128(ax[0][0]); fwr[0] = rd128(aw[0][0]); fx1[0] = rd128(ax[0][0] + 4096); fwi[0] = rd128(aw[0][0] + 8192);
      sb();
      f32x4 lc;
      u32 sa = A_SEG + (4 * wm + 2 * h) * 512 + (32 * wn + l31) * 8;
      asm volatile("" : "+v"(sa));
#pragma unroll
      for (int n = 0; n < 32; ++n) {
        const int s = n >> 2, k = n & 3, c = n & 1, m = n + 1, hb = (m >> 3) & 1, so = ((m >> 2) & 1) * 32768;
        aR[0] = mfma(fx0[c], fwr[c], aR[0]); sb();
        if (AB == 3) {
        if (n == 15) { asm volatile("s_waitcnt vmcnt(0) lgkmcnt(0)\n\ts_barrier" ::: "memory"); sb(); }
        else if (k == 3 && s < 7) { bar_l(); sb(); }
        } else if (AB < 3) {
        if (n == 12 + AB) { asm volatile("s_waitcnt vmcnt(0)\n\ts_barrier" ::: "memory"); sb(); }
        else if (k == AB && s < 7) { bar(); sb(); }
        }
        if (n < 31) { fx0[c ^ 1] = rd128(ax[hb][m & 3] + so); fwr[c ^ 1] = rd128(aw[hb][m & 3] + so); sb(); }
        else { lc = *(const LDS3 f32x4*)(A_LAM + (32 * wn + l31) * 16); sb(); }
        aR[1] = mfma(fx1[c], fwr[c], aR[1]); sb();
        if (n < 31) { fx1[c ^ 1] = rd128(ax[hb][m & 3] + so + 4096); fwi[c ^ 1] = rd128(aw[hb][m & 3] + so + 8192); sb(); }
        aI[0] = mfma(fx0[c], fwi[c], aI[0]); sb();
        if (k == 0 && __builtin_expect(sx == s, XE)) {
#pragma unroll
          for (int q = 0; q < 4; ++q) {
            const u32 o = (32 * (q & 1) + (q >> 1)) * 128;
            xk[q] = __builtin_amdgcn_ds_read_tr16_b64_v4i16((LDS3 s16x4*)(o + xs0));
            xk[4 + q] = __builtin_amdgcn_ds_read_tr16_b64_v4i16((LDS3 s16x4*)(o + xs1));
          }
        }
        sb();
        aI[1] = mfma(fx1[c], fwi[c], aI[1]); sb();
        if (AB == 4) {
        if (n == 14) { asm volatile("s_waitcnt vmcnt(0)\n\ts_barrier" ::: "memory"); sb(); }
        else if (k == 2 && s < 7) { bar(); sb(); }
        }
      }
      E = early_epoch(pE);
      const cpx lam = {lc.x, lc.y}, coef = {lc.z, lc.w};
      const f32x2 Lr = bc(lam.r), Li = bc(lam.i), nLi = bc(-lam.i);
      {
        f32x2 hr = {aR[0][0], aR[0][1]}, hi = {aI[0][0], aI[0][1]};
#pragma unroll
        for (int p = 1; p < 16; ++p) {
          const f32x2 ur = {aR[p >> 3][2 * (p & 7)], aR[p >> 3][2 * (p & 7) + 1]}, ui = {aI[p >> 3][2 * (p & 7)], aI[p >> 3][2 * (p & 7) + 1]};
          scan_step(p, hr, hi, ur, ui, Lr, nLi, Li);
        }
        *(LDS3 f32x2*)sa = f32x2{hr.x, hi.x};
        *(LDS3 f32x2*)(sa + 512) = f32x2{hr.y, hi.y};
      }
      asm volatile("s_waitcnt lgkmcnt(0)\n\ts_barrier" : "+s"(E) :: "memory");
      const float dk0 = *(const LDS3 float*)(A_DK + l31 * 4), dk1 = *(const LDS3 float*)(A_DK + 128 + l31 * 4);
      const cpx l16 = csq(csq(csq(csq(lam))));
      const cpx c2 = {2.f * coef.r, 2.f * coef.i};
      cpx pre0 = {0.f, 0.f}, pre1;
      {
        const int sg = 4 * wm + 2 * h;
        cpx e0 = {0.f, 0.f};
#pragma unroll
        for (int s = 0; s < 7; ++s) {
          f32x2 e = *(const LDS3 f32x2*)(A_SEG + s * 512 + (32 * wn + l31) * 8);
          cpx t = cfma(l16, pre0, cpx{e.x, e.y});
          if (s < sg) pre0 = t;
          if (s == sg) e0 = cpx{e.x, e.y};
        }
        pre1 = cfma(l16, pre0, e0);
      }
      pre0 = cmul(c2, pre0); pre1 = cmul(c2, pre1);
      cpx m0 = c2;
      {
        const cpx l32 = csq(l16), l64 = csq(l32);
        if (h) m0 = cmul(m0, l32);
        if (wm) m0 = cmul(m0, l64);
      }
      const cpx m1 = cmul(m0, l16);
#pragma unroll
      for (int p = 0; p < 16; ++p) {
        const f32x2 ur = {aR[p >> 3][2 * (p & 7)], aR[p >> 3][2 * (p & 7) + 1]}, ui = {aI[p >> 3][2 * (p & 7)], aI[p >> 3][2 * (p & 7) + 1]};
        const f32x2 vr = fma2(bc(c2.r), ur, bc(-c2.i) * ui), vi = fma2(bc(-c2.r), ui, bc(-c2.i) * ur);
        aR[p >> 3][2 * (p & 7)] = vr.x; aR[p >> 3][2 * (p & 7) + 1] = vr.y;
        aI[p >> 3][2 * (p & 7)] = vi.x; aI[p >> 3][2 * (p & 7) + 1] = vi.y;
      }
      f32x16 y0, y1;
#pragma unroll
      for (int q = 0; q < 4; ++q) {
        const u32x2 t0 = __builtin_bit_cast(u32x2, xk[q]), t1 = __builtin_bit_cast(u32x2, xk[4 + q]);
#pragma unroll
        for (int e = 0; e < 2; ++e) {
          const u32 r0 = cvtpk(bf2f(t0[e] & 0xffffu) * dk0, __uint_as_float(t0[e] & 0xffff0000u) * dk0);
          const u32 r1 = cvtpk(bf2f(t1[e] & 0xffffu) * dk1, __uint_as_float(t1[e] & 0xffff0000u) * dk1);
          y0[4 * q + 2 * e] = __uint_as_float(r0 << 16); y0[4 * q + 2 * e + 1] = __uint_as_float(r0 & 0xffff0000u);
          y1[4 * q + 2 * e] = __uint_as_float(r1 << 16); y1[4 * q + 2 * e + 1] = __uint_as_float(r1 & 0xffff0000u);
        }
      }
      f32x2 Mr = {m0.r, m1.r}, nMr = -Mr, nMi = {-m0.i, -m1.i}, Pr = {pre0.r, pre1.r}, nPi = {-pre0.i, -pre1.i};
      const u32 chr = 8 * (G >> 1) + tr_r;
      const u32 fr = (tr_r & 1) | ((tr_r & 2) << 1);
      const u32 cq = 4 * wn + 2 * (G & 1) + (tr_c >> 1);
      u32 rb0 = A_STG + w * 8192 + chr * 128 + (tr_c & 1) * 8 + ((cq ^ fr) << 4), rb1 = (rb0 + 512) ^ 32, rb2 = rb0 ^ 64, rb3 = rb1 ^ 64;
      u32 wb[4];
#pragma unroll
      for (int j = 0; j < 4; ++j) wb[j] = A_STG + w * 8192 + l31 * 128 + (((4 * h + j) ^ ((l31 & 1) | ((l31 & 2) << 1) | ((l31 & 4) >> 1))) << 4);
      u32 hoff = WS_HA + ((tt * 4 + 2 * wm + wn) * 64 + ct * 8 + 2 * wn) * 1024 + l * 16, hof2 = hoff + 4096, hof3 = hoff ^ 65536, hof4 = hof2 ^ 65536;
      u32 pa = A_PART + w * 8192 + l31 * 32;
      u32 fo = l == 0 ? flagoff + (wm * 16 + ct * 2 + wn) * FS : 0x80000000u;
      u32 lp = WS_HA + ((tt * 4 + 2 * wm + (l >> 5)) * 64 + ct * 8 + 2 * wn + ((l >> 4) & 1) * 4 + ((l >> 3) & 1)) * 1024 + (l & 7) * 128;
      asm volatile("" : "+v"(y0), "+v"(y1), "+v"(aR[0]), "+v"(aR[1]), "+v"(aI[0]), "+v"(aI[1]));
      asm volatile("" : "+v"(Mr), "+v"(nMr), "+v"(nMi), "+v"(Pr), "+v"(nPi), "+v"(rb0), "+v"(rb1), "+v"(rb2), "+v"(rb3), "+v"(pa), "+v"(fo), "+v"(lp));
      asm volatile("" : "+v"(wb[0]), "+v"(wb[1]), "+v"(wb[2]), "+v"(wb[3]), "+v"(hoff), "+v"(hof2), "+v"(hof3), "+v"(hof4));
      tags();
      u32x2 tg = {tl32, thi};
      asm volatile("" : "+s"(thi), "+s"(tl32), "+v"(tg));
      bar();
      bf16x8 own[8];
      {
        const f32x4 p0 = *(const LDS3 f32x4*)pa, p1 = *(const LDS3 f32x4*)(pa + 16);
        sb();
        const f32x2 cc = (f32x2{p0.x, p0.y} + f32x2{p0.z, p0.w}) + (f32x2{p1.x, p1.y} + f32x2{p1.z, p1.w});
        f32x2 hr = fma2(nMi, bc(cc.y), Pr), hi = fma2(nMi, bc(cc.x), nPi);
        sb();
        hr = fma2(Mr, bc(cc.x), hr); sb();
        hi = fma2(nMr, bc(cc.y), hi); sb();
        u32x4 v[4];
        f32x2 qr, qi;
#pragma unroll
        for (int p = 0; p < 16; ++p) {
          const f32x2 ur = {aR[p >> 3][2 * (p & 7)], aR[p >> 3][2 * (p & 7) + 1]}, ui = {aI[p >> 3][2 * (p & 7)], aI[p >> 3][2 * (p & 7) + 1]};
          scan_step(p + 1, hr, hi, ur, ui, Lr, Li, nLi);
          if (p & 1) {
            const int j = (p & 7) >> 1;
            v[2][j] = cvtpk(qi.x, hi.x); v[3][j] = cvtpk(qi.y, hi.y); v[0][j] = cvtpk(qr.x, hr.x); v[1][j] = cvtpk(qr.y, hr.y);
            sb();
          } else { qr = hr; qi = hi; }
          if ((p & 7) == 7) {
#pragma unroll
            for (int j = 0; j < 4; ++j) *(LDS3 u32x4*)(wb[2 * (j & 1) + (p >> 3)] + (j >> 1) * 4096) = v[j];
            sb();
          }
        }
        bf16x8 nb[4];
#pragma unroll
        for (int j = 0; j < 4; ++j) { own[j] = rdtr(rb0 + j * 2048, rb1 + j * 2048); nb[j] = rdtr(rb2 + j * 2048, rb3 + j * 2048); sb(); }
#pragma unroll
        for (int j = 0; j < 4; ++j) {
          __builtin_amdgcn_raw_buffer_store_b128(__builtin_bit_cast(u32x4, own[j]), rw, ((j & 2) ? hof2 : hoff) + (j & 1) * 1024, 0, AUX_SC1);
          __builtin_amdgcn_raw_buffer_store_b128(__builtin_bit_cast(u32x4, nb[j]), rw, ((j & 2) ? hof4 : hof3) + (j & 1) * 1024, 0, AUX_SC1);
          sb();
        }
      }
      bar();
      WAITV(0);
      __builtin_amdgcn_raw_buffer_store_b64(tg, rw, fo, 0, AUX_SC1);
      const u32 warm = __builtin_amdgcn_raw_buffer_load_b32(rw, lp, 0, 0);
      u32 ca[4], cb[4];
#pragma unroll
      for (int k = 0; k < 4; ++k) { const u32 t = fragrow + (((2 * k + h) ^ fsw) << 4); ca[k] = t + 98304; cb[k] = t + 32768; }
      bf16x8 bq[3][2];
      {
#pragma unroll
        for (int j = 0; j < 4; ++j) own[4 + j] = rdtr((rb0 ^ 8192) + j * 2048, (rb1 ^ 8192) + j * 2048);
        const u32 c0[2] = {ca[0] ^ (wn * 64), ca[1] ^ (wn * 64)};
        auto cad = [&](int n) { return (c0[n & 1] ^ ((n >> 2) * 64)) + 32768 + ((n >> 1) & 1) * 8192; };
        bq[0][0] = rd128(cad(0)); bq[0][1] = rd128(cad(0) + 4096);
        bq[1][0] = rd128(cad(1)); bq[1][1] = rd128(cad(1) + 4096);
        sb();
#pragma unroll
        for (int n = 0; n < 8; ++n) {
          y0 = mfma(own[n], bq[n % 3][0], y0); sb();
          if (n + 2 < 8) bq[(n + 2) % 3][0] = rd128(cad(n + 2));
          sb();
          y1 = mfma(own[n], bq[n % 3][1], y1); sb();
          if (n + 2 < 8) bq[(n + 2) % 3][1] = rd128(cad(n + 2) + 4096);
          sb();
        }
      }
      if (OPOLL & 1) {
        if (w == 0) { if (PDA) __builtin_amdgcn_s_sleep(PDA); wait_flags(rw, flagoff + l31 * FS, thi, tl32); }
        bar();
      } else {
        if (!CE) bar();
        if (PDA) __builtin_amdgcn_s_sleep(PDA);
        wait_flags(rw, flagoff + (wm * 16 + (l & 15)) * FS, thi, tl32);
      }
      asm volatile("" :: "v"(warm));
      {
        const u32 ha = WS_HA + (tt * 4 + w) * 65536 + l * 16;
        u32x4 af[4][8];
        auto ld = [&](int i) {
          const u32 so = ((ct + i) & 7) * 8192;
#pragma unroll
          for (int k = 0; k < 8; ++k) af[i & 3][k] = __builtin_amdgcn_raw_buffer_load_b128(rw, ha + k * 1024, so, AUX_SC1);
        };
        auto cad = [&](int n) { const int i = n >> 3, k = n & 7; return (i < 2 ? ca[k & 3] + 32768 + i * 16384 : (i < 6 ? cb[k & 3] + (i - 2) * 16384 : ca[k & 3] + (i - 6) * 16384)) + (k >> 2) * 8192; };
        ld(1); ld(2); ld(3);
        bq[8 % 3][0] = rd128(cad(8)); bq[8 % 3][1] = rd128(cad(8) + 4096);
        bq[9 % 3][0] = rd128(cad(9)); bq[9 % 3][1] = rd128(cad(9) + 4096);
        sb();
#pragma unroll
        for (int n = 8; n < 64; ++n) {
          const bf16x8 av = __builtin_bit_cast(bf16x8, af[(n >> 3) & 3][n & 7]);
          y0 = mfma(av, bq[n % 3][0], y0); sb();
          if (n + 2 < 64) bq[(n + 2) % 3][0] = rd128(cad(n + 2));
          if (n + 24 < 64) af[((n >> 3) + 3) & 3][n & 7] = __builtin_amdgcn_raw_buffer_load_b128(rw, ha + (n & 7) * 1024, ((ct + (n >> 3) + 3) & 7) * 8192, AUX_SC1);
          sb();
          y1 = mfma(av, bq[n % 3][1], y1); sb();
          if (n + 2 < 64) bq[(n + 2) % 3][1] = rd128(cad(n + 2) + 4096);
          sb();
        }
      }
      store_out(out, y0, y1, 128 * tt + 32 * w, 64 * ct, l);
      if (tt == 15 && ct == 0 && w == 0 && l == 0) __builtin_amdgcn_raw_buffer_store_b32(E + 1, rw, WS_EPA, 0, AUX_SC1);
    }
  } else {
    u32x4 pq[2][2][2];
    u32 kva = B_KVT + (l31 < 16 ? (8 * ((l31 >> 2) & 1) + 4 * (l31 >> 3) + (l31 & 3)) : 16) * 64 + h * 32;
    u32 ooff = WS_OB + ((tb * 8 + 2 * w) * 32 + 2 * cb) * 1024 + l * 16;
    u32 ooff2 = ooff + 32768;
    if (w < 4) {
      asm volatile("" : "+v"(kva), "+v"(ooff), "+v"(ooff2));
      u32 ax[2][4], aw[2];
#pragma unroll
      for (int k = 0; k < 4; ++k) {
        ax[0][k] = fragrow + (((2 * k + h) ^ fsw) << 4) + w * 8192; ax[1][k] = ax[0][k] + 2 * B_SLOT;
        asm volatile("" : "+v"(ax[0][k]), "+v"(ax[1][k]));
      }
      aw[0] = 32768 + (8 * (G >> 1) + tr_r) * 64 + (2 * (G & 1) + (tr_c >> 1)) * 16 + (tr_c & 1) * 8;
      aw[1] = aw[0] + 2 * B_SLOT;
      asm volatile("" : "+v"(aw[0]), "+v"(aw[1]));
      const u32 stg = B_STG + w * 8192;
      u32 wb0 = stg + l31 * 64 + (((2 * h) ^ ((l31 >> 1) & 3)) << 4), wb1 = wb0 ^ 16;
      u32 ra = stg + (4 * (G >> 1) + tr_r) * 64 + (((2 * (G & 1) + (tr_c >> 1)) ^ (2 * (G >> 1) + (tr_r >> 1))) << 4) + (tr_c & 1) * 8;
      u32 pa0 = stg + (l31 < 16 ? l31 : 16) * 144 + h * 16, pa1 = stg + 2448 + (l31 >= 16 ? l31 - 16 : 16) * 144 + h * 16;
      u32 pad = B_STG + (w == 2 ? (l >> 2 & 1) * 2448 + 16 * 144 + (l & 3) * 32 : w * 2448 + (l >> 2) * 144 + (l & 3) * 32), pad2 = pad + 16384;
      u32 pof = w == 2 && l >= 8 ? 0x80000000u : WS_G1 + cb * 24576 + tb * 1024 + w * 8192 + l * 16;
      asm volatile("" : "+v"(wb0), "+v"(wb1), "+v"(ra), "+v"(pa0), "+v"(pa1), "+v"(pad), "+v"(pad2), "+v"(pof));
      f32x16 q[2], kk[2], v[2];
      bf16x8 wf[2];
#pragma unroll
      for (int i = 0; i < 16; ++i) { q[0][i] = 0.f; q[1][i] = 0.f; kk[0][i] = 0.f; kk[1][i] = 0.f; v[0][i] = 0.f; v[1][i] = 0.f; }
      bf16x8 fx0[2], fx1[2], fwq[2], fwk[2], fwv[2];
      E = early_epoch(pE);
      {
        const u32x4 pz = {~0u, ~0u, ~0u, ~0u};
        if (tid < 136) __builtin_amdgcn_raw_buffer_store_b128(pz, rw, WS_G1 + cb * 24576 + w * 8192 + tb * 1024 + l * 16, 0, AUX_SC1);
      }
      bar();
      fwk[0] = rdtr(aw[0] + 4096, aw[0] + 4352); fx0[0] = rd128(ax[0][0]); fx1[0] = rd128(ax[0][0] + 4096);
      fwv[0] = rdtr(aw[0] + 8192, aw[0] + 8448); fwq[0] = rdtr(aw[0], aw[0] + 256);
      {
        const u32 pa = B_WPHI + (4 * (G >> 1) + tr_r) * 32 + (2 * (G & 1) + (tr_c >> 1)) * 8;
#pragma unroll
        for (int hd = 0; hd < 2; ++hd) wf[hd] = rdtr(pa + hd * 512, pa + hd * 512 + 256);
      }
      sb();
#pragma unroll
      for (int n = 0; n < 32; ++n) {
        const int s = n >> 2, k = n & 3, c = n & 1, m = n + 1, sl = (m >> 2) % 3, hb = sl == 2, so = sl == 1 ? B_SLOT : 0, wo_ = so + (m & 3) * 1024;
        kk[0] = mfma(fwk[c], fx0[c], kk[0]); sb();
        if (BB == 0) {
        if (n == 11) { asm volatile("s_waitcnt vmcnt(0) lgkmcnt(0)\n\ts_barrier" : "+s"(E) :: "memory"); sb(); }
        else if (k == 3 && s < 7) { bar_l(); sb(); }
        } else if (k == 3 - BB && s < 7) { bar(); sb(); }
        if (n < 31) { fwk[c ^ 1] = rdtr(aw[hb] + wo_ + 4096, aw[hb] + wo_ + 4352); sb(); }
        kk[1] = mfma(fwk[c], fx1[c], kk[1]); sb();
        if (n < 31) { fx0[c ^ 1] = rd128(ax[hb][m & 3] + so); fx1[c ^ 1] = rd128(ax[hb][m & 3] + so + 4096); sb(); }
        v[0] = mfma(fx0[c], fwv[c], v[0]); sb();
        if (n < 31) { fwv[c ^ 1] = rdtr(aw[hb] + wo_ + 8192, aw[hb] + wo_ + 8448); sb(); }
        v[1] = mfma(fx1[c], fwv[c], v[1]); sb();
        if (m < 4 * QD) { fwq[c ^ 1] = rdtr(aw[hb] + wo_, aw[hb] + wo_ + 256); sb(); }
        if (s < QD) { q[0] = mfma(fwq[c], fx0[c], q[0]); sb(); }
        if (BB) {
        if (n == 11) { asm volatile("s_waitcnt vmcnt(0) lgkmcnt(0)\n\ts_barrier" : "+s"(E) :: "memory"); sb(); }
        else if (k == 3 && s < 5) { bar(); sb(); }
        }
        if (n == 11) { tags(); asm volatile("" : "+s"(thi), "+s"(tl32)); sb(); }
        if (n == 1 || n == 2) {
          const u32 sgn = ((l >> 2) & 1) ? 0x80008000u : 0u;
          u32x4 t = __builtin_bit_cast(u32x4, wf[n - 1]);
          t.x ^= sgn; t.y ^= sgn; t.z ^= sgn; t.w ^= sgn;
          asm volatile("" : "+v"(t));
          wf[n - 1] = __builtin_bit_cast(bf16x8, t);
        }
        if (n == 12 && tid == 0) __builtin_amdgcn_raw_buffer_store_b64(u32x2{tl32, thi}, rw, WS_PF + (cb * 8 + tb) * 8, 0, AUX_SC1);
        sb();
        if (s < QD) { q[1] = mfma(fwq[c], fx1[c], q[1]); sb(); }
      }
      f32x16 zero;
#pragma unroll
      for (int i = 0; i < 16; ++i) zero[i] = 0.f;
      {
        f32x16 p[4], kv[2];
        u32x4 vb[2][4], o[2][2];
        bf16x8 fa[4];
        const bool lo = l31 < 16;
        const u32 one = 0x3F803F80u;
        auto vpack = [&](int i) {
          const u32x4 t = __builtin_bit_cast(u32x4, pack8(v[i >> 1], 8 * (i & 1)));
          vb[0][i] = u32x4{lo ? t.x : one, lo ? t.y : one, lo ? t.z : one, lo ? t.w : one};
          vb[1][i] = u32x4{lo ? one : t.x, lo ? one : t.y, lo ? one : t.z, lo ? one : t.w};
        };
#pragma unroll
        for (int i = 0; i < 4; ++i) { p[i] = mfma(wf[i & 1], pack8(kk[i >> 1], 8 * (i & 1)), zero); sb(); }
        vpack(0); vpack(1);
        sb();
        softmax2(p[0], p[1], o[0], o[1], [&](int i) {
          if (i == 8 || i == 9) { *(LDS3 u32x4*)((i == 8 ? wb0 : wb1)) = o[0][i - 8]; *(LDS3 u32x4*)((i == 8 ? wb0 : wb1) + 4096) = o[1][i - 8]; }
        });
        softmax2(p[2], p[3], o[0], o[1], [&](int i) {
          if (i == 0) { fa[0] = rdtr(ra, ra + 512); fa[1] = rdtr(ra + 4096, ra + 4096 + 512); fa[2] = rdtr(ra + 1024, ra + 1536); fa[3] = rdtr(ra + 5120, ra + 5120 + 512); }
          if (i == 12) { kv[0] = mfma(fa[0], __builtin_bit_cast(bf16x8, vb[0][0]), zero); kv[1] = mfma(fa[1], __builtin_bit_cast(bf16x8, vb[1][0]), zero); }
          if (i == 13) { kv[0] = mfma(fa[2], __builtin_bit_cast(bf16x8, vb[0][1]), kv[0]); kv[1] = mfma(fa[3], __builtin_bit_cast(bf16x8, vb[1][1]), kv[1]); }
          if (i == 8 || i == 9) { *(LDS3 u32x4*)((i == 8 ? wb0 : wb1) + 2048) = o[0][i - 8]; *(LDS3 u32x4*)((i == 8 ? wb0 : wb1) + 6144) = o[1][i - 8]; }
        });
        fa[0] = rdtr(ra + 2048, ra + 2560); fa[1] = rdtr(ra + 6144, ra + 6144 + 512); fa[2] = rdtr(ra + 3072, ra + 3584); fa[3] = rdtr(ra + 7168, ra + 7168 + 512);
        sb();
        vpack(2); vpack(3);
        sb();
        kv[0] = mfma(fa[0], __builtin_bit_cast(bf16x8, vb[0][2]), kv[0]);
        kv[1] = mfma(fa[1], __builtin_bit_cast(bf16x8, vb[1][2]), kv[1]);
        kv[0] = mfma(fa[2], __builtin_bit_cast(bf16x8, vb[0][3]), kv[0]);
        kv[1] = mfma(fa[3], __builtin_bit_cast(bf16x8, vb[1][3]), kv[1]);
        sb();
        if (l31 <= 16)
#pragma unroll
          for (int qq = 0; qq < 4; ++qq) *(LDS3 f32x4*)(pa0 + qq * 32) = f32x4{kv[0][4 * qq], kv[0][4 * qq + 1], kv[0][4 * qq + 2], kv[0][4 * qq + 3]};
        if (l31 >= 15)
#pragma unroll
          for (int qq = 0; qq < 4; ++qq) *(LDS3 f32x4*)(pa1 + qq * 32) = f32x4{kv[1][4 * qq], kv[1][4 * qq + 1], kv[1][4 * qq + 2], kv[1][4 * qq + 3]};
      }
      bar_l();
      if (w < 3) {
        f32x4 t[8];
#pragma unroll
        for (int i = 0; i < 8; ++i) t[i] = *(const LDS3 f32x4*)(((i & 2) ? pad2 : pad) + (i & 1) * 8192 + (i >> 2) * 16);
        sb();
        const f32x4 t0 = (t[0] + t[1]) + (t[2] + t[3]), t1 = (t[4] + t[5]) + (t[6] + t[7]);
        const u32x4 r = {umin_(cvtpk(t0.x, t0.y), ~1u), umin_(cvtpk(t0.z, t0.w), ~1u), umin_(cvtpk(t1.x, t1.y), ~1u), umin_(cvtpk(t1.z, t1.w), ~1u)};
        __builtin_amdgcn_raw_buffer_store_b128(r, rw, pof, 0, AUX_SC1);
      }
      {
        auto rq = [&](int m) {
          const int sl = (m >> 2) % 3, hb = sl == 2, so = sl == 1 ? B_SLOT : 0, wo_ = so + (m & 3) * 1024;
          fwq[m & 1] = rdtr(aw[hb] + wo_, aw[hb] + wo_ + 256); fx0[m & 1] = rd128(ax[hb][m & 3] + so); fx1[m & 1] = rd128(ax[hb][m & 3] + so + 4096);
        };
        if (QD < 8) rq(4 * QD);
        sb();
#pragma unroll
        for (int n = 4 * QD; n < 32; ++n) {
          if (n < 31) rq(n + 1);
          sb();
          q[0] = mfma(fwq[n & 1], fx0[n & 1], q[0]); sb();
          q[1] = mfma(fwq[n & 1], fx1[n & 1], q[1]); sb();
          if (n == YQ) { bar(); sb(); }
        }
      }
      {
        f32x16 p[4];
#pragma unroll
        for (int i = 0; i < 4; ++i) { p[i] = mfma(wf[i & 1], pack8(q[i >> 1], 8 * (i & 1)), zero); sb(); }
        softmax2(p[0], p[1], pq[0][0], pq[0][1], [&](int) {});
        softmax2(p[2], p[3], pq[1][0], pq[1][1], [&](int) {});
      }
      asm volatile("" : "+v"(pq[0][0][0]), "+v"(pq[0][0][1]), "+v"(pq[0][1][0]), "+v"(pq[0][1][1]), "+v"(pq[1][0][0]), "+v"(pq[1][0][1]), "+v"(pq[1][1][0]), "+v"(pq[1][1][1]));
    } else {
      tags();
      wait_flags(rw, WS_PF + cb * 64 + (l & 7) * 8, thi, tl32);
#define HPRIO 3
      __builtin_amdgcn_s_setprio(HPRIO);
      bar();
      bar();
      {
        const u32x4 ro = srd(ka->wo, 524288u);
        const u32 vo = (l >> 3) * 1024 + (((l & 7) ^ (((l >> 4) & 1) << 2)) << 4);
        const u32 ro2 = 2 * (jb >> 3);
#define WOW 4
        if ((w & 3) < WOW)
#pragma unroll 1
        for (u32 i = w & 3; i < 8; i += WOW) {
          const u32 so = (((ct + i) & 7) << 16) + 128 * ct;
#pragma unroll
          for (int p = 0; p < 8; ++p) { const u32 q = (p + ro2) & 7; glds(B_WO + i * 8192 + q * 1024, vo, ro, so + q * 8192); }
        }
      }
    }
    if (w >= 5) {
      const u32 off = WS_G1 + cb * 24576 + (w == 5 ? 16384 + (l & 7) * 16 : (w & 1) * 8192 + l * 16);
      u32x4 gq[8];
      const u32x4 rg = srd(ws, WS_BYTES);
      u32 pend = 0xffu;
      for (int it = 0; it < SPIN_MAX; ++it) {
        asm volatile(
          "s_bitcmp1_b32 %8, 7\n\ts_cselect_b64 exec, -1, 0\n\tbuffer_load_dwordx4 %0, %9, %10, %11 offen offset:0 sc1\n\t"
          "s_bitcmp1_b32 %8, 6\n\ts_cselect_b64 exec, -1, 0\n\tbuffer_load_dwordx4 %1, %9, %10, %11 offen offset:1024 sc1\n\t"
          "s_bitcmp1_b32 %8, 5\n\ts_cselect_b64 exec, -1, 0\n\tbuffer_load_dwordx4 %2, %9, %10, %11 offen offset:2048 sc1\n\t"
          "s_bitcmp1_b32 %8, 4\n\ts_cselect_b64 exec, -1, 0\n\tbuffer_load_dwordx4 %3, %9, %10, %11 offen offset:3072 sc1\n\t"
          "s_bitcmp1_b32 %8, 3\n\ts_cselect_b64 exec, -1, 0\n\tbuffer_load_dwordx4 %4, %9, %10, %12 offen offset:0 sc1\n\t"
          "s_bitcmp1_b32 %8, 2\n\ts_cselect_b64 exec, -1, 0\n\tbuffer_load_dwordx4 %5, %9, %10, %12 offen offset:1024 sc1\n\t"
          "s_bitcmp1_b32 %8, 1\n\ts_cselect_b64 exec, -1, 0\n\tbuffer_load_dwordx4 %6, %9, %10, %12 offen offset:2048 sc1\n\t"
          "s_bitcmp1_b32 %8, 0\n\ts_cselect_b64 exec, -1, 0\n\tbuffer_load_dwordx4 %7, %9, %10, %12 offen offset:3072 sc1\n\t"
          "s_mov_b64 exec, -1\n\ts_waitcnt vmcnt(0)"
          : "+v"(gq[0]), "+v"(gq[1]), "+v"(gq[2]), "+v"(gq[3]), "+v"(gq[4]), "+v"(gq[5]), "+v"(gq[6]), "+v"(gq[7]), "+s"(pend)
          : "v"(off), "s"(rg), "s"(0u), "s"(4096u) : "scc", "memory");
        u32 mx[8];
#pragma unroll
        for (int m = 0; m < 8; ++m) mx[m] = umax_(umax_(gq[m].x, gq[m].y), gq[m].z);
        {
          const u32 a0 = umax_(umax_(mx[0], mx[1]), mx[2]), a1 = umax_(umax_(mx[3], mx[4]), mx[5]), a2 = umax_(umax_(mx[6], mx[7]), gq[0].w);
          const u32 a3 = umax_(umax_(gq[1].w, gq[2].w), gq[3].w), a4 = umax_(umax_(gq[4].w, gq[5].w), gq[6].w);
          const u32 a5 = umax_(umax_(umax_(umax_(a0, a1), a2), umax_(umax_(a3, a4), gq[7].w)), 0u);
          if (__builtin_amdgcn_ballot_w64(a5 == ~0u) == 0) break;
        }
        pend = 0;
#pragma unroll
        for (int m = 0; m < 8; ++m)
          asm volatile("v_cmp_eq_u32 vcc, -1, %1\n\ts_cmp_lg_u64 vcc, 0\n\ts_addc_u32 %0, %0, %0" : "+s"(pend) : "v"(umax_(mx[m], gq[m].w)) : "vcc", "scc");
      }
      float ac[8];
#pragma unroll
      for (int i = 0; i < 8; ++i) ac[i] = 0.f;
#pragma unroll
      for (int m = 0; m < 8; ++m)
#pragma unroll
        for (int i = 0; i < 4; ++i) {
          const u32 t = gq[m][i];
          asm("v_dot2c_f32_bf16 %0, %1, %2" : "+v"(ac[2 * i]) : "s"(0x00003F80u), "v"(t));
          asm("v_dot2c_f32_bf16 %0, %1, %2" : "+v"(ac[2 * i + 1]) : "s"(0x3F800000u), "v"(t));
        }
      asm volatile("s_nop 2" : "+v"(ac[0]), "+v"(ac[1]), "+v"(ac[2]), "+v"(ac[3]), "+v"(ac[4]), "+v"(ac[5]), "+v"(ac[6]), "+v"(ac[7]));
      const u32 ka = B_KVT + (w == 5 ? (((l >> 2) & 1) * 17 + 16) * 64 : ((w & 1) * 17 + (l >> 2)) * 64) + (l & 3) * 16;
      *(LDS3 u32x4*)ka = u32x4{cvtpk(ac[0], ac[1]), cvtpk(ac[2], ac[3]), cvtpk(ac[4], ac[5]), cvtpk(ac[6], ac[7])};
    }
    bar_l();
    if (w >= 4) {
      WAITV(0);
      bar();
    } else {
      {
        const u32 ka = kva;
        f32x16 zero;
#pragma unroll
        for (int i = 0; i < 16; ++i) zero[i] = 0.f;
        bf16x8 kf[4];
#pragma unroll
        for (int i = 0; i < 4; ++i) kf[i] = rd128(ka + (i >> 1) * 1088 + (i & 1) * 16);
        sb();
        f32x16 o[4];
        float r[4];
        auto epi = [&](int i) {
          float t[8];
          if (i < 2) {
#pragma unroll
            for (int j = 0; j < 8; ++j) asm("v_mul_f32 %0, %1, %2" : "=v"(t[j]) : "v"(o[i][j]), "v"(r[i]));
          } else {
#pragma unroll
            for (int j = 0; j < 8; ++j) t[j] = o[i][j] * r[i];
          }
          const u32x4 ov = {cvtpk(t[0], t[1]), cvtpk(t[2], t[3]), cvtpk(t[4], t[5]), cvtpk(t[6], t[7])};
          __builtin_amdgcn_raw_buffer_store_b128(ov, rw, ((i & 1) ? ooff2 : ooff) + (i >> 1) * 1024, 0, AUX_SC1);
          sb();
        };
#pragma unroll
        for (int i = 0; i < 8; ++i) {
          const int t = (i & 1) + (i >> 2) * 2, s2 = (i >> 1) & 1;
          o[t] = mfma(kf[(t & 2) + s2], __builtin_bit_cast(bf16x8, pq[t & 1][t >> 1][s2]), s2 ? o[t] : zero);
          sb();
          if (i == 4 || i == 5) { r[i - 4] = __builtin_amdgcn_rcpf(o[i - 4][8] + 1e-6f); asm volatile("" : "+v"(r[i - 4])); sb(); }
          if (i == 6) epi(0);
        }
        epi(1);
        asm volatile("" : "+v"(o[2]), "+v"(o[3]));
        r[2] = __builtin_amdgcn_rcpf(o[2][8] + 1e-6f); r[3] = __builtin_amdgcn_rcpf(o[3][8] + 1e-6f);
        asm volatile("" : "+v"(r[2]), "+v"(r[3]));
        sb();
        epi(2); epi(3);
      }
      WAITV(0);
      if (l == 0) __builtin_amdgcn_raw_buffer_store_b64(u32x2{tl32, thi}, rw, WS_FLAG + (512 + tb * 64 + w * 16 + cb) * FS, 0, AUX_SC1);
      if (LPF & 2) gpre(w * 256, WS_OB + ((tb * 8 + 2 * w + ((l >> 4) & 1)) * 32 + 2 * cb) * 1024 + (l & 15) * 128, srd(ws, WS_BYTES), 0);
      f32x16 y0, y1;
      u32 oa = WS_OB + (tt * 4 + w) * 32768 + l * 16;
      u32 b0a = B_WO + (8 * (G >> 1) + tr_r) * 128 + (((2 * (G & 1) + (tr_c >> 1)) ^ ((tr_r >> 1) << 2)) << 4) + (tr_c & 1) * 8;
      u32 b1a = b0a ^ 64;
      u32 so[8];
#pragma unroll
      for (int i = 0; i < 8; ++i) { so[i] = ((ct + i) & 7) * 4096; asm volatile("" : "+s"(so[i])); }
      asm volatile("" : "+v"(oa), "+v"(b0a), "+v"(b1a));
      if (OPOLL & 2) {
        if (w == 0) { if (PD) __builtin_amdgcn_s_sleep(PD); wait_flags(rw, flagoff + l31 * FS, thi, tl32); }
        bar();
      } else {
        bar();
        if (PD) __builtin_amdgcn_s_sleep(PD);
        wait_flags(rw, flagoff + (wm * 16 + (l & 15)) * FS, thi, tl32);
      }
      {
#pragma unroll
        for (int i = 0; i < 16; ++i) { y0[i] = 0.f; y1[i] = 0.f; }
        u32x4 af[8][4];
        asm volatile("" : "+v"(oa) :: "memory");
#pragma unroll
        for (int i = 0; i < 8; ++i) {
#pragma unroll
          for (int k = 0; k < 4; ++k) af[i][k] = __builtin_amdgcn_raw_buffer_load_b128(rw, oa + k * 1024, so[i], AUX_SC1);
          sb();
        }
        bf16x8 bq[3][2];
        bq[0][0] = rdtr(b0a, b0a + 512); bq[0][1] = rdtr(b1a, b1a + 512);
        bq[1][0] = rdtr(b0a + 2048, b0a + 2560); bq[1][1] = rdtr(b1a + 2048, b1a + 2560);
        sb();
#pragma unroll
        for (int n = 0; n < 32; ++n) {
          const u32 o = (n + 2) * 2048;
          const u32x4 au = af[n >> 2][n & 3];
          const bf16x8 av = __builtin_bit_cast(bf16x8, au);
          y0 = mfma(av, bq[n % 3][0], y0); sb();
          if (n + 2 < 32) bq[(n + 2) % 3][0] = rdtr(b0a + o, b0a + o + 512);
          sb();
          y1 = mfma(av, bq[n % 3][1], y1); sb();
          if (n + 2 < 32) bq[(n + 2) % 3][1] = rdtr(b1a + o, b1a + o + 512);
          sb();
        }
      }
      store_out(out, y0, y1, 128 * tt + 32 * w, 512 + 64 * ct, l);
      if (tt == 15 && ct == 0 && w == 0 && l == 0) __builtin_amdgcn_raw_buffer_store_b32(E + 1, rw, WS_EPB, 0, AUX_SC1);
    }
  }
  asm volatile("fk_code_hi:");
}


extern "C" void kernel_launch(void* const* d_in, const int* in_sizes, int n_in, void* d_out, int out_size, void* d_ws, size_t ws_size, hipStream_t stream) {
  Args a;
  a.xa = (const u16*)d_in[0];
  a.lre = (const u16*)d_in[1];
  a.lim = (const u16*)d_in[2];
  a.bre = (const u16*)d_in[3];
  a.bim = (const u16*)d_in[4];
  a.cre = (const u16*)d_in[5];
  a.cim = (const u16*)d_in[6];
  a.ldt = (const u16*)d_in[7];
  a.dsk = (const u16*)d_in[8];
  a.xb = (const u16*)d_in[9];
  a.wqkv = (const u16*)d_in[10];
  a.wphi = (const u16*)d_in[11];
  a.wo = (const u16*)d_in[12];
  a.out = (float*)d_out;
  a.ws = (unsigned char*)d_ws;
  (void)hipFuncSetAttribute((const void*)fused_k, hipFuncAttributeMaxDynamicSharedMemorySize, 163840);
  fused_k<<<dim3(256), dim3(512), 163840, stream>>>(a);
}
```
